```python
import torch

def _softmax(x, axis=-1):
    return torch.softmax(x, dim=axis)
def _gelu(x):
    return torch.nn.functional.gelu(x, approximate="tanh")
def _silu(x):
    return torch.nn.functional.silu(x)
def _sigmoid(x):
    return torch.sigmoid(x)
def _layernorm(x, axis=-1, eps=1e-5):
    m = x.mean(dim=axis, keepdim=True)
    v = x.var(dim=axis, keepdim=True, unbiased=False)
    return (x - m) / torch.sqrt(v + eps)
def _scatter_add(tgt, idx, src, axis=0):
    return tgt.clone().index_add_(axis, idx, src)
def _segment_sum(src, seg, n):
    out = torch.zeros((n,) + src.shape[1:], dtype=src.dtype)
    return out.index_add_(0, seg, src)
def _pad(x, pw):
    flat = tuple(v for pair in reversed(pw) for v in pair)
    return torch.nn.functional.pad(x, flat)

kernel_name = 'standalone__mha_block_cached__near_ridge'

def setup_inputs():

    _w = (lambda a: (lambda t: t.to(torch.bfloat16) if t.is_floating_point() else t)(torch.as_tensor(a)))
    B, Sq, D = 8, 224, 512
    H, Sk, Dh = 32, 1024, 128
    return {
        "x": _w((lambda *s: torch.randn(*s, dtype=torch.bfloat16) * 0.8)(B, Sq, D)),
        "k_cache": _w((lambda *s: torch.randn(*s, dtype=torch.bfloat16) * 0.8)(B, H, Sk, Dh)),
        "v_cache": _w((lambda *s: torch.randn(*s, dtype=torch.bfloat16) * 0.05)(B, H, Sk, Dh)),
        "wq": _w((lambda *s: torch.randn(*s, dtype=torch.bfloat16) * 0.8)(D, H * Dh) * (D ** -0.5)),
        "wo": _w((lambda *s: torch.randn(*s, dtype=torch.bfloat16) * 0.05)(H * Dh, D) * ((H * Dh) ** -0.5)),
    }

def reference(x, k_cache, v_cache, wq, wo):
    B, Sq, D = x.shape
    H, Dh = 32, 128
    q = (lambda x, *d: x.permute(*d))((x @ wq).reshape(B, Sq, H, Dh), 0, 2, 1, 3)
    scores = torch.einsum("bhqd,bhkd->bhqk", q, k_cache) * (Dh ** -0.5)
    p = _softmax(scores, axis=-1)
    o = torch.einsum("bhqk,bhkd->bhqd", p, v_cache)
    return (lambda x, *d: x.permute(*d))(o, 0, 2, 1, 3).reshape(B, Sq, H * Dh) @ wo
```

```cpp
#include <hip/hip_runtime.h>
#include <hip/hip_bf16.h>
#include <stdint.h>

typedef short    bf16x8 __attribute__((ext_vector_type(8)));
typedef short    s16x4  __attribute__((ext_vector_type(4)));
typedef float    f32x16 __attribute__((ext_vector_type(16)));
typedef float    f32x4  __attribute__((ext_vector_type(4)));
typedef float    f32x2  __attribute__((ext_vector_type(2)));
typedef __bf16   bf16x2 __attribute__((ext_vector_type(2)));
typedef unsigned u32x4  __attribute__((ext_vector_type(4)));
typedef __attribute__((address_space(3))) char* lds_ptr;
typedef __attribute__((address_space(3))) const char* lds_cptr;

#define DI __device__ __forceinline__
#define SBAR() __builtin_amdgcn_sched_barrier(0)
#define PIN(x) asm volatile("" : "+v"(x))
#define MFMA(a, b, c) __builtin_amdgcn_mfma_f32_32x32x16_bf16(a, b, c, 0, 0, 0)
#define BARRIER() asm volatile("s_barrier" ::: "memory")

constexpr int NB = 8, SQ = 224, DM = 512, NH = 32, SK = 1024, DH = 128;
constexpr float QSCALE = 0.088388347648318440f * 1.4426950408889634f;
constexpr float THR = 8.0f;

DI unsigned cvtpk(float lo, float hi) { f32x2 v = {lo, hi}; bf16x2 b = __builtin_convertvector(v, bf16x2); return __builtin_bit_cast(unsigned, b); }
DI bf16x8 ldsrd128(lds_cptr p) { return *(const __attribute__((address_space(3))) bf16x8*)p; }
DI s16x4 ldstr(lds_cptr p) { return __builtin_bit_cast(s16x4, __builtin_amdgcn_ds_read_tr16_b64_v4i16((__attribute__((address_space(3))) s16x4*)p)); }
DI bf16x8 cat(s16x4 a, s16x4 b) { return __builtin_shufflevector(a, b, 0, 1, 2, 3, 4, 5, 6, 7); }
DI void ldswr128(lds_ptr p, u32x4 v) { *(__attribute__((address_space(3))) u32x4*)p = v; }
#define MX3(a, b, c) __builtin_fmaxf(__builtin_fmaxf((a), (b)), (c))

#define DMA4(POL, m0v, v0, v1, v2, v3, srd, soff)                                                     \
  asm volatile("s_mov_b32 m0, %0\n\ts_nop 0\n\t"                                                      \
               "buffer_load_dwordx4 %1, %5, %6 offen " POL " lds\n\t"                                 \
               "buffer_load_dwordx4 %2, %5, %6 offen offset:1024 " POL " lds\n\t"                     \
               "buffer_load_dwordx4 %3, %5, %6 offen offset:2048 " POL " lds\n\t"                     \
               "buffer_load_dwordx4 %4, %5, %6 offen offset:3072 " POL " lds"                         \
               :: "s"(m0v), "v"(v0), "v"(v1), "v"(v2), "v"(v3), "s"(srd), "s"(soff) : "memory")
#define DMA2(POL, m0v, v0, v1, srd, soff)                                                             \
  asm volatile("s_mov_b32 m0, %0\n\ts_nop 0\n\t"                                                      \
               "buffer_load_dwordx4 %1, %3, %4 offen " POL " lds\n\t"                                 \
               "buffer_load_dwordx4 %2, %3, %4 offen offset:1024 " POL " lds"                         \
               :: "s"(m0v), "v"(v0), "v"(v1), "s"(srd), "s"(soff) : "memory")
#define BLOAD(POL, dst, voff, srd, soff, imm)                                                         \
  asm volatile("buffer_load_dwordx4 %0, %1, %2, %3 offen offset:%4 " POL                              \
               : "=v"(dst) : "v"(voff), "s"(srd), "s"(soff), "i"(imm) : "memory")
#define WAITV(n) asm volatile("s_waitcnt vmcnt(%0)" :: "i"(n) : "memory")
#define WAITV4(n, a, b, c, d) asm volatile("s_waitcnt vmcnt(%4)" : "+v"(a), "+v"(b), "+v"(c), "+v"(d) : "i"(n) : "memory")

constexpr int K1_LDS = 163840;
constexpr int XST = 131072;
DI constexpr int WQB(int c) { return c == 0 ? 4 : c == 1 ? 5 : c == 2 ? 6 : c == 3 ? 7 : c == 4 ? 1 : c == 5 ? 0 : c == 6 ? 2 : 3; }

struct AttnState {
  f32x16 o[4];
  bf16x8 q[8];
  f32x16 negm;
  float mhat, lsum, fct;
  bool resc;
  bf16x8 kf[16];
  s16x4 vlo[16], vhi[16];
  u32x4 pw[4];
};

template <bool FIRST, bool LAST>
DI void attn_step(AttnState& st, f32x16& C0, f32x16& C1, f32x16& P0, f32x16& P1,
                  lds_cptr kp0, lds_cptr kp1, lds_cptr vp, lds_cptr kn0, lds_cptr kn1) {
#define KRD(m, b0, b1) st.kf[m] = ldsrd128(((((m) >> 1) & 1) ? (b1) : (b0)) + 8192 * ((m) & 1) + 256 * ((m) >> 2))
#define VRD(m) do { st.vlo[m] = ldstr(vp + 4096 * ((m) >> 2) + 256 * ((m) & 3)); st.vhi[m] = ldstr(vp + 4096 * ((m) >> 2) + 2048 + 256 * ((m) & 3)); } while (0)
#define EX(v) __builtin_amdgcn_exp2f(v)
  float sacc = 0.f;
  SBAR();
#pragma unroll
  for (int m = 0; m < 16; ++m) {
    if constexpr (!LAST) {
      if (m == 0) C0 = MFMA(st.kf[0], st.q[0], st.negm);
      else if (m == 1) C1 = MFMA(st.kf[1], st.q[0], st.negm);
      else if (m & 1) C1 = MFMA(st.kf[m], st.q[m >> 1], C1);
      else C0 = MFMA(st.kf[m], st.q[m >> 1], C0);
      if (m + 4 < 16) { KRD(m + 4, kp0, kp1); }
    }
    if constexpr (!FIRST) {
      if (m < 8) {
        P1[2 * m] = EX(P1[2 * m]); P1[2 * m + 1] = EX(P1[2 * m + 1]);
        st.pw[m >> 2][m & 3] = cvtpk(P0[2 * m], P0[2 * m + 1]);
        sacc += P0[2 * m]; sacc += P0[2 * m + 1];
        PIN(P1); PIN(st.pw[m >> 2]); PIN(sacc);
      } else {
        const int j = m - 8;
        st.pw[2 + (j >> 2)][j & 3] = cvtpk(P1[2 * j], P1[2 * j + 1]);
        sacc += P1[2 * j]; sacc += P1[2 * j + 1];
        PIN(st.pw[2 + (j >> 2)]); PIN(sacc);
      }
      if (m >= 12) { VRD(m - 12); }
    }
    SBAR();
  }
  if constexpr (!FIRST) st.lsum += sacc;
  float ma = 0.f, mb = 0.f;
#pragma unroll
  for (int m = 0; m < 16; ++m) {
    if constexpr (!FIRST) {
      st.o[m & 3] = MFMA(cat(st.vlo[m], st.vhi[m]), __builtin_bit_cast(bf16x8, st.pw[m >> 2]), st.o[m & 3]);
      if (m + 4 < 16) { VRD(m + 4); }
    }
    if constexpr (!LAST) {
      if (m == 1) { ma = MX3(C0[0], C0[1], C0[2]); mb = MX3(C0[3], C0[4], C0[5]); ma = MX3(ma, C0[6], C0[7]); mb = MX3(mb, C0[8], C0[9]); PIN(ma); PIN(mb); }
      if (m == 2) { ma = MX3(ma, C0[10], C0[11]); mb = MX3(mb, C0[12], C0[13]); ma = MX3(ma, C0[14], C0[15]); mb = MX3(mb, C1[0], C1[1]); PIN(ma); PIN(mb); }
      if (m == 3) { ma = MX3(ma, C1[2], C1[3]); mb = MX3(mb, C1[4], C1[5]); ma = MX3(ma, C1[6], C1[7]); mb = MX3(mb, C1[8], C1[9]); PIN(ma); PIN(mb); }
      if (m == 4) { ma = MX3(ma, C1[10], C1[11]); mb = MX3(mb, C1[12], C1[13]); ma = MX3(ma, C1[14], C1[15]); ma = __builtin_fmaxf(ma, mb); PIN(ma); }
      if (m == 5) {
        st.resc = false;
        if (FIRST || __builtin_expect(__any(ma > THR), 0)) {
          auto rr = __builtin_amdgcn_permlane32_swap(__float_as_uint(ma), __float_as_uint(ma), false, false);
          float rm = __builtin_fmaxf(__uint_as_float(rr[0]), __uint_as_float(rr[1]));
          const float dl = FIRST ? rm : __builtin_fmaxf(rm, 0.f);
          st.mhat += dl;
#pragma unroll
          for (int r = 0; r < 16; ++r) { C0[r] -= dl; C1[r] -= dl; }
#pragma unroll
          for (int r = 0; r < 16; ++r) st.negm[r] = -st.mhat;
          PIN(st.negm);
          if constexpr (!FIRST) { st.fct = EX(-dl); st.lsum *= st.fct; st.resc = true; }
        }
      }
      if (m >= 6 && m < 14) { const int j = m - 6; C0[2 * j] = EX(C0[2 * j]); C0[2 * j + 1] = EX(C0[2 * j + 1]); PIN(C0); }
      if (m >= 12) { KRD(m - 12, kn0, kn1); }
    }
    SBAR();
  }
  if constexpr (!FIRST && !LAST) {
    if (__builtin_expect(st.resc, 0)) {
#pragma unroll
      for (int d = 0; d < 4; ++d)
#pragma unroll
        for (int r = 0; r < 16; ++r) st.o[d][r] *= st.fct;
    }
  }
#undef KRD
#undef VRD
#undef EX
}

__global__ __launch_bounds__(512, 2)
void mha_k1(const char* __restrict__ x, const char* __restrict__ kc, const char* __restrict__ vc,
            const char* __restrict__ wq, char* __restrict__ ows) {
  extern __shared__ __attribute__((aligned(16))) char lds[];
  const int tid = threadIdx.x, lane = tid & 63;
  const int wid = __builtin_amdgcn_readfirstlane(tid >> 6);
  const int b = blockIdx.x & 7, h = blockIdx.x >> 3;
  const unsigned lds0 = (unsigned)(uintptr_t)lds;
  const lds_ptr L = (lds_ptr)lds;

  const __amdgpu_buffer_rsrc_t srdW = __builtin_amdgcn_make_buffer_rsrc((void*)(wq + h * 256), (short)0, DM * 8192 - h * 256, 0x00020000);
  const unsigned pl = ((lane >> 2) & 3), dbl = lane >> 4, c4 = lane & 3;
  const unsigned wv0 = pl * 8192 + dbl * 64 + c4 * 16, wv1 = wv0 + 4 * 8192 - 1024;

  if (wid == 7) {
    const __amdgpu_buffer_rsrc_t srdK = __builtin_amdgcn_make_buffer_rsrc((void*)(kc + (size_t)(b * NH + h) * (SK * DH * 2)), (short)0, SK * DH * 2, 0x00020000);
    const __amdgpu_buffer_rsrc_t srdV = __builtin_amdgcn_make_buffer_rsrc((void*)(vc + (size_t)(b * NH + h) * (SK * DH * 2)), (short)0, SK * DH * 2, 0x00020000);
    const unsigned vv = pl * 256 + dbl * 64 + c4 * 16;
    const unsigned kv0 = pl * 256 + dbl * 64 + ((c4 ^ 0) * 16), kv1 = pl * 256 + dbl * 64 + ((c4 ^ 1) * 16),
                   kv2 = pl * 256 + dbl * 64 + ((c4 ^ 2) * 16), kv3 = pl * 256 + dbl * 64 + ((c4 ^ 3) * 16);
#pragma unroll
    for (int c = 0; c < 8; ++c) DMA2("", lds0 + WQB(c) * 16384 + 2048 * 7, wv0, wv1, srdW, (unsigned)((c * 64 + 8 * 7) * 8192));
    u32x4 rk0[16], rk1[16], rv0[16];
#pragma unroll
    for (int i = 0; i < 4; ++i) {
      BLOAD("nt", rk0[4 * i + 0], kv0, srdK, (unsigned)(4096 * i), 0);    BLOAD("nt", rk0[4 * i + 1], kv1, srdK, (unsigned)(4096 * i), 1024);
      BLOAD("nt", rk0[4 * i + 2], kv2, srdK, (unsigned)(4096 * i), 2048); BLOAD("nt", rk0[4 * i + 3], kv3, srdK, (unsigned)(4096 * i), 3072);
    }
#pragma unroll
    for (int i = 0; i < 4; ++i) {
      BLOAD("nt", rk1[4 * i + 0], kv0, srdK, (unsigned)(16384 + 4096 * i), 0);    BLOAD("nt", rk1[4 * i + 1], kv1, srdK, (unsigned)(16384 + 4096 * i), 1024);
      BLOAD("nt", rk1[4 * i + 2], kv2, srdK, (unsigned)(16384 + 4096 * i), 2048); BLOAD("nt", rk1[4 * i + 3], kv3, srdK, (unsigned)(16384 + 4096 * i), 3072);
    }
#pragma unroll
    for (int i = 0; i < 4; ++i) {
      BLOAD("nt", rv0[4 * i + 0], vv, srdV, (unsigned)(4096 * i), 0);    BLOAD("nt", rv0[4 * i + 1], vv, srdV, (unsigned)(4096 * i), 1024);
      BLOAD("nt", rv0[4 * i + 2], vv, srdV, (unsigned)(4096 * i), 2048); BLOAD("nt", rv0[4 * i + 3], vv, srdV, (unsigned)(4096 * i), 3072);
    }
#define DMAK(t) do { _Pragma("unroll") for (int i_ = 0; i_ < 4; ++i_) DMA4("nt", lds0 + (unsigned)(((t) & 3) * 32768 + 4096 * i_), kv0, kv1, kv2, kv3, srdK, (unsigned)((t) * 16384 + 4096 * i_)); } while (0)
#define DMAV(t) do { _Pragma("unroll") for (int i_ = 0; i_ < 4; ++i_) DMA4("nt", lds0 + (unsigned)((((t) + 1) & 3) * 32768 + 16384 + 4096 * i_), vv, vv, vv, vv, srdV, (unsigned)((t) * 16384 + 4096 * i_)); } while (0)
    WAITV(48);
    BARRIER();
    BARRIER();
    DMAK(2);
    BARRIER();
    DMAV(1);
    BARRIER();
    DMAK(3);
    BARRIER();
    DMAV(2);
    BARRIER();
    BARRIER();
    WAITV(63);
#pragma unroll
    for (int i = 0; i < 4; ++i) asm volatile("" : "+v"(rk0[4 * i]), "+v"(rk0[4 * i + 1]), "+v"(rk0[4 * i + 2]), "+v"(rk0[4 * i + 3]));
#pragma unroll
    for (int i = 0; i < 16; ++i) ldswr128(L + 0 * 16384 + 1024 * i + 16 * lane, rk0[i]);
    asm volatile("s_waitcnt lgkmcnt(0)" ::: "memory");
    BARRIER();
#pragma unroll
    for (int i = 0; i < 4; ++i) asm volatile("" : "+v"(rk1[4 * i]), "+v"(rk1[4 * i + 1]), "+v"(rk1[4 * i + 2]), "+v"(rk1[4 * i + 3]));
#pragma unroll
    for (int i = 0; i < 16; ++i) ldswr128(L + 2 * 16384 + 1024 * i + 16 * lane, rk1[i]);
    asm volatile("s_waitcnt lgkmcnt(0)" ::: "memory");
    BARRIER();
#pragma unroll
    for (int i = 0; i < 4; ++i) asm volatile("" : "+v"(rv0[4 * i]), "+v"(rv0[4 * i + 1]), "+v"(rv0[4 * i + 2]), "+v"(rv0[4 * i + 3]));
#pragma unroll
    for (int i = 0; i < 16; ++i) ldswr128(L + 3 * 16384 + 1024 * i + 16 * lane, rv0[i]);
    asm volatile("s_waitcnt lgkmcnt(0)" ::: "memory");
    WAITV(48);
    BARRIER();
    for (int t = 0; t < 12; ++t) {
      DMAK(t + 4); DMAV(t + 3);
      WAITV(48);
      BARRIER();
    }
    DMAV(15);
    WAITV(32);
    BARRIER();
    WAITV(16);
    BARRIER();
    WAITV(0);
    BARRIER();
    return;
#undef DMAK
#undef DMAV
  }

  const int r32 = lane & 31, hi = lane >> 5;
  const __amdgpu_buffer_rsrc_t srdX = __builtin_amdgcn_make_buffer_rsrc((void*)(x + (size_t)(b * SQ + wid * 32) * (DM * 2)), (short)0, 32 * DM * 2, 0x00020000);
  const unsigned xv = (lane >> 3) * 1024 + (lane & 7) * 16;
  u32x4 xr[8][4];
#pragma unroll
  for (int c = 0; c < 8; ++c) {
    DMA2("", lds0 + WQB(c) * 16384 + 2048 * wid, wv0, wv1, srdW, (unsigned)((c * 64 + 8 * wid) * 8192));
#pragma unroll
    for (int g = 0; g < 4; ++g) BLOAD("", xr[c][g], xv, srdX, (unsigned)(g * 8192), 128 * c);
  }
  const lds_ptr xw = L + XST + wid * 4096 + (lane >> 3) * 128 + (((lane & 7) ^ ((lane >> 4) & 3)) * 16);
  const unsigned xrb = (unsigned)(XST + wid * 4096 + r32 * 128 + ((hi ^ ((r32 >> 1) & 7)) * 16));
  const int tq = (lane >> 2) & 3, tp = lane & 3, tblk = (lane >> 4) & 1;
  const int tpp = ((tp & 1) << 1) | (tp >> 1);
  const lds_cptr wrb = (lds_cptr)L + 2048 * hi + 64 * tq + 32 * tblk + 8 * tpp;

  AttnState st;
  f32x16 acc[4];
#pragma unroll
  for (int i = 0; i < 4; ++i) acc[i] = f32x16{};
  bf16x8 xf[2][4];
  s16x4 wlo[16], whi[16];
#define XSTAGE(c) do { \
    _Pragma("unroll") for (int g_ = 0; g_ < 4; ++g_) ldswr128((lds_ptr)((unsigned)(uintptr_t)(xw + 1024 * g_) ^ ((g_ & 1) ? 64u : 0u)), xr[c][g_]); \
    _Pragma("unroll") for (int k_ = 0; k_ < 4; ++k_) xf[(c) & 1][k_] = ldsrd128((lds_cptr)L + (xrb ^ (unsigned)(k_ << 5))); } while (0)
#define WRD(c, m) do { wlo[m] = ldstr(wrb + WQB(c) * 16384 + 4096 * ((m) >> 2) + 256 * ((m) & 3)); whi[m] = ldstr(wrb + WQB(c) * 16384 + 4096 * ((m) >> 2) + 1024 + 256 * ((m) & 3)); } while (0)
  WAITV4(42, xr[0][0], xr[0][1], xr[0][2], xr[0][3]);
  XSTAGE(0);
  WAITV4(36, xr[1][0], xr[1][1], xr[1][2], xr[1][3]);
  BARRIER();
  WRD(0, 0); WRD(0, 1); WRD(0, 2); WRD(0, 3);
#pragma unroll
  for (int c = 0; c < 8; ++c) {
    SBAR();
#pragma unroll
    for (int m = 0; m < 16; ++m) {
      acc[m & 3] = MFMA(cat(wlo[m], whi[m]), xf[c & 1][m >> 2], acc[m & 3]);
      if (m + 4 < 16) { WRD(c, m + 4); }
      else if (c < 7) { WRD(c + 1, m - 12); }
      if (m == 5 && c < 7) { XSTAGE(c + 1); }
      SBAR();
    }
    if (c < 6) { WAITV4(6 * (5 - c), xr[c + 2][0], xr[c + 2][1], xr[c + 2][2], xr[c + 2][3]); }
    BARRIER();
  }
#undef XSTAGE
#undef WRD
#pragma unroll
  for (int nb = 0; nb < 4; ++nb)
#pragma unroll
    for (int s = 0; s < 2; ++s) {
      u32x4 w;
#pragma unroll
      for (int e = 0; e < 4; ++e) w[e] = cvtpk(acc[nb][8 * s + 2 * e] * QSCALE, acc[nb][8 * s + 2 * e + 1] * QSCALE);
      st.q[2 * nb + s] = __builtin_bit_cast(bf16x8, w);
    }
#pragma unroll
  for (int i = 0; i < 4; ++i) st.o[i] = f32x16{};
  st.negm = f32x16{}; PIN(st.negm);
  st.mhat = 0.f; st.lsum = 0.f; st.fct = 1.f; st.resc = false;

  const lds_cptr kb0 = (lds_cptr)L + 1024 * (r32 >> 2) + 64 * (r32 & 3) + 16 * ((0 + hi) ^ ((r32 >> 2) & 3));
  const lds_cptr kb1 = (lds_cptr)L + 1024 * (r32 >> 2) + 64 * (r32 & 3) + 16 * ((2 + hi) ^ ((r32 >> 2) & 3));
  const lds_cptr vb = (lds_cptr)L + 16384 + 1024 * hi + 64 * tq + 32 * tblk + 8 * tp;

  f32x16 A0, A1, B0, B1;
#pragma unroll
  for (int m = 0; m < 4; ++m) st.kf[m] = ldsrd128((((m >> 1) & 1) ? kb1 : kb0) + 8192 * (m & 1));
  attn_step<true, false>(st, A0, A1, B0, B1, kb0, kb1, vb, kb0 + 32768, kb1 + 32768);
  BARRIER();
  for (int t = 1; t < 15; t += 2) {
    const int s0 = (t & 3) * 32768, s1 = ((t + 1) & 3) * 32768, s2 = ((t + 2) & 3) * 32768;
    attn_step<false, false>(st, B0, B1, A0, A1, kb0 + s0, kb1 + s0, vb + s0, kb0 + s1, kb1 + s1);
    BARRIER();
    attn_step<false, false>(st, A0, A1, B0, B1, kb0 + s1, kb1 + s1, vb + s1, kb0 + s2, kb1 + s2);
    BARRIER();
  }
  attn_step<false, false>(st, B0, B1, A0, A1, kb0 + 3 * 32768, kb1 + 3 * 32768, vb + 3 * 32768, kb0, kb1);
  BARRIER();
  attn_step<false, true>(st, A0, A1, B0, B1, kb0, kb1, vb, kb0, kb1);

  {
    auto rr = __builtin_amdgcn_permlane32_swap(__float_as_uint(st.lsum), __float_as_uint(st.lsum), false, false);
    const float l = __uint_as_float(rr[0]) + __uint_as_float(rr[1]);
    const float inv = 1.0f / l;
    char* op = ows + (size_t)(((b * NH + h) * 7 + wid) * 8) * 1024 + lane * 16;
#pragma unroll
    for (int db = 0; db < 4; ++db)
#pragma unroll
      for (int s = 0; s < 2; ++s) {
        u32x4 w;
#pragma unroll
        for (int e = 0; e < 4; ++e) w[e] = cvtpk(st.o[db][8 * s + 2 * e] * inv, st.o[db][8 * s + 2 * e + 1] * inv);
        *(u32x4*)(op + (2 * db + s) * 1024) = w;
      }
  }
}

constexpr int K2_LDS = 131072;
__global__ __launch_bounds__(512, 2)
void mha_k2(const char* __restrict__ ows, const char* __restrict__ wo, char* __restrict__ out) {
  extern __shared__ __attribute__((aligned(16))) char lds[];
  const int tid = threadIdx.x, lane = tid & 63;
  const int wid = __builtin_amdgcn_readfirstlane(tid >> 6);
  const int lin = (blockIdx.x & 7) * 28 + (blockIdx.x >> 3);
  const int qt = lin >> 3, nt = lin & 7;
  const unsigned lds0 = (unsigned)(uintptr_t)lds;
  const lds_ptr L = (lds_ptr)lds;
  const int gq0 = 2 * qt, gq1 = 2 * qt + 1;
  const int b0 = gq0 / 7, qb0 = gq0 - 7 * b0, b1 = gq1 / 7, qb1 = gq1 - 7 * b1;
  const __amdgpu_buffer_rsrc_t srdO = __builtin_amdgcn_make_buffer_rsrc((void*)ows, (short)0, NB * NH * 7 * 8 * 1024, 0x00020000);
  const __amdgpu_buffer_rsrc_t srdW = __builtin_amdgcn_make_buffer_rsrc((void*)(wo + nt * 128), (short)0, NH * DH * DM * 2 - nt * 128, 0x00020000);
  const unsigned ob0 = (unsigned)(((b0 * NH + 4 * wid) * 7 + qb0) * 8192), ob1 = (unsigned)(((b1 * NH + 4 * wid) * 7 + qb1) * 8192);
  const unsigned ov = lane * 16;
  const unsigned wl = (4 * (lane >> 5) + ((lane >> 2) & 3)) * 1024 + ((lane >> 4) & 1) * 64 + (lane & 3) * 16;
  const unsigned w0 = wl, w1 = wl + 7168, w2 = wl + 2 * 7168, w3 = wl + 3 * 7168;
  const unsigned wsb = (unsigned)(512 * wid * 1024);
  const unsigned lb = lds0 + wid * 16384;
  const int hi = lane >> 5, tq = (lane >> 2) & 3, tp = lane & 3, tblk = (lane >> 4) & 1;
  const lds_cptr rb = (lds_cptr)L + wid * 16384 + 512 * hi + 64 * tq + 32 * tblk + 8 * tp;
  u32x4 of[4][4];
  f32x16 acc[2][2];
#pragma unroll
  for (int i = 0; i < 2; ++i) { acc[i][0] = f32x16{}; acc[i][1] = f32x16{}; }
#define ISSUE(g) do { \
    DMA4("", lb + 4096 * ((g) & 3), w0, w1, w2, w3, srdW, wsb + (unsigned)((g) * 32 * 1024)); \
    BLOAD("", of[(g) & 3][0], ov, srdO, ob0 + (unsigned)(((g) >> 2) * 7 * 8192), 2048 * ((g) & 1) + 0 + 0); \
    BLOAD("", of[(g) & 3][1], ov, srdO, ob1 + (unsigned)(((g) >> 2) * 7 * 8192), 2048 * ((g) & 1) + 0 + 0); \
    BLOAD("", of[(g) & 3][2], ov, srdO, ob0 + (unsigned)(((g) >> 2) * 7 * 8192), 2048 * ((g) & 1) + 1024); \
    BLOAD("", of[(g) & 3][3], ov, srdO, ob1 + (unsigned)(((g) >> 2) * 7 * 8192), 2048 * ((g) & 1) + 1024); } while (0)
#undef ISSUE
#define ISSUE(g) do { \
    const unsigned so_ = (unsigned)(((g) >> 2) * 7 * 8192 + ((g) & 2) * 2048); \
    DMA4("", lb + 4096 * ((g) & 3), w0, w1, w2, w3, srdW, wsb + (unsigned)((g) * 32 * 1024)); \
    BLOAD("", of[(g) & 3][0], ov, srdO, ob0 + so_, 2048 * ((g) & 1)); \
    BLOAD("", of[(g) & 3][1], ov, srdO, ob1 + so_, 2048 * ((g) & 1)); \
    BLOAD("", of[(g) & 3][2], ov, srdO, ob0 + so_, 2048 * ((g) & 1) + 1024); \
    BLOAD("", of[(g) & 3][3], ov, srdO, ob1 + so_, 2048 * ((g) & 1) + 1024); } while (0)
  ISSUE(0); ISSUE(1); ISSUE(2); ISSUE(3);
#pragma unroll
  for (int g = 0; g < 16; ++g) {
    const int sl = g & 3;
    const int rem = (g + 3 < 16 ? 3 : 15 - g);
    WAITV4(8 * rem, of[sl][0], of[sl][1], of[sl][2], of[sl][3]);
    bf16x8 wf[2][2];
#pragma unroll
    for (int ks = 0; ks < 2; ++ks)
#pragma unroll
      for (int nb = 0; nb < 2; ++nb)
        wf[ks][nb] = cat(ldstr(rb + 4096 * sl + 2048 * ks + 256 * nb), ldstr(rb + 4096 * sl + 2048 * ks + 1024 + 256 * nb));
#pragma unroll
    for (int ks = 0; ks < 2; ++ks)
#pragma unroll
      for (int nb = 0; nb < 2; ++nb)
#pragma unroll
        for (int qq = 0; qq < 2; ++qq)
          acc[nb][qq] = MFMA(wf[ks][nb], __builtin_bit_cast(bf16x8, of[sl][2 * ks + qq]), acc[nb][qq]);
    SBAR();
    if (g + 4 < 16) { ISSUE(g + 4); }
    SBAR();
  }
#undef ISSUE
  {
    const int r32 = lane & 31;
#pragma unroll
    for (int qq = 0; qq < 2; ++qq)
#pragma unroll
      for (int nb = 0; nb < 2; ++nb)
#pragma unroll
        for (int rg = 0; rg < 4; ++rg) {
          const int q = 32 * qq + r32, slot = 8 * nb + 2 * rg + hi;
          f32x4 v = {acc[nb][qq][4 * rg], acc[nb][qq][4 * rg + 1], acc[nb][qq][4 * rg + 2], acc[nb][qq][4 * rg + 3]};
          *(__attribute__((address_space(3))) f32x4*)(L + wid * 16384 + q * 256 + ((slot ^ (q & 15)) * 16)) = v;
        }
    __syncthreads();
    const int q = tid >> 3, pc = tid & 7;
    f32x4 s0 = {0, 0, 0, 0}, s1 = {0, 0, 0, 0};
#pragma unroll
    for (int w = 0; w < 8; ++w) {
      s0 += *(const __attribute__((address_space(3))) f32x4*)(L + w * 16384 + q * 256 + (((2 * pc) ^ (q & 15)) * 16));
      s1 += *(const __attribute__((address_space(3))) f32x4*)(L + w * 16384 + q * 256 + (((2 * pc + 1) ^ (q & 15)) * 16));
    }
    u32x4 w = {cvtpk(s0[0], s0[1]), cvtpk(s0[2], s0[3]), cvtpk(s1[0], s1[1]), cvtpk(s1[2], s1[3])};
    *(u32x4*)(out + (size_t)(qt * 64 + q) * (DM * 2) + nt * 128 + pc * 16) = w;
  }
}

extern "C" void kernel_launch(void* const* d_in, const int* in_sizes, int n_in,
                              void* d_out, int out_size, void* d_ws, size_t ws_size,
                              hipStream_t stream) {
  (void)in_sizes; (void)n_in; (void)out_size; (void)ws_size;
  (void)hipFuncSetAttribute((const void*)mha_k1, hipFuncAttributeMaxDynamicSharedMemorySize, K1_LDS);
  (void)hipFuncSetAttribute((const void*)mha_k2, hipFuncAttributeMaxDynamicSharedMemorySize, K2_LDS);
  mha_k1<<<256, 512, K1_LDS, stream>>>((const char*)d_in[0], (const char*)d_in[1], (const char*)d_in[2], (const char*)d_in[3], (char*)d_ws);
  mha_k2<<<224, 512, K2_LDS, stream>>>((const char*)d_ws, (const char*)d_in[4], (char*)d_out);
}
```

```cpp
#include <hip/hip_runtime.h>
#include <hip/hip_bf16.h>
#include <stdint.h>

typedef short    bf16x8 __attribute__((ext_vector_type(8)));
typedef short    s16x4  __attribute__((ext_vector_type(4)));
typedef float    f32x16 __attribute__((ext_vector_type(16)));
typedef float    f32x4  __attribute__((ext_vector_type(4)));
typedef float    f32x2  __attribute__((ext_vector_type(2)));
typedef __bf16   bf16x2 __attribute__((ext_vector_type(2)));
typedef unsigned u32x4  __attribute__((ext_vector_type(4)));
typedef __attribute__((address_space(3))) char* lds_ptr;
typedef __attribute__((address_space(3))) const char* lds_cptr;

#define DI __device__ __forceinline__
#define SBAR() __builtin_amdgcn_sched_barrier(0)
#define PIN(x) asm volatile("" : "+v"(x))
#define MFMA(a, b, c) __builtin_amdgcn_mfma_f32_32x32x16_bf16(a, b, c, 0, 0, 0)
#define BARRIER() asm volatile("s_barrier" ::: "memory")

#define STAMP(i)
#define RSTAMP(i)
#define STFLUSH(dbg)
#define POLT "nt"
#define POLW ""
#define POLWO ""
#define POLT2 ""
#define MAPK1 1
constexpr int NB = 8, SQ = 224, DM = 512, NH = 32, SK = 1024, DH = 128;
constexpr float QSCALE = 0.088388347648318440f * 1.4426950408889634f;
constexpr float THR = 8.0f;

DI unsigned cvtpk(float lo, float hi) { f32x2 v = {lo, hi}; bf16x2 b = __builtin_convertvector(v, bf16x2); return __builtin_bit_cast(unsigned, b); }
DI bf16x8 ldsrd128(lds_cptr p) { return *(const __attribute__((address_space(3))) bf16x8*)p; }
DI s16x4 ldstr(lds_cptr p) { return __builtin_bit_cast(s16x4, __builtin_amdgcn_ds_read_tr16_b64_v4i16((__attribute__((address_space(3))) s16x4*)p)); }
DI bf16x8 cat(s16x4 a, s16x4 b) { return __builtin_shufflevector(a, b, 0, 1, 2, 3, 4, 5, 6, 7); }
DI void ldswr128(lds_ptr p, u32x4 v) { *(__attribute__((address_space(3))) u32x4*)p = v; }
#define MX3(a, b, c) __builtin_fmaxf(__builtin_fmaxf((a), (b)), (c))

#define DMA4(POL, m0v, v0, v1, v2, v3, srd, soff)                                                     \
  asm volatile("s_mov_b32 m0, %0\n\ts_nop 0\n\t"                                                      \
               "buffer_load_dwordx4 %1, %5, %6 offen " POL " lds\n\t"                                 \
               "buffer_load_dwordx4 %2, %5, %6 offen offset:1024 " POL " lds\n\t"                     \
               "buffer_load_dwordx4 %3, %5, %6 offen offset:2048 " POL " lds\n\t"                     \
               "buffer_load_dwordx4 %4, %5, %6 offen offset:3072 " POL " lds"                         \
               :: "s"(m0v), "v"(v0), "v"(v1), "v"(v2), "v"(v3), "s"(srd), "s"(soff) : "memory")
#define DMA2(POL, m0v, v0, v1, srd, soff)                                                             \
  asm volatile("s_mov_b32 m0, %0\n\ts_nop 0\n\t"                                                      \
               "buffer_load_dwordx4 %1, %3, %4 offen " POL " lds\n\t"                                 \
               "buffer_load_dwordx4 %2, %3, %4 offen offset:1024 " POL " lds"                         \
               :: "s"(m0v), "v"(v0), "v"(v1), "s"(srd), "s"(soff) : "memory")
#define BLOAD(POL, dst, voff, srd, soff, imm)                                                         \
  asm volatile("buffer_load_dwordx4 %0, %1, %2, %3 offen offset:%4 " POL                              \
               : "=v"(dst) : "v"(voff), "s"(srd), "s"(soff), "i"(imm) : "memory")
#define WAITV(n) asm volatile("s_waitcnt vmcnt(%0)" :: "i"(n) : "memory")
#define WAITV4(n, a, b, c, d) asm volatile("s_waitcnt vmcnt(%4)" : "+v"(a), "+v"(b), "+v"(c), "+v"(d) : "i"(n) : "memory")

constexpr int K1_LDS = 163840;
constexpr int XST = 131072;
DI constexpr int WQB(int c) { return c == 0 ? 0 : c == 7 ? 1 : c + 1; }

struct AttnState {
  f32x16 o[4];
  bf16x8 q[8];
  f32x16 negm;
  float mhat, lsum;
  bf16x8 kf[16];
  s16x4 vlo[16], vhi[16];
  u32x4 pw[4];
};

constexpr int PD = 6;
template <bool FIRST, bool LAST>
DI void attn_step(AttnState& st, f32x16& C0, f32x16& C1, f32x16& P0, f32x16& P1,
                  lds_cptr kp0, lds_cptr kp1, lds_cptr vp, lds_cptr kn0, lds_cptr kn1) {
#define KRD(m, b0, b1) st.kf[m] = ldsrd128((((m) & 1) ? (b1) : (b0)) + 8192 * ((m) >> 3) + 256 * (((m) & 7) >> 1))
#define VRD(m) do { st.vlo[m] = ldstr(vp + 4096 * ((m) >> 2) + 256 * ((m) & 3)); st.vhi[m] = ldstr(vp + 4096 * ((m) >> 2) + 2048 + 256 * ((m) & 3)); } while (0)
#define EX(v) __builtin_amdgcn_exp2f(v)
#define PE(e) (((e) < 16) ? P0[(e) & 15] : P1[(e) & 15])
  float ma = 0.f, mb = 0.f, fct = 1.f;
  bool resc = false;
  SBAR();
#pragma unroll
  for (int g = 0; g < 32; ++g) {
    if (g < 16) {
      if constexpr (!LAST) {
        if (g == 0) C0 = MFMA(st.kf[0], st.q[0], st.negm);
        else if (g < 8) C0 = MFMA(st.kf[g], st.q[g], C0);
        else if (g == 8) C1 = MFMA(st.kf[8], st.q[0], st.negm);
        else C1 = MFMA(st.kf[g], st.q[g - 8], C1);
        if (g < 8) PIN(C0); else PIN(C1);
      }
    } else {
      if constexpr (!FIRST) {
        const int m = g - 16;
        st.o[m & 3] = MFMA(cat(st.vlo[m], st.vhi[m]), __builtin_bit_cast(bf16x8, st.pw[m >> 2]), st.o[m & 3]);
      }
    }
    {
      const int f = g + PD;
      if (f < 16) { if constexpr (!LAST) { KRD(f, kp0, kp1); } }
      else if (f < 32) { if constexpr (!FIRST) { VRD(f - 16); } }
      else { if constexpr (!LAST) { KRD(f - 32, kn0, kn1); } }
    }
    if (g <= 22) {
      if constexpr (!FIRST) {
        st.lsum += PE(9 + g); PIN(st.lsum);
        if ((g & 1) == 0) { const int e = 8 + g; st.pw[e >> 3][(e >> 1) & 3] = cvtpk(PE(e), PE(e + 1)); PIN(st.pw[e >> 3]); }
      }
    } else {
      if constexpr (!LAST) {
        st.lsum += C0[g - 23]; PIN(st.lsum);
        if ((g & 1) == 0) { const int e = g - 24; st.pw[0][e >> 1] = cvtpk(C0[e], C0[e + 1]); PIN(st.pw[0]); }
      }
    }
    if constexpr (!LAST) {
      if (g == 9)  { ma = MX3(C0[0], C0[1], C0[2]); mb = MX3(C0[3], C0[4], C0[5]); PIN(ma); PIN(mb); }
      if (g == 11) { ma = MX3(ma, C0[6], C0[7]); mb = MX3(mb, C0[8], C0[9]); PIN(ma); PIN(mb); }
      if (g == 13) { ma = MX3(ma, C0[10], C0[11]); mb = MX3(mb, C0[12], C0[13]); PIN(ma); PIN(mb); }
      if (g == 15) { ma = MX3(ma, C0[14], C0[15]); PIN(ma); }
      if (g == 17) { ma = MX3(ma, C1[0], C1[1]); mb = MX3(mb, C1[2], C1[3]); PIN(ma); PIN(mb); }
      if (g == 18) { ma = MX3(ma, C1[4], C1[5]); PIN(ma); }
      if (g == 19) { ma = MX3(ma, C1[6], C1[7]); mb = MX3(mb, C1[8], C1[9]); PIN(ma); PIN(mb); }
      if (g == 20) { mb = MX3(mb, C1[10], C1[11]); PIN(mb); }
      if (g == 21) { ma = MX3(ma, C1[12], C1[13]); mb = MX3(mb, C1[14], C1[15]); PIN(ma); PIN(mb); }
      if (g == 22) {
        if (FIRST || __builtin_expect(__any((ma > THR) | (mb > THR)), 0)) {
          ma = __builtin_fmaxf(ma, mb);
          auto rr = __builtin_amdgcn_permlane32_swap(__float_as_uint(ma), __float_as_uint(ma), false, false);
          float rm = __builtin_fmaxf(__uint_as_float(rr[0]), __uint_as_float(rr[1]));
          const float dl = FIRST ? rm : __builtin_fmaxf(rm, 0.f);
          st.mhat += dl;
#pragma unroll
          for (int r = 0; r < 16; ++r) { C0[r] -= dl; C1[r] -= dl; }
#pragma unroll
          for (int r = 0; r < 16; ++r) st.negm[r] = -st.mhat;
          PIN(st.negm);
          if constexpr (!FIRST) { fct = EX(-dl); st.lsum *= fct; resc = true; }
        }
      }
    }
    if (g <= 21) {
      if constexpr (!FIRST) { const int e = 10 + g; if (e < 16) { P0[e] = EX(P0[e]); PIN(P0); } else { P1[e - 16] = EX(P1[e - 16]); PIN(P1); } }
    } else {
      if constexpr (!LAST) { C0[g - 22] = EX(C0[g - 22]); PIN(C0); }
    }
    SBAR();
  }
  if constexpr (!FIRST && !LAST) {
    if (__builtin_expect(resc, 0)) {
#pragma unroll
      for (int d = 0; d < 4; ++d)
#pragma unroll
        for (int r = 0; r < 16; ++r) st.o[d][r] *= fct;
    }
  }
#undef KRD
#undef VRD
#undef EX
#undef PE
}

__global__ __launch_bounds__(512, 2)
void mha_k1(const char* __restrict__ x, const char* __restrict__ kc, const char* __restrict__ vc,
            const char* __restrict__ wq, const char* __restrict__ wo, char* __restrict__ ows) {
  extern __shared__ __attribute__((aligned(16))) char lds[];
  const int tid = threadIdx.x, lane = tid & 63;
  const int wid = __builtin_amdgcn_readfirstlane(tid >> 6);
  const int b = 4 * (blockIdx.x & 1) + ((blockIdx.x >> 3) & 3), h = 8 * ((blockIdx.x >> 1) & 3) + (blockIdx.x >> 5);
  const unsigned lds0 = (unsigned)(uintptr_t)lds;
  const lds_ptr L = (lds_ptr)lds;
  constexpr int STOFF = 159744;
  RSTAMP(62); STAMP(0);

  const char *pfx, *pfw;
  {
    const int j = blockIdx.x >> 3, half = lane >> 5, l5 = lane & 31;
    const int xrow = (b & 4) * SQ + 28 * j + (l5 < 28 ? l5 : 27);
    const int idx = 32 * j + l5;
    pfw = wq + (size_t)(64 * half + (idx >> 4)) * 8192 + (h & 24) * 256 + (idx & 15) * 128;
    pfx = x + (size_t)xrow * (DM * 2) + half * 128;
  }
  unsigned pfd0;
  const char* pfe = wid >= 4 ? x : (wid & 1) ? pfw + (size_t)(wid >> 1) * (128 * 8192) : pfx + (wid >> 1) * 256;
#define TOUCH(d, p) asm volatile("global_load_dword %0, %1, off " POLT : "=v"(d) : "v"(p) : "memory")
#define KEEP(d) asm volatile("" :: "v"(d) : "memory")
  const __amdgpu_buffer_rsrc_t srdW = __builtin_amdgcn_make_buffer_rsrc((void*)(wq + h * 256), (short)0, DM * 8192 - h * 256, 0x00020000);
  const unsigned pl = ((lane >> 2) & 3), dbl = lane >> 4, c4 = lane & 3;
  const unsigned wv0 = pl * 8192 + dbl * 64 + c4 * 16, wv1 = wv0 + 31744;
#define DMAW2(c) DMA2(POLW, lds0 + WQB(c) * 16384 + 2048 * wid, wv0, wv1, srdW, (unsigned)(((c) * 64 + 8 * wid) * 8192))
  TOUCH(pfd0, pfe);
  if (wid == 7) {
    const __amdgpu_buffer_rsrc_t srdK = __builtin_amdgcn_make_buffer_rsrc((void*)(kc + (size_t)(b * NH + h) * (SK * DH * 2)), (short)0, SK * DH * 2, 0x00020000);
    const __amdgpu_buffer_rsrc_t srdV = __builtin_amdgcn_make_buffer_rsrc((void*)(vc + (size_t)(b * NH + h) * (SK * DH * 2)), (short)0, SK * DH * 2, 0x00020000);
    const unsigned wv2 = wv0 + 2 * 31744, wv3 = wv0 + 3 * 31744;
    const unsigned vv = pl * 256 + dbl * 64 + c4 * 16;
    const unsigned kv0 = pl * 256 + dbl * 64 + ((c4 ^ 0) * 16), kv1 = pl * 256 + dbl * 64 + ((c4 ^ 1) * 16),
                   kv2 = pl * 256 + dbl * 64 + ((c4 ^ 2) * 16), kv3 = pl * 256 + dbl * 64 + ((c4 ^ 3) * 16);
#define DMAW(c) do { _Pragma("unroll") for (int i_ = 0; i_ < 4; ++i_) DMA4(POLW, lds0 + (unsigned)(WQB(c) * 16384 + 4096 * i_), wv0, wv1, wv2, wv3, srdW, (unsigned)(((c) * 64 + 16 * i_) * 8192)); } while (0)
#define DMAK(t) do { _Pragma("unroll") for (int i_ = 0; i_ < 4; ++i_) DMA4("nt", lds0 + (unsigned)(((t) & 3) * 32768 + 4096 * i_), kv0, kv1, kv2, kv3, srdK, (unsigned)((t) * 16384 + 4096 * i_)); } while (0)
#define DMAV(t) do { _Pragma("unroll") for (int i_ = 0; i_ < 4; ++i_) DMA4("nt", lds0 + (unsigned)((((t) + 1) & 3) * 32768 + 16384 + 4096 * i_), vv, vv, vv, vv, srdV, (unsigned)((t) * 16384 + 4096 * i_)); } while (0)
    unsigned t4, t5, t6, t7;
    STAMP(45); DMAW2(0); STAMP(46);
    BARRIER();
    STAMP(47); DMAW2(1); STAMP(48);
    WAITV(2); STAMP(49); STAMP(1); STAMP(2); BARRIER(); STAMP(3);
    DMAW(2); DMAW(3);
    WAITV(32); BARRIER();
    { const char* p4 = pfx + 512; const char* p5 = pfw + (size_t)2 * 128 * 8192; const char* p6 = pfx + 768; const char* p7 = pfw + (size_t)3 * 128 * 8192;
      TOUCH(t4, p4); TOUCH(t5, p5); TOUCH(t6, p6); TOUCH(t7, p7); } STAMP(4);
    WAITV(20); STAMP(5); BARRIER(); STAMP(6);
    STAMP(7);
    WAITV(4); STAMP(8); BARRIER(); STAMP(9);
    DMAW(4); DMAW(5); STAMP(10);
    WAITV(16); STAMP(11); BARRIER(); STAMP(12);
    KEEP(pfd0); KEEP(t4); KEEP(t5); KEEP(t6); KEEP(t7);
    DMAW(6); DMAW(7); STAMP(13);
    WAITV(32); STAMP(14); BARRIER(); STAMP(15);
    DMAK(0); DMAK(1); STAMP(16);
    WAITV(48); STAMP(17); BARRIER(); STAMP(18);
    STAMP(19);
    WAITV(32); STAMP(20); BARRIER(); STAMP(21);
    DMAV(0); DMAK(2); STAMP(22);
    STAMP(23); BARRIER(); STAMP(24);
    STAMP(25);
    WAITV(32); STAMP(26); BARRIER(); STAMP(27);
    DMAV(1); DMAK(3); STAMP(28);
    WAITV(32); STAMP(29); BARRIER(); STAMP(30);
    DMAV(2); DMAK(4); DMAV(3); STAMP(31);
    WAITV(48); STAMP(32); BARRIER(); STAMP(33);
    DMAK(5); DMAV(4); STAMP(34);
    WAITV(48); STAMP(35); BARRIER(); STAMP(36);
    for (int t = 2; t < 12; ++t) {
      DMAK(t + 4); DMAV(t + 3);
      WAITV(48);
      BARRIER();
    }
    DMAV(15);
    WAITV(32);
    BARRIER();
    WAITV(16);
    BARRIER();
    WAITV(0);
    {
      const char* wp = wo + (size_t)(blockIdx.x >> 3) * 65536 + lane * 128;
      unsigned d[8];
#pragma unroll
      for (int i = 0; i < 8; ++i) { const char* p_ = wp + i * 8192; asm volatile("global_load_dword %0, %1, off " POLT2 : "=v"(d[i]) : "v"(p_) : "memory"); }
      BARRIER();
      WAITV(0);
#pragma unroll
      for (int i = 0; i < 8; ++i) KEEP(d[i]);
    }
    STAMP(44); RSTAMP(63);
    STFLUSH(ows + (64 << 20));
    return;
#undef DMAW
#undef DMAK
#undef DMAV
  }

  const int r32 = lane & 31, hi = lane >> 5;
  const __amdgpu_buffer_rsrc_t srdX = __builtin_amdgcn_make_buffer_rsrc((void*)(x + (size_t)(b * SQ + wid * 32) * (DM * 2)), (short)0, 32 * DM * 2, 0x00020000);
  const unsigned xv = ((lane >> 2) & 7) * 4096 + (lane >> 5) * 64 + (lane & 3) * 16;
  u32x4 xr[8][4];
#define QISSUE(c) do { _Pragma("unroll") for (int g_ = 0; g_ < 4; ++g_) BLOAD(POLW, xr[c][g_], xv, srdX, 0u, 1024 * g_ + 128 * (c)); } while (0)
  STAMP(45);
  DMAW2(0); QISSUE(0);
  STAMP(46);
  BARRIER();
  STAMP(47);
  DMAW2(1); QISSUE(1);
  STAMP(48);
  const int tq = (lane >> 2) & 3, tp = lane & 3, tblk = (lane >> 4) & 1;
  const int tpp = ((tp & 1) << 1) | (tp >> 1);
  const lds_cptr wrb = (lds_cptr)L + 8192 * hi + 64 * tq + 32 * tblk + 8 * tpp;
  const bool odd1 = lane & 1, odd2 = lane & 2;

  AttnState st;
  f32x16 acc[4];
#pragma unroll
  for (int i = 0; i < 4; ++i) acc[i] = f32x16{};
  s16x4 wlo[16], whi[16];
#define DPP1(v) (unsigned)__builtin_amdgcn_mov_dpp((int)(v), 0xB1, 0xF, 0xF, true)
#define DPP2(v) (unsigned)__builtin_amdgcn_mov_dpp((int)(v), 0x4E, 0xF, 0xF, true)
#define XTR(c, e) do { \
    const unsigned a0 = xr[c][0][e], a1 = xr[c][1][e], a2 = xr[c][2][e], a3 = xr[c][3][e]; \
    const unsigned t0 = DPP1(a0), t1 = DPP1(a1), t2 = DPP1(a2), t3 = DPP1(a3); \
    const unsigned b0 = odd1 ? t1 : a0, b1 = odd1 ? a1 : t0, b2 = odd1 ? t3 : a2, b3 = odd1 ? a3 : t2; \
    const unsigned u0 = DPP2(b0), u1 = DPP2(b1), u2 = DPP2(b2), u3 = DPP2(b3); \
    xr[c][0][e] = odd2 ? u2 : b0; xr[c][2][e] = odd2 ? b2 : u0; xr[c][1][e] = odd2 ? u3 : b1; xr[c][3][e] = odd2 ? b3 : u1; \
    PIN(xr[c][0]); PIN(xr[c][1]); PIN(xr[c][2]); PIN(xr[c][3]); } while (0)
  lds_cptr wrb2 = wrb + 65536; PIN(wrb2);
#define WRB(c) ((WQB(c) < 4 ? wrb : wrb2) + (WQB(c) & 3) * 16384)
#define WRD(c, m) do { wlo[m] = ldstr(WRB(c) + 2048 * ((m) >> 2) + 256 * ((m) & 3)); whi[m] = ldstr(WRB(c) + 2048 * ((m) >> 2) + 1024 + 256 * ((m) & 3)); } while (0)
  WAITV4(6, xr[0][0], xr[0][1], xr[0][2], xr[0][3]);
  STAMP(49);
  QISSUE(2);
  XTR(0, 0); XTR(0, 1); XTR(0, 2); XTR(0, 3);
  STAMP(1);
  BARRIER();
  STAMP(2);
#pragma unroll
  for (int m = 0; m < PD; ++m) WRD(0, m);
#pragma unroll
  for (int c = 0; c < 8; ++c) {
    SBAR();
#pragma unroll
    for (int m = 0; m < 16; ++m) {
      acc[m & 3] = MFMA(cat(wlo[m], whi[m]), __builtin_bit_cast(bf16x8, xr[c][m >> 2]), acc[m & 3]);
      if (c == 0 && m + PD == 16) { BARRIER(); }
      if (m + PD < 16) { WRD(c, m + PD); }
      else if (c < 7) { WRD(c + 1, m + PD - 16); }
      if (m == 0 && c == 0) { QISSUE(3); }
      if (m == 0 && c == 2) { QISSUE(4); QISSUE(5); }
      if (m == 0 && c == 3) { QISSUE(6); QISSUE(7); }
      if (c < 7) {
        if (m == 4) { WAITV4(4 * (c == 0 ? 2 : c == 1 ? 1 : c == 2 ? 2 : 6 - c), xr[c + 1][0], xr[c + 1][1], xr[c + 1][2], xr[c + 1][3]); }
        if (m == 5 && c == 0) { KEEP(pfd0); }
        if (m >= 4 && m < 12 && !(m & 1)) { XTR(c + 1, (m - 4) >> 1); }
      }
      SBAR();
    }
    BARRIER();
    STAMP(3 + c);
  }
#undef XTR
#undef WRD
#undef QISSUE
#pragma unroll
  for (int nb = 0; nb < 4; ++nb)
#pragma unroll
    for (int s = 0; s < 2; ++s) {
      u32x4 w;
#pragma unroll
      for (int e = 0; e < 4; ++e) w[e] = cvtpk(acc[nb][8 * s + 2 * e] * QSCALE, acc[nb][8 * s + 2 * e + 1] * QSCALE);
      st.q[2 * nb + s] = __builtin_bit_cast(bf16x8, w);
    }
#pragma unroll
  for (int i = 0; i < 4; ++i) st.o[i] = f32x16{};
  st.negm = f32x16{}; PIN(st.negm);
  st.mhat = 0.f; st.lsum = 0.f;

  const lds_cptr kb0 = (lds_cptr)L + 1024 * (r32 >> 2) + 64 * (r32 & 3) + 16 * ((0 + hi) ^ ((r32 >> 2) & 3));
  const lds_cptr kb1 = (lds_cptr)L + 1024 * (r32 >> 2) + 64 * (r32 & 3) + 16 * ((2 + hi) ^ ((r32 >> 2) & 3));
  const lds_cptr vb = (lds_cptr)L + 16384 + 1024 * hi + 64 * tq + 32 * tblk + 8 * tp;

  f32x16 A0, A1, B0, B1;
#pragma unroll
  for (int m = 0; m < PD; ++m) st.kf[m] = ldsrd128(((m & 1) ? kb1 : kb0) + 256 * (m >> 1));
  attn_step<true, false>(st, A0, A1, B0, B1, kb0, kb1, vb, kb0 + 32768, kb1 + 32768);
  STAMP(11);
  BARRIER();
  STAMP(12);
  for (int t = 1; t < 15; t += 2) {
    const int s0 = (t & 3) * 32768, s1 = ((t + 1) & 3) * 32768, s2 = ((t + 2) & 3) * 32768;
    attn_step<false, false>(st, B0, B1, A0, A1, kb0 + s0, kb1 + s0, vb + s0, kb0 + s1, kb1 + s1);
    STAMP(11 + 2 * t);
    BARRIER();
    STAMP(12 + 2 * t);
    attn_step<false, false>(st, A0, A1, B0, B1, kb0 + s1, kb1 + s1, vb + s1, kb0 + s2, kb1 + s2);
    STAMP(13 + 2 * t);
    BARRIER();
    STAMP(14 + 2 * t);
  }
  attn_step<false, false>(st, B0, B1, A0, A1, kb0 + 3 * 32768, kb1 + 3 * 32768, vb + 3 * 32768, kb0, kb1);
  STAMP(41);
  BARRIER();
  STAMP(42);
  attn_step<false, true>(st, A0, A1, B0, B1, kb0, kb1, vb, kb0, kb1);

  STAMP(43);
  {
    auto rr = __builtin_amdgcn_permlane32_swap(__float_as_uint(st.lsum), __float_as_uint(st.lsum), false, false);
    const float l = __uint_as_float(rr[0]) + __uint_as_float(rr[1]);
    const float inv = 1.0f / l;
    char* op = ows + (size_t)(((b * NH + h) * 7 + wid) * 8) * 1024 + lane * 16;
#pragma unroll
    for (int db = 0; db < 4; ++db)
#pragma unroll
      for (int s = 0; s < 2; ++s) {
        u32x4 w;
#pragma unroll
        for (int e = 0; e < 4; ++e) w[e] = cvtpk(st.o[db][8 * s + 2 * e] * inv, st.o[db][8 * s + 2 * e + 1] * inv);
        *(u32x4*)(op + (2 * db + s) * 1024) = w;
      }
  }
  STAMP(44); RSTAMP(63);
  STFLUSH(ows + (64 << 20));
}

constexpr int K2_LDS = 131072 + 4096;
__global__ __launch_bounds__(576, 2)
void mha_k2(char* __restrict__ ows, const char* __restrict__ wo, char* __restrict__ out) {
  extern __shared__ __attribute__((aligned(16))) char lds[];
  const int tid = threadIdx.x, lane = tid & 63;
  const int wid = __builtin_amdgcn_readfirstlane(tid >> 6);
  const int lin = (blockIdx.x & 7) * 28 + (blockIdx.x >> 3);
  const int qt = lin >> 3, nt = lin & 7;
  const unsigned lds0 = (unsigned)(uintptr_t)lds;
  const lds_ptr L = (lds_ptr)lds;
  constexpr int STOFF = 131072;
  if (wid < 8) { RSTAMP(62); STAMP(0); }
  if (wid == 8) {
    const int tl = (int)(blockIdx.x >> 3) * 74 + lane;
    const char* p0 = wo + (size_t)2048 * 1024 + (size_t)(tl < 2048 ? tl : 2047) * 128;
    const char* p1 = wo + (size_t)2048 * 1024 + (size_t)((tl + 64 < 2048 && lane < 10) ? tl + 64 : (tl < 2048 ? tl : 2047)) * 128;
    unsigned d0 = 0, d1 = 0;
    for (int r = 0; r < 8; ++r) {
      asm volatile("global_load_dword %0, %2, off " POLT2 "\n\tglobal_load_dword %1, %3, off " POLT2 "\n\ts_waitcnt vmcnt(2)" : "+v"(d0), "+v"(d1) : "v"(p0), "v"(p1) : "memory");
      p0 += 262144; p1 += 262144;
    }
    asm volatile("s_waitcnt vmcnt(0)" : "+v"(d0), "+v"(d1) :: "memory");
    return;
  }
  const int gq0 = 2 * qt, gq1 = 2 * qt + 1;
  const int b0 = gq0 / 7, qb0 = gq0 - 7 * b0, b1 = gq1 / 7, qb1 = gq1 - 7 * b1;
  const __amdgpu_buffer_rsrc_t srdO = __builtin_amdgcn_make_buffer_rsrc((void*)ows, (short)0, NB * NH * 7 * 8 * 1024, 0x00020000);
  const __amdgpu_buffer_rsrc_t srdW = __builtin_amdgcn_make_buffer_rsrc((void*)(wo + nt * 128), (short)0, NH * DH * DM * 2 - nt * 128, 0x00020000);
  const unsigned ob0 = (unsigned)(((b0 * NH + (wid >> 2)) * 7 + qb0) * 8192 + (wid & 3) * 2048), ob1 = (unsigned)(((b1 * NH + (wid >> 2)) * 7 + qb1) * 8192 + (wid & 3) * 2048);
  const unsigned ov = lane * 16;
  const unsigned wl = (4 * (lane >> 5) + ((lane >> 2) & 3)) * 1024 + ((lane >> 4) & 1) * 64 + (lane & 3) * 16;
  const unsigned w0 = wl, w1 = wl + 7168, w2 = wl + 2 * 7168, w3 = wl + 3 * 7168;
  const unsigned wsb = (unsigned)(32 * wid * 1024);
  const unsigned lb = lds0 + wid * 16384;
  const int hi = lane >> 5, tq = (lane >> 2) & 3, tp = lane & 3, tblk = (lane >> 4) & 1;
  const lds_cptr rb = (lds_cptr)L + wid * 16384 + 512 * hi + 64 * tq + 32 * tblk + 8 * tp;
  u32x4 of[4][4];
  f32x16 acc[2][2];
#pragma unroll
  for (int i = 0; i < 2; ++i) { acc[i][0] = f32x16{}; acc[i][1] = f32x16{}; }
#define ISSUE(g) do { \
    const unsigned so_ = (unsigned)((g) * 2 * 7 * 8192); \
    DMA4(POLWO, lb + 4096 * ((g) & 3), w0, w1, w2, w3, srdW, wsb + (unsigned)((g) * 256 * 1024)); \
    BLOAD("", of[(g) & 3][0], ov, srdO, ob0 + so_, 0); \
    BLOAD("", of[(g) & 3][1], ov, srdO, ob1 + so_, 0); \
    BLOAD("", of[(g) & 3][2], ov, srdO, ob0 + so_, 1024); \
    BLOAD("", of[(g) & 3][3], ov, srdO, ob1 + so_, 1024); } while (0)
  ISSUE(0); ISSUE(1); ISSUE(2); ISSUE(3);
#pragma unroll
  for (int g = 0; g < 16; ++g) {
    const int sl = g & 3;
    const int rem = (g + 3 < 16 ? 3 : 15 - g);
    STAMP(1 + 2 * g);
    WAITV4(8 * rem, of[sl][0], of[sl][1], of[sl][2], of[sl][3]);
    STAMP(2 + 2 * g);
    bf16x8 wf[2][2];
#pragma unroll
    for (int ks = 0; ks < 2; ++ks)
#pragma unroll
      for (int nb = 0; nb < 2; ++nb)
        wf[ks][nb] = cat(ldstr(rb + 4096 * sl + 2048 * ks + 256 * nb), ldstr(rb + 4096 * sl + 2048 * ks + 1024 + 256 * nb));
#pragma unroll
    for (int ks = 0; ks < 2; ++ks)
#pragma unroll
      for (int nb = 0; nb < 2; ++nb)
#pragma unroll
        for (int qq = 0; qq < 2; ++qq)
          acc[nb][qq] = MFMA(wf[ks][nb], __builtin_bit_cast(bf16x8, of[sl][2 * ks + qq]), acc[nb][qq]);
    SBAR();
    if (g + 4 < 16) { ISSUE(g + 4); }
    SBAR();
  }
#undef ISSUE
  {
    const int r32 = lane & 31;
#pragma unroll
    for (int qq = 0; qq < 2; ++qq)
#pragma unroll
      for (int nb = 0; nb < 2; ++nb)
#pragma unroll
        for (int rg = 0; rg < 4; ++rg) {
          const int q = 32 * qq + r32, slot = 8 * nb + 2 * rg + hi;
          f32x4 v = {acc[nb][qq][4 * rg], acc[nb][qq][4 * rg + 1], acc[nb][qq][4 * rg + 2], acc[nb][qq][4 * rg + 3]};
          *(__attribute__((address_space(3))) f32x4*)(L + wid * 16384 + q * 256 + ((slot ^ (q & 15)) * 16)) = v;
        }
    STAMP(33);
    __syncthreads();
    STAMP(34);
    const int q = tid >> 3, pc = tid & 7;
    f32x4 s0 = {0, 0, 0, 0}, s1 = {0, 0, 0, 0};
#pragma unroll
    for (int w = 0; w < 8; ++w) {
      s0 += *(const __attribute__((address_space(3))) f32x4*)(L + w * 16384 + q * 256 + (((2 * pc) ^ (q & 15)) * 16));
      s1 += *(const __attribute__((address_space(3))) f32x4*)(L + w * 16384 + q * 256 + (((2 * pc + 1) ^ (q & 15)) * 16));
    }
    u32x4 w = {cvtpk(s0[0], s0[1]), cvtpk(s0[2], s0[3]), cvtpk(s1[0], s1[1]), cvtpk(s1[2], s1[3])};
    *(u32x4*)(out + (size_t)(qt * 64 + q) * (DM * 2) + nt * 128 + pc * 16) = w;
  }
  STAMP(35); RSTAMP(63);
  STFLUSH(ows + (65 << 20));
}

extern "C" void kernel_launch(void* const* d_in, const int* in_sizes, int n_in,
                              void* d_out, int out_size, void* d_ws, size_t ws_size,
                              hipStream_t stream) {
  (void)in_sizes; (void)n_in; (void)out_size; (void)ws_size;
  (void)hipFuncSetAttribute((const void*)mha_k1, hipFuncAttributeMaxDynamicSharedMemorySize, K1_LDS);
  (void)hipFuncSetAttribute((const void*)mha_k2, hipFuncAttributeMaxDynamicSharedMemorySize, K2_LDS);
  mha_k1<<<256, 512, K1_LDS, stream>>>((const char*)d_in[0], (const char*)d_in[1], (const char*)d_in[2], (const char*)d_in[3], (const char*)d_in[4], (char*)d_ws);
  mha_k2<<<224, 576, K2_LDS, stream>>>((char*)d_ws, (const char*)d_in[4], (char*)d_out);
}
```

```cpp
#include <hip/hip_runtime.h>
#include <hip/hip_bf16.h>
#include <stdint.h>

typedef short    bf16x8 __attribute__((ext_vector_type(8)));
typedef short    s16x4  __attribute__((ext_vector_type(4)));
typedef float    f32x16 __attribute__((ext_vector_type(16)));
typedef float    f32x4  __attribute__((ext_vector_type(4)));
typedef float    f32x2  __attribute__((ext_vector_type(2)));
typedef __bf16   bf16x2 __attribute__((ext_vector_type(2)));
typedef unsigned u32x4  __attribute__((ext_vector_type(4)));
typedef __attribute__((address_space(3))) char* lds_ptr;
typedef __attribute__((address_space(3))) const char* lds_cptr;

#define DI __device__ __forceinline__
#define SBAR() __builtin_amdgcn_sched_barrier(0)
#define PIN(x) asm volatile("" : "+v"(x))
#define MFMA(a, b, c) __builtin_amdgcn_mfma_f32_32x32x16_bf16(a, b, c, 0, 0, 0)
#define BARRIER() asm volatile("s_barrier" ::: "memory")

#define STAMP(i)
#define RSTAMP(i)
#define STFLUSH(dbg)
#define POLT "nt"
#define POLW ""
#define POLWO ""
#define POLT2 ""
#define POLO "sc1"
#define MAPK1 1
constexpr int NB = 8, SQ = 224, DM = 512, NH = 32, SK = 1024, DH = 128;
constexpr float QSCALE = 0.088388347648318440f * 1.4426950408889634f;
constexpr float THR = 8.0f;

DI unsigned cvtpk(float lo, float hi) { f32x2 v = {lo, hi}; bf16x2 b = __builtin_convertvector(v, bf16x2); return __builtin_bit_cast(unsigned, b); }
DI bf16x8 ldsrd128(lds_cptr p) { return *(const __attribute__((address_space(3))) bf16x8*)p; }
DI s16x4 ldstr(lds_cptr p) { return __builtin_bit_cast(s16x4, __builtin_amdgcn_ds_read_tr16_b64_v4i16((__attribute__((address_space(3))) s16x4*)p)); }
DI bf16x8 cat(s16x4 a, s16x4 b) { return __builtin_shufflevector(a, b, 0, 1, 2, 3, 4, 5, 6, 7); }
DI void ldswr128(lds_ptr p, u32x4 v) { *(__attribute__((address_space(3))) u32x4*)p = v; }
#define MX3(a, b, c) __builtin_fmaxf(__builtin_fmaxf((a), (b)), (c))

#define DMA4(POL, m0v, v0, v1, v2, v3, srd, soff)                                                     \
  asm volatile("s_mov_b32 m0, %0\n\ts_nop 0\n\t"                                                      \
               "buffer_load_dwordx4 %1, %5, %6 offen " POL " lds\n\t"                                 \
               "buffer_load_dwordx4 %2, %5, %6 offen offset:1024 " POL " lds\n\t"                     \
               "buffer_load_dwordx4 %3, %5, %6 offen offset:2048 " POL " lds\n\t"                     \
               "buffer_load_dwordx4 %4, %5, %6 offen offset:3072 " POL " lds"                         \
               :: "s"(m0v), "v"(v0), "v"(v1), "v"(v2), "v"(v3), "s"(srd), "s"(soff) : "memory")
#define DMA2(POL, m0v, v0, v1, srd, soff)                                                             \
  asm volatile("s_mov_b32 m0, %0\n\ts_nop 0\n\t"                                                      \
               "buffer_load_dwordx4 %1, %3, %4 offen " POL " lds\n\t"                                 \
               "buffer_load_dwordx4 %2, %3, %4 offen offset:1024 " POL " lds"                         \
               :: "s"(m0v), "v"(v0), "v"(v1), "s"(srd), "s"(soff) : "memory")
#define BLOAD(POL, dst, voff, srd, soff, imm)                                                         \
  asm volatile("buffer_load_dwordx4 %0, %1, %2, %3 offen offset:%4 " POL                              \
               : "=v"(dst) : "v"(voff), "s"(srd), "s"(soff), "i"(imm) : "memory")
#define WAITV(n) asm volatile("s_waitcnt vmcnt(%0)" :: "i"(n) : "memory")
#define WAITV4(n, a, b, c, d) asm volatile("s_waitcnt vmcnt(%4)" : "+v"(a), "+v"(b), "+v"(c), "+v"(d) : "i"(n) : "memory")

constexpr int K1_LDS = 163840;
constexpr size_t WS_FLAGS = (size_t)32 << 20, WS_EPOCH = WS_FLAGS + 4096;
DI constexpr int RB(int i) { return (i % 5) == 0 ? 131072 : (i % 5) == 4 ? 0 : (i % 5) * 32768; }
DI constexpr int WQB(int c) { return c == 0 ? 0 : c == 7 ? 1 : c + 1; }

struct AttnState {
  f32x16 o[4];
  bf16x8 q[8];
  f32x16 negm;
  float mhat, lsum;
  bf16x8 kf[16];
  s16x4 vlo[16], vhi[16];
  u32x4 pw[4];
};

constexpr int PD = 6;
template <bool FIRST, bool LAST>
DI void attn_step(AttnState& st, f32x16& C0, f32x16& C1, f32x16& P0, f32x16& P1,
                  lds_cptr kp0, lds_cptr kp1, lds_cptr vp, lds_cptr kn0, lds_cptr kn1) {
#define KRD(m, b0, b1) st.kf[m] = ldsrd128((((m) & 1) ? (b1) : (b0)) + 8192 * ((m) >> 3) + 256 * (((m) & 7) >> 1))
#define VRD(m) do { st.vlo[m] = ldstr(vp + 4096 * ((m) >> 2) + 256 * ((m) & 3)); st.vhi[m] = ldstr(vp + 4096 * ((m) >> 2) + 2048 + 256 * ((m) & 3)); } while (0)
#define EX(v) __builtin_amdgcn_exp2f(v)
#define PE(e) (((e) < 16) ? P0[(e) & 15] : P1[(e) & 15])
  float ma = 0.f, mb = 0.f, fct = 1.f;
  bool resc = false;
  SBAR();
#pragma unroll
  for (int g = 0; g < 32; ++g) {
    if (g < 16) {
      if constexpr (!LAST) {
        if (g == 0) C0 = MFMA(st.kf[0], st.q[0], st.negm);
        else if (g < 8) C0 = MFMA(st.kf[g], st.q[g], C0);
        else if (g == 8) C1 = MFMA(st.kf[8], st.q[0], st.negm);
        else C1 = MFMA(st.kf[g], st.q[g - 8], C1);
        if (g < 8) PIN(C0); else PIN(C1);
      }
    } else {
      if constexpr (!FIRST) {
        const int m = g - 16;
        st.o[m & 3] = MFMA(cat(st.vlo[m], st.vhi[m]), __builtin_bit_cast(bf16x8, st.pw[m >> 2]), st.o[m & 3]);
      }
    }
    {
      const int f = g + PD;
      if (f < 16) { if constexpr (!LAST) { KRD(f, kp0, kp1); } }
      else if (f < 32) { if constexpr (!FIRST) { VRD(f - 16); } }
      else { if constexpr (!LAST) { KRD(f - 32, kn0, kn1); } }
    }
    if (g <= 22) {
      if constexpr (!FIRST) {
        st.lsum += PE(9 + g); PIN(st.lsum);
        if ((g & 1) == 0) { const int e = 8 + g; st.pw[e >> 3][(e >> 1) & 3] = cvtpk(PE(e), PE(e + 1)); PIN(st.pw[e >> 3]); }
      }
    } else {
      if constexpr (!LAST) {
        st.lsum += C0[g - 23]; PIN(st.lsum);
        if ((g & 1) == 0) { const int e = g - 24; st.pw[0][e >> 1] = cvtpk(C0[e], C0[e + 1]); PIN(st.pw[0]); }
      }
    }
    if constexpr (!LAST) {
      if (g == 9)  { ma = MX3(C0[0], C0[1], C0[2]); mb = MX3(C0[3], C0[4], C0[5]); PIN(ma); PIN(mb); }
      if (g == 11) { ma = MX3(ma, C0[6], C0[7]); mb = MX3(mb, C0[8], C0[9]); PIN(ma); PIN(mb); }
      if (g == 13) { ma = MX3(ma, C0[10], C0[11]); mb = MX3(mb, C0[12], C0[13]); PIN(ma); PIN(mb); }
      if (g == 15) { ma = MX3(ma, C0[14], C0[15]); PIN(ma); }
      if (g == 17) { ma = MX3(ma, C1[0], C1[1]); mb = MX3(mb, C1[2], C1[3]); PIN(ma); PIN(mb); }
      if (g == 18) { ma = MX3(ma, C1[4], C1[5]); PIN(ma); }
      if (g == 19) { ma = MX3(ma, C1[6], C1[7]); mb = MX3(mb, C1[8], C1[9]); PIN(ma); PIN(mb); }
      if (g == 20) { mb = MX3(mb, C1[10], C1[11]); PIN(mb); }
      if (g == 21) { ma = MX3(ma, C1[12], C1[13]); mb = MX3(mb, C1[14], C1[15]); PIN(ma); PIN(mb); }
      if (g == 22) {
        if (FIRST || __builtin_expect(__any((ma > THR) | (mb > THR)), 0)) {
          ma = __builtin_fmaxf(ma, mb);
          auto rr = __builtin_amdgcn_permlane32_swap(__float_as_uint(ma), __float_as_uint(ma), false, false);
          float rm = __builtin_fmaxf(__uint_as_float(rr[0]), __uint_as_float(rr[1]));
          const float dl = FIRST ? rm : __builtin_fmaxf(rm, 0.f);
          st.mhat += dl;
#pragma unroll
          for (int r = 0; r < 16; ++r) { C0[r] -= dl; C1[r] -= dl; }
#pragma unroll
          for (int r = 0; r < 16; ++r) st.negm[r] = -st.mhat;
          PIN(st.negm);
          if constexpr (!FIRST) { fct = EX(-dl); st.lsum *= fct; resc = true; }
        }
      }
    }
    if (g <= 21) {
      if constexpr (!FIRST) { const int e = 10 + g; if (e < 16) { P0[e] = EX(P0[e]); PIN(P0); } else { P1[e - 16] = EX(P1[e - 16]); PIN(P1); } }
    } else {
      if constexpr (!LAST) { C0[g - 22] = EX(C0[g - 22]); PIN(C0); }
    }
    SBAR();
  }
  if constexpr (!FIRST && !LAST) {
    if (__builtin_expect(resc, 0)) {
#pragma unroll
      for (int d = 0; d < 4; ++d)
#pragma unroll
        for (int r = 0; r < 16; ++r) st.o[d][r] *= fct;
    }
  }
#undef KRD
#undef VRD
#undef EX
#undef PE
}

__global__ __launch_bounds__(512, 2)
void mha_k1(const char* __restrict__ x, const char* __restrict__ kc, const char* __restrict__ vc,
            const char* __restrict__ wq, const char* __restrict__ wo, char* ows, char* __restrict__ out) {
  extern __shared__ __attribute__((aligned(16))) char lds[];
  const int tid = threadIdx.x, lane = tid & 63;
  const int wid = __builtin_amdgcn_readfirstlane(tid >> 6);
  const int b = 4 * (blockIdx.x & 1) + ((blockIdx.x >> 3) & 3), h = 8 * ((blockIdx.x >> 1) & 3) + (blockIdx.x >> 5);
  const unsigned lds0 = (unsigned)(uintptr_t)lds;
  const lds_ptr L = (lds_ptr)lds;
  RSTAMP(62); STAMP(0);

  const char *pfx, *pfw;
  {
    const int j = blockIdx.x >> 3, half = lane >> 5, l5 = lane & 31;
    const int xrow = (b & 4) * SQ + 28 * j + (l5 < 28 ? l5 : 27);
    const int idx = 32 * j + l5;
    pfw = wq + (size_t)(64 * half + (idx >> 4)) * 8192 + (h & 24) * 256 + (idx & 15) * 128;
    pfx = x + (size_t)xrow * (DM * 2) + half * 128;
  }
  unsigned pfd0;
  const char* pfe = wid >= 4 ? x : (wid & 1) ? pfw + (size_t)(wid >> 1) * (128 * 8192) : pfx + (wid >> 1) * 256;
#define TOUCH(d, p) asm volatile("global_load_dword %0, %1, off " POLT : "=v"(d) : "v"(p) : "memory")
#define KEEP(d) asm volatile("" :: "v"(d) : "memory")
  const __amdgpu_buffer_rsrc_t srdW = __builtin_amdgcn_make_buffer_rsrc((void*)(wq + h * 256), (short)0, DM * 8192 - h * 256, 0x00020000);
  const unsigned pl = ((lane >> 2) & 3), dbl = lane >> 4, c4 = lane & 3;
  const unsigned wv0 = pl * 8192 + dbl * 64 + c4 * 16, wv1 = wv0 + 31744;
#define DMAW2(c) DMA2(POLW, lds0 + WQB(c) * 16384 + 2048 * wid, wv0, wv1, srdW, (unsigned)(((c) * 64 + 8 * wid) * 8192))
  TOUCH(pfd0, pfe);
  const int ob = blockIdx.x & 7, oqt = blockIdx.x >> 6, ont = (blockIdx.x >> 3) & 7;
  const __amdgpu_buffer_rsrc_t srdWo = __builtin_amdgcn_make_buffer_rsrc((void*)(wo + ont * 128), (short)0, NH * DH * DM * 2 - ont * 128, 0x00020000);
  const unsigned ow0 = (4 * (lane >> 5) + ((lane >> 2) & 3)) * 1024 + ((lane >> 4) & 1) * 64 + (lane & 3) * 16;
  const unsigned ow1 = ow0 + 7168, ow2 = ow0 + 2 * 7168, ow3 = ow0 + 3 * 7168;
  unsigned long long* const epochp = (unsigned long long*)(ows + WS_EPOCH);
  unsigned long long* const flags = (unsigned long long*)(ows + WS_FLAGS);
  unsigned long long tag = 0;
  unsigned td = 0;
#define RR(i) (((i) + 2 * ont) & 15)
  if (wid == 7) {
    const __amdgpu_buffer_rsrc_t srdK = __builtin_amdgcn_make_buffer_rsrc((void*)(kc + (size_t)(b * NH + h) * (SK * DH * 2)), (short)0, SK * DH * 2, 0x00020000);
    const __amdgpu_buffer_rsrc_t srdV = __builtin_amdgcn_make_buffer_rsrc((void*)(vc + (size_t)(b * NH + h) * (SK * DH * 2)), (short)0, SK * DH * 2, 0x00020000);
    const unsigned wv2 = wv0 + 2 * 31744, wv3 = wv0 + 3 * 31744;
    const unsigned vv = pl * 256 + dbl * 64 + c4 * 16;
    const unsigned kv0 = pl * 256 + dbl * 64 + ((c4 ^ 0) * 16), kv1 = pl * 256 + dbl * 64 + ((c4 ^ 1) * 16),
                   kv2 = pl * 256 + dbl * 64 + ((c4 ^ 2) * 16), kv3 = pl * 256 + dbl * 64 + ((c4 ^ 3) * 16);
#define DMAW(c) do { _Pragma("unroll") for (int i_ = 0; i_ < 4; ++i_) DMA4(POLW, lds0 + (unsigned)(WQB(c) * 16384 + 4096 * i_), wv0, wv1, wv2, wv3, srdW, (unsigned)(((c) * 64 + 16 * i_) * 8192)); } while (0)
#define DMAK(t) do { _Pragma("unroll") for (int i_ = 0; i_ < 4; ++i_) DMA4("nt", lds0 + (unsigned)(((t) & 3) * 32768 + 4096 * i_), kv0, kv1, kv2, kv3, srdK, (unsigned)((t) * 16384 + 4096 * i_)); } while (0)
#define DMAV(t) do { _Pragma("unroll") for (int i_ = 0; i_ < 4; ++i_) DMA4("nt", lds0 + (unsigned)((((t) + 1) & 3) * 32768 + 16384 + 4096 * i_), vv, vv, vv, vv, srdV, (unsigned)((t) * 16384 + 4096 * i_)); } while (0)
    unsigned t4, t5, t6, t7;
    asm volatile("global_load_dwordx2 %0, %1, off sc1" : "=v"(tag) : "v"(epochp) : "memory");
    STAMP(45); DMAW2(0); STAMP(46);
    BARRIER();
    STAMP(47); DMAW2(1); STAMP(48);
    WAITV(2); STAMP(49); STAMP(1); STAMP(2); BARRIER(); STAMP(3);
    DMAW(2); DMAW(3);
    WAITV(32); BARRIER();
    { const char* p4 = pfx + 512; const char* p5 = pfw + (size_t)2 * 128 * 8192; const char* p6 = pfx + 768; const char* p7 = pfw + (size_t)3 * 128 * 8192;
      TOUCH(t4, p4); TOUCH(t5, p5); TOUCH(t6, p6); TOUCH(t7, p7); } STAMP(4);
    WAITV(20); STAMP(5); BARRIER(); STAMP(6);
    STAMP(7);
    WAITV(4); STAMP(8); BARRIER(); STAMP(9);
    DMAW(4); DMAW(5); STAMP(10);
    WAITV(16); STAMP(11); BARRIER(); STAMP(12);
    KEEP(pfd0); KEEP(t4); KEEP(t5); KEEP(t6); KEEP(t7);
    DMAW(6); DMAW(7); STAMP(13);
    WAITV(32); STAMP(14); BARRIER(); STAMP(15);
    DMAK(0); DMAK(1); STAMP(16);
    WAITV(48); STAMP(17); BARRIER(); STAMP(18);
    STAMP(19);
    WAITV(32); STAMP(20); BARRIER(); STAMP(21);
    DMAV(0); DMAK(2); STAMP(22);
    STAMP(23); BARRIER(); STAMP(24);
    STAMP(25);
    WAITV(32); STAMP(26); BARRIER(); STAMP(27);
    DMAV(1); DMAK(3); STAMP(28);
    WAITV(32); STAMP(29); BARRIER(); STAMP(30);
    DMAV(2); DMAK(4); DMAV(3); STAMP(31);
    WAITV(48); STAMP(32); BARRIER(); STAMP(33);
    DMAK(5); DMAV(4); STAMP(34);
    WAITV(48); STAMP(35); BARRIER(); STAMP(36);
    for (int t = 2; t < 12; ++t) {
      DMAK(t + 4); DMAV(t + 3);
      WAITV(48);
      BARRIER();
    }
    const char* tw = wo + (size_t)(blockIdx.x >> 3) * 8192 + lane * 128;
#define TW(r) do { const char* p_ = tw + (size_t)((((r) + 2 * (lane & 7)) & 15) * 262144); asm volatile("global_load_dword %0, %1, off" : "+v"(td) : "v"(p_) : "memory"); } while (0)
#define RW(i) do { _Pragma("unroll") for (int w_ = 0; w_ < 8; ++w_) DMA4("", lds0 + (unsigned)(RB(i) + 4096 * w_), ow0, ow1, ow2, ow3, srdWo, (unsigned)((RR(i) * 256 + 32 * w_) * 1024)); } while (0)
    DMAV(15); TW(0); TW(1); TW(2); TW(3); TW(4); TW(5);
    WAITV(38);
    BARRIER();
    RW(0); RW(1); TW(6); TW(7); TW(8); TW(9); TW(10);
    WAITV(63);
    BARRIER();
    RW(2); TW(11); TW(12); TW(13); TW(14); TW(15);
    WAITV(5);
    BARRIER();
    RW(3);
    asm volatile("" : "+v"(tag));
#undef TW
#undef RW
#undef DMAW
#undef DMAK
#undef DMAV
  } else {
  const int r32 = lane & 31, hi = lane >> 5;
  const __amdgpu_buffer_rsrc_t srdX = __builtin_amdgcn_make_buffer_rsrc((void*)(x + (size_t)(b * SQ + wid * 32) * (DM * 2)), (short)0, 32 * DM * 2, 0x00020000);
  const unsigned xv = ((lane >> 2) & 7) * 4096 + (lane >> 5) * 64 + (lane & 3) * 16;
  u32x4 xr[8][4];
#define QISSUE(c) do { _Pragma("unroll") for (int g_ = 0; g_ < 4; ++g_) BLOAD(POLW, xr[c][g_], xv, srdX, 0u, 1024 * g_ + 128 * (c)); } while (0)
  STAMP(45);
  DMAW2(0); QISSUE(0);
  STAMP(46);
  BARRIER();
  STAMP(47);
  DMAW2(1); QISSUE(1);
  STAMP(48);
  const int tq = (lane >> 2) & 3, tp = lane & 3, tblk = (lane >> 4) & 1;
  const int tpp = ((tp & 1) << 1) | (tp >> 1);
  const lds_cptr wrb = (lds_cptr)L + 8192 * hi + 64 * tq + 32 * tblk + 8 * tpp;
  const bool odd1 = lane & 1, odd2 = lane & 2;

  AttnState st;
  f32x16 acc[4];
#pragma unroll
  for (int i = 0; i < 4; ++i) acc[i] = f32x16{};
  s16x4 wlo[16], whi[16];
#define DPP1(v) (unsigned)__builtin_amdgcn_mov_dpp((int)(v), 0xB1, 0xF, 0xF, true)
#define DPP2(v) (unsigned)__builtin_amdgcn_mov_dpp((int)(v), 0x4E, 0xF, 0xF, true)
#define XTR(c, e) do { \
    const unsigned a0 = xr[c][0][e], a1 = xr[c][1][e], a2 = xr[c][2][e], a3 = xr[c][3][e]; \
    const unsigned t0 = DPP1(a0), t1 = DPP1(a1), t2 = DPP1(a2), t3 = DPP1(a3); \
    const unsigned b0 = odd1 ? t1 : a0, b1 = odd1 ? a1 : t0, b2 = odd1 ? t3 : a2, b3 = odd1 ? a3 : t2; \
    const unsigned u0 = DPP2(b0), u1 = DPP2(b1), u2 = DPP2(b2), u3 = DPP2(b3); \
    xr[c][0][e] = odd2 ? u2 : b0; xr[c][2][e] = odd2 ? b2 : u0; xr[c][1][e] = odd2 ? u3 : b1; xr[c][3][e] = odd2 ? b3 : u1; \
    PIN(xr[c][0]); PIN(xr[c][1]); PIN(xr[c][2]); PIN(xr[c][3]); } while (0)
  lds_cptr wrb2 = wrb + 65536; PIN(wrb2);
#define WRB(c) ((WQB(c) < 4 ? wrb : wrb2) + (WQB(c) & 3) * 16384)
#define WRD(c, m) do { wlo[m] = ldstr(WRB(c) + 2048 * ((m) >> 2) + 256 * ((m) & 3)); whi[m] = ldstr(WRB(c) + 2048 * ((m) >> 2) + 1024 + 256 * ((m) & 3)); } while (0)
  WAITV4(6, xr[0][0], xr[0][1], xr[0][2], xr[0][3]);
  STAMP(49);
  QISSUE(2);
  XTR(0, 0); XTR(0, 1); XTR(0, 2); XTR(0, 3);
  STAMP(1);
  BARRIER();
  STAMP(2);
#pragma unroll
  for (int m = 0; m < PD; ++m) WRD(0, m);
#pragma unroll
  for (int c = 0; c < 8; ++c) {
    SBAR();
#pragma unroll
    for (int m = 0; m < 16; ++m) {
      acc[m & 3] = MFMA(cat(wlo[m], whi[m]), __builtin_bit_cast(bf16x8, xr[c][m >> 2]), acc[m & 3]);
      if (c == 0 && m + PD == 16) { BARRIER(); }
      if (m + PD < 16) { WRD(c, m + PD); }
      else if (c < 7) { WRD(c + 1, m + PD - 16); }
      if (m == 0 && c == 0) { QISSUE(3); }
      if (m == 0 && c == 2) { QISSUE(4); QISSUE(5); }
      if (m == 0 && c == 3) { QISSUE(6); QISSUE(7); }
      if (c < 7) {
        if (m == 4) { WAITV4(4 * (c == 0 ? 2 : c == 1 ? 1 : c == 2 ? 2 : 6 - c), xr[c + 1][0], xr[c + 1][1], xr[c + 1][2], xr[c + 1][3]); }
        if (m == 5 && c == 0) { KEEP(pfd0); }
        if (m >= 4 && m < 12 && !(m & 1)) { XTR(c + 1, (m - 4) >> 1); }
      }
      SBAR();
    }
    BARRIER();
    STAMP(3 + c);
  }
#undef XTR
#undef WRD
#undef QISSUE
#pragma unroll
  for (int nb = 0; nb < 4; ++nb)
#pragma unroll
    for (int s = 0; s < 2; ++s) {
      u32x4 w;
#pragma unroll
      for (int e = 0; e < 4; ++e) w[e] = cvtpk(acc[nb][8 * s + 2 * e] * QSCALE, acc[nb][8 * s + 2 * e + 1] * QSCALE);
      st.q[2 * nb + s] = __builtin_bit_cast(bf16x8, w);
    }
#pragma unroll
  for (int i = 0; i < 4; ++i) st.o[i] = f32x16{};
  st.negm = f32x16{}; PIN(st.negm);
  st.mhat = 0.f; st.lsum = 0.f;

  const lds_cptr kb0 = (lds_cptr)L + 1024 * (r32 >> 2) + 64 * (r32 & 3) + 16 * ((0 + hi) ^ ((r32 >> 2) & 3));
  const lds_cptr kb1 = (lds_cptr)L + 1024 * (r32 >> 2) + 64 * (r32 & 3) + 16 * ((2 + hi) ^ ((r32 >> 2) & 3));
  const lds_cptr vb = (lds_cptr)L + 16384 + 1024 * hi + 64 * tq + 32 * tblk + 8 * tp;

  f32x16 A0, A1, B0, B1;
#pragma unroll
  for (int m = 0; m < PD; ++m) st.kf[m] = ldsrd128(((m & 1) ? kb1 : kb0) + 256 * (m >> 1));
  attn_step<true, false>(st, A0, A1, B0, B1, kb0, kb1, vb, kb0 + 32768, kb1 + 32768);
  STAMP(11);
  BARRIER();
  STAMP(12);
  for (int t = 1; t < 15; t += 2) {
    const int s0 = (t & 3) * 32768, s1 = ((t + 1) & 3) * 32768, s2 = ((t + 2) & 3) * 32768;
    attn_step<false, false>(st, B0, B1, A0, A1, kb0 + s0, kb1 + s0, vb + s0, kb0 + s1, kb1 + s1);
    STAMP(11 + 2 * t);
    BARRIER();
    STAMP(12 + 2 * t);
    attn_step<false, false>(st, A0, A1, B0, B1, kb0 + s1, kb1 + s1, vb + s1, kb0 + s2, kb1 + s2);
    STAMP(13 + 2 * t);
    BARRIER();
    STAMP(14 + 2 * t);
  }
  attn_step<false, false>(st, B0, B1, A0, A1, kb0 + 3 * 32768, kb1 + 3 * 32768, vb + 3 * 32768, kb0, kb1);
  STAMP(41);
  BARRIER();
  STAMP(42);
  attn_step<false, true>(st, A0, A1, B0, B1, kb0, kb1, vb, kb0, kb1);

  STAMP(43);
  {
    auto rr = __builtin_amdgcn_permlane32_swap(__float_as_uint(st.lsum), __float_as_uint(st.lsum), false, false);
    const float l = __uint_as_float(rr[0]) + __uint_as_float(rr[1]);
    const float inv = 1.0f / l;
    const __amdgpu_buffer_rsrc_t srdOs = __builtin_amdgcn_make_buffer_rsrc((void*)(ows + (size_t)(((b * NH + h) * 7 + wid) * 8) * 1024), (short)0, 8192, 0x00020000);
#pragma unroll
    for (int db = 0; db < 4; ++db)
#pragma unroll
      for (int s = 0; s < 2; ++s) {
        u32x4 w;
#pragma unroll
        for (int e = 0; e < 4; ++e) w[e] = cvtpk(st.o[db][8 * s + 2 * e] * inv, st.o[db][8 * s + 2 * e + 1] * inv);
        __builtin_amdgcn_raw_buffer_store_b128(w, srdOs, lane * 16 + (2 * db + s) * 1024, 0, 16);
      }
    STAMP(44);
  }
  }

  {
    constexpr int NA = 6;
    const int r32 = lane & 31, hi = lane >> 5, tq = (lane >> 2) & 3, tp = lane & 3, tblk = (lane >> 4) & 1;
    const __amdgpu_buffer_rsrc_t srdO = __builtin_amdgcn_make_buffer_rsrc((void*)(ows + (size_t)ob * (NH * 7 * 8192)), (short)0, NH * 7 * 8192, 0x00020000);
    const int ql0 = 56 * oqt + (r32 < 28 ? r32 : 27), ql1 = ql0 + 28;
    const unsigned ov0 = (ql0 >> 5) * 8192 + ((ql0 & 31) + 32 * hi) * 16, ov1 = (ql1 >> 5) * 8192 + ((ql1 & 31) + 32 * hi) * 16;
    unsigned osb = (unsigned)__builtin_amdgcn_readfirstlane((wid >> 2) * 7 * 8192 + (wid & 3) * 2048);
    asm volatile("s_nop 4" : "+s"(osb));
    const unsigned wsb = (unsigned)(32 * wid * 1024);
    const lds_cptr rbl = (lds_cptr)L + wid * 4096 + 512 * hi + 64 * tq + 32 * tblk + 8 * tp;
    lds_cptr rbm = rbl + 65536; PIN(rbm);
    lds_cptr rbh = rbl + 131072; PIN(rbh);
#define RBP(i) (RB(i) >= 131072 ? rbh + (RB(i) - 131072) : RB(i) >= 65536 ? rbm + (RB(i) - 65536) : rbl + RB(i))
    bf16x8 wf[NA][4];
    u32x4 of[4][4];
    f32x16 acc[2][2];
#define ORR(i) RR(i)
#define WRR(i) RR(i)
#define WDMA(i) DMA4("", lds0 + (unsigned)(RB(i)) + 4096u * wid, ow0, ow1, ow2, ow3, srdWo, wsb + (unsigned)(WRR(i) * 256 * 1024))
#define WREAD(i) do { _Pragma("unroll") for (int f_ = 0; f_ < 4; ++f_) \
      wf[(i) % NA][f_] = cat(ldstr(RBP(i) + 2048 * (f_ >> 1) + 256 * (f_ & 1)), ldstr(RBP(i) + 2048 * (f_ >> 1) + 1024 + 256 * (f_ & 1))); \
      asm volatile("s_waitcnt lgkmcnt(0)" : "+v"(wf[(i) % NA][0]), "+v"(wf[(i) % NA][1]), "+v"(wf[(i) % NA][2]), "+v"(wf[(i) % NA][3]) :: "memory"); } while (0)
#define OLOAD(i) do { const unsigned so_ = osb + (unsigned)(ORR(i) * 2 * 7 * 8192); \
      BLOAD(POLO, of[(i) & 3][0], ov0, srdO, so_, 0); BLOAD(POLO, of[(i) & 3][1], ov1, srdO, so_, 0); \
      BLOAD(POLO, of[(i) & 3][2], ov0, srdO, so_, 1024); BLOAD(POLO, of[(i) & 3][3], ov1, srdO, so_, 1024); } while (0)
    WREAD(0); WREAD(1); WREAD(2);
    BARRIER();
    WDMA(5); WDMA(6); WDMA(7);
    WAITV(12);
    KEEP(td);
    STAMP(50);
    BARRIER();
    if (wid == 7) {
      asm volatile("" : "+v"(tag));
      tag += 1;
      if (lane == 0) __hip_atomic_store(flags + b * NH + h, tag, __ATOMIC_RELAXED, __HIP_MEMORY_SCOPE_AGENT);
    }
    STAMP(51);
    WDMA(4);
    WREAD(3); WDMA(8);
    WAITV(16);
    WREAD(5); WDMA(10);
    WAITV(8);
    KEEP(td);
    WREAD(4); WDMA(9);
#pragma unroll
    for (int i = 0; i < 2; ++i) { acc[i][0] = f32x16{}; acc[i][1] = f32x16{}; }
    STAMP(52);
    if (wid == 7) {
      const unsigned long long* fp = flags + ob * NH + r32;
      for (unsigned spins = 0; spins < (1u << 18); ++spins) {
        const unsigned long long v = __hip_atomic_load(fp, __ATOMIC_RELAXED, __HIP_MEMORY_SCOPE_AGENT);
        if (__all(v == tag)) break;
        __builtin_amdgcn_s_sleep(2);
      }
    }
    STAMP(53);
    BARRIER();
    STAMP(54);
#define WREAD_NW(i) do { _Pragma("unroll") for (int f_ = 0; f_ < 4; ++f_) \
      wf[(i) % NA][f_] = cat(ldstr(RBP(i) + 2048 * (f_ >> 1) + 256 * (f_ & 1)), ldstr(RBP(i) + 2048 * (f_ >> 1) + 1024 + 256 * (f_ & 1))); } while (0)
#define WRWAIT(i) asm volatile("s_waitcnt lgkmcnt(0)" : "+v"(wf[(i) % NA][0]), "+v"(wf[(i) % NA][1]), "+v"(wf[(i) % NA][2]), "+v"(wf[(i) % NA][3]) :: "memory")
    OLOAD(0); OLOAD(1); OLOAD(2);
#pragma unroll
    for (int i = 0; i < 16; ++i) {
      constexpr int LASTD = 15 - NA - 4;
      const int nd = (i - 1 >= 1 && i - 1 <= LASTD) + (i - 2 >= 1 && i - 2 <= LASTD);
      const int cnt = 4 * (15 - i < 2 ? 15 - i : 2) + 4 * nd;
      WAITV4(cnt, of[i & 3][0], of[i & 3][1], of[i & 3][2], of[i & 3][3]);
      if (i >= 1 && i <= LASTD) { WRWAIT(i - 1 + NA); WDMA(i + NA + 4); }
      if (i + 3 < 16) { OLOAD(i + 3); }
      SBAR();
#pragma unroll
      for (int ks = 0; ks < 2; ++ks)
#pragma unroll
        for (int nb = 0; nb < 2; ++nb)
#pragma unroll
          for (int vb = 0; vb < 2; ++vb)
            acc[nb][vb] = MFMA(wf[i % NA][2 * ks + nb], __builtin_bit_cast(bf16x8, of[i & 3][2 * ks + vb]), acc[nb][vb]);
      SBAR();
      if (i + NA < 16) { WREAD_NW(i + NA); }
      SBAR();
    }
#undef WREAD_NW
#undef WRWAIT
    STAMP(55);
#undef WDMA
#undef WREAD
#undef OLOAD
#undef RBP
#pragma unroll
    for (int vb = 0; vb < 2; ++vb)
#pragma unroll
      for (int nb = 0; nb < 2; ++nb)
#pragma unroll
        for (int rg = 0; rg < 4; ++rg) {
          const int q = 32 * vb + r32, slot = 8 * nb + 2 * rg + hi;
          f32x4 v = {acc[nb][vb][4 * rg], acc[nb][vb][4 * rg + 1], acc[nb][vb][4 * rg + 2], acc[nb][vb][4 * rg + 3]};
          *(__attribute__((address_space(3))) f32x4*)(L + (q >> 4) * 32768 + wid * 4096 + (q & 15) * 256 + ((slot ^ (q & 15)) * 16)) = v;
        }
    asm volatile("s_waitcnt lgkmcnt(0)" ::: "memory");
    BARRIER();
    STAMP(56);
    const int q = tid >> 3, pc = tid & 7;
    f32x4 s0 = {0, 0, 0, 0}, s1 = {0, 0, 0, 0};
#pragma unroll
    for (int w = 0; w < 8; ++w) {
      s0 += *(const __attribute__((address_space(3))) f32x4*)(L + (q >> 4) * 32768 + w * 4096 + (q & 15) * 256 + (((2 * pc) ^ (q & 15)) * 16));
      s1 += *(const __attribute__((address_space(3))) f32x4*)(L + (q >> 4) * 32768 + w * 4096 + (q & 15) * 256 + (((2 * pc + 1) ^ (q & 15)) * 16));
    }
    u32x4 w = {cvtpk(s0[0], s0[1]), cvtpk(s0[2], s0[3]), cvtpk(s1[0], s1[1]), cvtpk(s1[2], s1[3])};
    if ((q & 31) < 28)
      *(u32x4*)(out + (size_t)(ob * SQ + 56 * oqt + 28 * (q >> 5) + (q & 31)) * (DM * 2) + ont * 128 + pc * 16) = w;
    if (blockIdx.x == 0 && wid == 7) {
      for (unsigned spins = 0; spins < (1u << 18); ++spins) {
        bool ok = true;
#pragma unroll
        for (int k = 0; k < 4; ++k) ok &= __hip_atomic_load(flags + 64 * k + lane, __ATOMIC_RELAXED, __HIP_MEMORY_SCOPE_AGENT) == tag;
        if (__all(ok)) break;
        __builtin_amdgcn_s_sleep(2);
      }
      if (lane == 0) __hip_atomic_store(epochp, tag, __ATOMIC_RELAXED, __HIP_MEMORY_SCOPE_AGENT);
    }
  }
  STAMP(57); RSTAMP(63);
  STFLUSH(ows + (64 << 20));
}

extern "C" void kernel_launch(void* const* d_in, const int* in_sizes, int n_in,
                              void* d_out, int out_size, void* d_ws, size_t ws_size,
                              hipStream_t stream) {
  (void)in_sizes; (void)n_in; (void)out_size; (void)ws_size;
  (void)hipFuncSetAttribute((const void*)mha_k1, hipFuncAttributeMaxDynamicSharedMemorySize, K1_LDS);
  mha_k1<<<256, 512, K1_LDS, stream>>>((const char*)d_in[0], (const char*)d_in[1], (const char*)d_in[2], (const char*)d_in[3], (const char*)d_in[4], (char*)d_ws, (char*)d_out);
}
```

```cpp
#include <hip/hip_runtime.h>
#include <hip/hip_bf16.h>
#include <stdint.h>
#include <utility>
#include <type_traits>

typedef short    bf16x8 __attribute__((ext_vector_type(8)));
typedef short    s16x4  __attribute__((ext_vector_type(4)));
typedef float    f32x16 __attribute__((ext_vector_type(16)));
typedef float    f32x4  __attribute__((ext_vector_type(4)));
typedef float    f32x2  __attribute__((ext_vector_type(2)));
typedef __bf16   bf16x2 __attribute__((ext_vector_type(2)));
typedef unsigned u32x4  __attribute__((ext_vector_type(4)));
typedef __attribute__((address_space(3))) char* lds_ptr;
typedef __attribute__((address_space(3))) const char* lds_cptr;

#define DI __device__ __forceinline__
#define SBAR() __builtin_amdgcn_sched_barrier(0)
#define PIN(x) asm volatile("" : "+v"(x))
#define MFMA(a, b, c) __builtin_amdgcn_mfma_f32_32x32x16_bf16(a, b, c, 0, 0, 0)
#define BARRIER() asm volatile("s_barrier" ::: "memory")

#define STAMP(i)
#define RSTAMP(i)
#define LSTAMP(i)
#define STFLUSH(dbg)
#define POLT "nt"
#define POLW ""
#define POLWO ""
#define POLT2 ""
#define POLO "sc1"
#define P3NA 8
#define TSCHED 3
#define PRIO_TOGGLE 16
#define MAPK1 1
constexpr int NB = 8, SQ = 224, DM = 512, NH = 32, SK = 1024, DH = 128;
constexpr float QSCALE = 0.088388347648318440f * 1.4426950408889634f;
constexpr float THR = 8.0f;

template <int... Is, class F> DI void static_for_(std::integer_sequence<int, Is...>, F&& f) { (f(std::integral_constant<int, Is>{}), ...); }
template <int N, class F> DI void static_for(F&& f) { static_for_(std::make_integer_sequence<int, N>{}, f); }
DI unsigned cvtpk(float lo, float hi) { f32x2 v = {lo, hi}; bf16x2 b = __builtin_convertvector(v, bf16x2); return __builtin_bit_cast(unsigned, b); }
DI bf16x8 ldsrd128(lds_cptr p) { return *(const __attribute__((address_space(3))) bf16x8*)p; }
DI s16x4 ldstr(lds_cptr p) { return __builtin_bit_cast(s16x4, __builtin_amdgcn_ds_read_tr16_b64_v4i16((__attribute__((address_space(3))) s16x4*)p)); }
DI bf16x8 cat(s16x4 a, s16x4 b) { return __builtin_shufflevector(a, b, 0, 1, 2, 3, 4, 5, 6, 7); }
DI void ldswr128(lds_ptr p, u32x4 v) { *(__attribute__((address_space(3))) u32x4*)p = v; }
#define MX3(a, b, c) __builtin_fmaxf(__builtin_fmaxf((a), (b)), (c))

#define DMA4(POL, m0v, v0, v1, v2, v3, srd, soff)                                                     \
  asm volatile("s_mov_b32 m0, %0\n\ts_nop 0\n\t"                                                      \
               "buffer_load_dwordx4 %1, %5, %6 offen " POL " lds\n\t"                                 \
               "buffer_load_dwordx4 %2, %5, %6 offen offset:1024 " POL " lds\n\t"                     \
               "buffer_load_dwordx4 %3, %5, %6 offen offset:2048 " POL " lds\n\t"                     \
               "buffer_load_dwordx4 %4, %5, %6 offen offset:3072 " POL " lds"                         \
               :: "s"(m0v), "v"(v0), "v"(v1), "v"(v2), "v"(v3), "s"(srd), "s"(soff) : "memory")
#define DMA2(POL, m0v, v0, v1, srd, soff)                                                             \
  asm volatile("s_mov_b32 m0, %0\n\ts_nop 0\n\t"                                                      \
               "buffer_load_dwordx4 %1, %3, %4 offen " POL " lds\n\t"                                 \
               "buffer_load_dwordx4 %2, %3, %4 offen offset:1024 " POL " lds"                         \
               :: "s"(m0v), "v"(v0), "v"(v1), "s"(srd), "s"(soff) : "memory")
#define BLOAD(POL, dst, voff, srd, soff, imm)                                                         \
  asm volatile("buffer_load_dwordx4 %0, %1, %2, %3 offen offset:%4 " POL                              \
               : "=v"(dst) : "v"(voff), "s"(srd), "s"(soff), "i"(imm) : "memory")
#define WAITV(n) asm volatile("s_waitcnt vmcnt(%0)" :: "i"(n) : "memory")
#define WAITV4(n, a, b, c, d) asm volatile("s_waitcnt vmcnt(%4)" : "+v"(a), "+v"(b), "+v"(c), "+v"(d) : "i"(n) : "memory")

constexpr int K1_LDS = 163840;
constexpr size_t WS_FLAGS = (size_t)32 << 20, WS_EPOCH = WS_FLAGS + 4096;
DI constexpr int RB(int i) { return (i % 5) == 0 ? 131072 : (i % 5) == 4 ? 0 : (i % 5) * 32768; }
DI constexpr int WQB(int c) { return c == 0 ? 0 : c == 7 ? 1 : c + 1; }

struct AttnState {
  f32x16 o[4];
  bf16x8 q[8];
  f32x16 negm;
  float mhat, lsum;
  bf16x8 kf[16];
  s16x4 vlo[16], vhi[16];
  u32x4 pw[4];
};

#define PDV 5
constexpr int PD = PDV;
template <bool FIRST, bool LAST>
DI void attn_step(AttnState& st, f32x16& C0, f32x16& C1, f32x16& P0, f32x16& P1,
                  lds_cptr kp0, lds_cptr kp1, lds_cptr vp, lds_cptr kn0, lds_cptr kn1, const bool young) {
#define KRD(m, b0, b1) st.kf[m] = ldsrd128((((m) & 1) ? (b1) : (b0)) + 8192 * ((m) >> 3) + 256 * (((m) & 7) >> 1))
#define VRD(m) do { st.vlo[m] = ldstr(vp + 4096 * ((m) >> 2) + 256 * ((m) & 3)); st.vhi[m] = ldstr(vp + 4096 * ((m) >> 2) + 2048 + 256 * ((m) & 3)); } while (0)
#define EX(v) __builtin_amdgcn_exp2f(v)
#define PE(e) (((e) < 16) ? P0[(e) & 15] : P1[(e) & 15])
  float ma = 0.f, mb = 0.f, fct = 1.f;
  bool resc = false;
  SBAR();
#pragma unroll
  for (int g = 0; g < 32; ++g) {
    if (g == 0) { if (young) __builtin_amdgcn_s_setprio(1); }
    if (g == PRIO_TOGGLE) { __builtin_amdgcn_s_setprio(0); }
    if (g < 16) {
      if constexpr (!LAST) {
        if (g == 0) C0 = MFMA(st.kf[0], st.q[0], st.negm);
        else if (g < 8) C0 = MFMA(st.kf[g], st.q[g], C0);
        else if (g == 8) C1 = MFMA(st.kf[8], st.q[0], st.negm);
        else C1 = MFMA(st.kf[g], st.q[g - 8], C1);
        if (g < 8) PIN(C0); else PIN(C1);
      }
    } else {
      if constexpr (!FIRST) {
        const int m = g - 16;
        st.o[m & 3] = MFMA(cat(st.vlo[m], st.vhi[m]), __builtin_bit_cast(bf16x8, st.pw[m >> 2]), st.o[m & 3]);
      }
    }
    {
      const int f = g + PD;
      if (f < 16) { if constexpr (!LAST) { KRD(f, kp0, kp1); } }
      else if (f < 32) { if constexpr (!FIRST) { VRD(f - 16); } }
      else { if constexpr (!LAST) { KRD(f - 32, kn0, kn1); } }
    }
    if (g <= 22) {
      if constexpr (!FIRST) {
        st.lsum += PE(9 + g); PIN(st.lsum);
        if ((g & 1) == 0) { const int e = 8 + g; st.pw[e >> 3][(e >> 1) & 3] = cvtpk(PE(e), PE(e + 1)); PIN(st.pw[e >> 3]); }
      }
    } else {
      if constexpr (!LAST) {
        st.lsum += C0[g - 23]; PIN(st.lsum);
        if ((g & 1) == 0) { const int e = g - 24; st.pw[0][e >> 1] = cvtpk(C0[e], C0[e + 1]); PIN(st.pw[0]); }
      }
    }
    if constexpr (!LAST) {
      if (g == 9)  { ma = MX3(C0[0], C0[1], C0[2]); mb = MX3(C0[3], C0[4], C0[5]); PIN(ma); PIN(mb); }
      if (g == 11) { ma = MX3(ma, C0[6], C0[7]); mb = MX3(mb, C0[8], C0[9]); PIN(ma); PIN(mb); }
      if (g == 13) { ma = MX3(ma, C0[10], C0[11]); mb = MX3(mb, C0[12], C0[13]); PIN(ma); PIN(mb); }
      if (g == 15) { ma = MX3(ma, C0[14], C0[15]); PIN(ma); }
      if (g == 17) { ma = MX3(ma, C1[0], C1[1]); mb = MX3(mb, C1[2], C1[3]); PIN(ma); PIN(mb); }
      if (g == 18) { ma = MX3(ma, C1[4], C1[5]); PIN(ma); }
      if (g == 19) { ma = MX3(ma, C1[6], C1[7]); mb = MX3(mb, C1[8], C1[9]); PIN(ma); PIN(mb); }
      if (g == 20) { mb = MX3(mb, C1[10], C1[11]); PIN(mb); }
      if (g == 21) { ma = MX3(ma, C1[12], C1[13]); mb = MX3(mb, C1[14], C1[15]); PIN(ma); PIN(mb); }
      if (g == 22) {
        if (FIRST || __builtin_expect(__any((ma > THR) | (mb > THR)), 0)) {
          ma = __builtin_fmaxf(ma, mb);
          auto rr = __builtin_amdgcn_permlane32_swap(__float_as_uint(ma), __float_as_uint(ma), false, false);
          float rm = __builtin_fmaxf(__uint_as_float(rr[0]), __uint_as_float(rr[1]));
          const float dl = FIRST ? rm : __builtin_fmaxf(rm, 0.f);
          st.mhat += dl;
#pragma unroll
          for (int r = 0; r < 16; ++r) { C0[r] -= dl; C1[r] -= dl; }
#pragma unroll
          for (int r = 0; r < 16; ++r) st.negm[r] = -st.mhat;
          PIN(st.negm);
          if constexpr (!FIRST) { fct = EX(-dl); st.lsum *= fct; resc = true; }
        }
      }
    }
    if (g <= 21) {
      if constexpr (!FIRST) { const int e = 10 + g; if (e < 16) { P0[e] = EX(P0[e]); PIN(P0); } else { P1[e - 16] = EX(P1[e - 16]); PIN(P1); } }
    } else {
      if constexpr (!LAST) { C0[g - 22] = EX(C0[g - 22]); PIN(C0); }
    }
    SBAR();
  }
  if constexpr (!FIRST && !LAST) {
    if (__builtin_expect(resc, 0)) {
#pragma unroll
      for (int d = 0; d < 4; ++d)
#pragma unroll
        for (int r = 0; r < 16; ++r) st.o[d][r] *= fct;
    }
  }
#undef KRD
#undef VRD
#undef EX
#undef PE
}

__global__ __launch_bounds__(512, 2)
void mha_k1(const char* __restrict__ x, const char* __restrict__ kc, const char* __restrict__ vc,
            const char* __restrict__ wq, const char* __restrict__ wo, char* ows, char* __restrict__ out) {
  extern __shared__ __attribute__((aligned(16))) char lds[];
  const int tid = threadIdx.x, lane = tid & 63;
  const int wid = __builtin_amdgcn_readfirstlane(tid >> 6);
  const int b = 4 * (blockIdx.x & 1) + ((blockIdx.x >> 3) & 3), h = 8 * ((blockIdx.x >> 1) & 3) + (blockIdx.x >> 5);
  const unsigned lds0 = (unsigned)(uintptr_t)lds;
  const lds_ptr L = (lds_ptr)lds;
  RSTAMP(62); STAMP(0);

  const char *pfx, *pfw;
  {
    const int j = blockIdx.x >> 3, half = lane >> 5, l5 = lane & 31;
    const int xrow = (b & 4) * SQ + 28 * j + (l5 < 28 ? l5 : 27);
    const int idx = 32 * j + l5;
    pfw = wq + (size_t)(64 * half + (idx >> 4)) * 8192 + (h & 24) * 256 + (idx & 15) * 128;
    pfx = x + (size_t)xrow * (DM * 2) + half * 128;
  }
  unsigned pfd0;
  const char* pfe = wid >= 4 ? x : (wid & 1) ? pfw + (size_t)(wid >> 1) * (128 * 8192) : pfx + (wid >> 1) * 256;
#define TOUCH(d, p) asm volatile("global_load_dword %0, %1, off " POLT : "=v"(d) : "v"(p) : "memory")
#define KEEP(d) asm volatile("" :: "v"(d) : "memory")
  const __amdgpu_buffer_rsrc_t srdW = __builtin_amdgcn_make_buffer_rsrc((void*)(wq + h * 256), (short)0, DM * 8192 - h * 256, 0x00020000);
  const unsigned pl = ((lane >> 2) & 3), dbl = lane >> 4, c4 = lane & 3;
  const unsigned wv0 = pl * 8192 + dbl * 64 + c4 * 16, wv1 = wv0 + 31744;
#define DMAW2(c) DMA2(POLW, lds0 + WQB(c) * 16384 + 2048 * wid, wv0, wv1, srdW, (unsigned)(((c) * 64 + 8 * wid) * 8192))
  TOUCH(pfd0, pfe);
  const int ob = blockIdx.x & 7, oqt = blockIdx.x >> 6, ont = (blockIdx.x >> 3) & 7;
  const __amdgpu_buffer_rsrc_t srdWo = __builtin_amdgcn_make_buffer_rsrc((void*)(wo + ont * 128), (short)0, NH * DH * DM * 2 - ont * 128, 0x00020000);
  const unsigned ow0 = (4 * (lane >> 5) + ((lane >> 2) & 3)) * 1024 + ((lane >> 4) & 1) * 64 + (lane & 3) * 16;
  const unsigned ow1 = ow0 + 7168, ow2 = ow0 + 2 * 7168, ow3 = ow0 + 3 * 7168;
  unsigned long long* const epochp = (unsigned long long*)(ows + WS_EPOCH);
  unsigned long long* const flags = (unsigned long long*)(ows + WS_FLAGS);
  unsigned long long tag = 0;
  unsigned td = 0;
#define ROTM 0
#define ROTA 0
#define RR(i) (((i) + ROTM * ont + ROTA) & 15)
  constexpr int NA = P3NA;
  const int r32 = lane & 31, hi = lane >> 5, tq = (lane >> 2) & 3, tp = lane & 3, tblk = (lane >> 4) & 1;
  bf16x8 wf[NA][4];
#define RBP(i) ((lds_cptr)L + RB(i) + rbo)
#define WDMA(i) DMA4("", lds0 + (unsigned)(RB(i)) + 4096u * w3, ow0, ow1, ow2, ow3, srdWo, 32768u * w3 + (unsigned)(RR(i) * 256 * 1024))
#define W3() unsigned w3 = (unsigned)__builtin_amdgcn_readfirstlane(tid >> 6); asm volatile("s_nop 4" : "+s"(w3))
#define WREAD_NW(i) do { lds_cptr p_ = RBP(i); PIN(p_); _Pragma("unroll") for (int f_ = 0; f_ < 4; ++f_) \
      wf[(i) % NA][f_] = cat(ldstr(p_ + 2048 * (f_ >> 1) + 256 * (f_ & 1)), ldstr(p_ + 2048 * (f_ >> 1) + 1024 + 256 * (f_ & 1))); } while (0)
#define WRWAIT(i) asm volatile("s_waitcnt lgkmcnt(0)" : "+v"(wf[(i) % NA][0]), "+v"(wf[(i) % NA][1]), "+v"(wf[(i) % NA][2]), "+v"(wf[(i) % NA][3]) :: "memory")
#define WREAD(i) do { WREAD_NW(i); WRWAIT(i); } while (0)
#define RBO() (wid * 4096 + 512 * hi + 64 * tq + 32 * tblk + 8 * tp)
  if (wid == 7) {
    const __amdgpu_buffer_rsrc_t srdK = __builtin_amdgcn_make_buffer_rsrc((void*)(kc + (size_t)(b * NH + h) * (SK * DH * 2)), (short)0, SK * DH * 2, 0x00020000);
    const __amdgpu_buffer_rsrc_t srdV = __builtin_amdgcn_make_buffer_rsrc((void*)(vc + (size_t)(b * NH + h) * (SK * DH * 2)), (short)0, SK * DH * 2, 0x00020000);
    const unsigned wv2 = wv0 + 2 * 31744, wv3 = wv0 + 3 * 31744;
    const unsigned vv = pl * 256 + dbl * 64 + c4 * 16;
    const unsigned kv0 = pl * 256 + dbl * 64 + ((c4 ^ 0) * 16), kv1 = pl * 256 + dbl * 64 + ((c4 ^ 1) * 16),
                   kv2 = pl * 256 + dbl * 64 + ((c4 ^ 2) * 16), kv3 = pl * 256 + dbl * 64 + ((c4 ^ 3) * 16);
#define DMAW(c) do { _Pragma("unroll") for (int i_ = 0; i_ < 4; ++i_) DMA4(POLW, lds0 + (unsigned)(WQB(c) * 16384 + 4096 * i_), wv0, wv1, wv2, wv3, srdW, (unsigned)(((c) * 64 + 16 * i_) * 8192)); } while (0)
#define DMAK(t) do { _Pragma("unroll") for (int i_ = 0; i_ < 4; ++i_) DMA4("nt", lds0 + (unsigned)(((t) & 3) * 32768 + 4096 * i_), kv0, kv1, kv2, kv3, srdK, (unsigned)((t) * 16384 + 4096 * i_)); } while (0)
#define DMAV(t) do { _Pragma("unroll") for (int i_ = 0; i_ < 4; ++i_) DMA4("nt", lds0 + (unsigned)((((t) + 1) & 3) * 32768 + 16384 + 4096 * i_), vv, vv, vv, vv, srdV, (unsigned)((t) * 16384 + 4096 * i_)); } while (0)
    const char* tw = wo + (size_t)(blockIdx.x >> 3) * 8192 + lane * 128;
#define TW(r) do { const char* p_ = tw + (size_t)((((r) + ROTM * (lane & 7) + ROTA) & 15) * 262144); asm volatile("global_load_dword %0, %1, off" : "+v"(td) : "v"(p_) : "memory"); } while (0)
    unsigned t4, t5, t6, t7;
    asm volatile("global_load_dwordx2 %0, %1, off sc1" : "=v"(tag) : "v"(epochp) : "memory");
    STAMP(45); DMAW2(0); STAMP(46);
    BARRIER();
    STAMP(47); DMAW(1); STAMP(48);
    WAITV(16); STAMP(49); STAMP(1); STAMP(2); BARRIER(); STAMP(3);
    DMAW(2); DMAW(3);
    WAITV(32); BARRIER();
    { const char* p4 = pfx + 512; const char* p5 = pfw + (size_t)2 * 128 * 8192; const char* p6 = pfx + 768; const char* p7 = pfw + (size_t)3 * 128 * 8192;
      TOUCH(t4, p4); TOUCH(t5, p5); TOUCH(t6, p6); TOUCH(t7, p7); } STAMP(4);
    WAITV(20); STAMP(5); BARRIER(); STAMP(6);
    STAMP(7);
    WAITV(4); STAMP(8); BARRIER(); STAMP(9);
    DMAW(4); DMAW(5); STAMP(10);
    WAITV(16); STAMP(11); BARRIER(); STAMP(12);
    KEEP(pfd0); KEEP(t4); KEEP(t5); KEEP(t6); KEEP(t7);
    DMAW(6); STAMP(13);
    WAITV(16); STAMP(14); BARRIER(); STAMP(15);
    DMAW(7); DMAK(0); STAMP(16);
    WAITV(32); STAMP(17); BARRIER(); STAMP(18);
    DMAK(1); STAMP(19);
    WAITV(32); STAMP(20); BARRIER(); STAMP(21);
    DMAV(0); STAMP(22);
    STAMP(23); BARRIER(); STAMP(24);
    DMAK(2); STAMP(25);
    WAITV(32); STAMP(26); BARRIER(); STAMP(27);
    DMAV(1); DMAK(3); STAMP(28);
    WAITV(32); STAMP(29); BARRIER(); STAMP(30);
    DMAV(2); DMAK(4); DMAV(3); STAMP(31);
    WAITV(48); STAMP(32); BARRIER(); STAMP(33);
    DMAK(5); DMAV(4); STAMP(34);
    WAITV(48); STAMP(35); BARRIER(); STAMP(36);
    for (int t = 2; t < 12; ++t) {
      DMAK(t + 4); DMAV(t + 3);
      WAITV(48);
      BARRIER();
    }
#define RW(i) do { _Pragma("unroll") for (int w_ = 0; w_ < 8; ++w_) DMA4("", lds0 + (unsigned)(RB(i) + 4096 * w_), ow0, ow1, ow2, ow3, srdWo, (unsigned)((RR(i) * 256 + 32 * w_) * 1024)); } while (0)
    DMAV(15); TW(0); TW(1); TW(2); TW(3); TW(5); TW(6);
    asm volatile("" : "+v"(tag));
    tag = (((unsigned long long)(unsigned)__builtin_amdgcn_readfirstlane((unsigned)(tag >> 32)) << 32) | (unsigned)__builtin_amdgcn_readfirstlane((unsigned)tag)) + 1;
    if (lane == 0) { *(__attribute__((address_space(3))) unsigned*)L = 0u; *(__attribute__((address_space(3))) unsigned long long*)(L + 8) = tag; }
    asm volatile("s_waitcnt lgkmcnt(0)" ::: "memory");
    WAITV(36);
    BARRIER();
    RW(0); RW(1); TW(7); TW(4); TW(8); TW(10); TW(11);
    WAITV(63);
    BARRIER();
    RW(2); TW(12); TW(9); TW(13); TW(14); TW(15);
    WAITV(5);
    BARRIER();
    {
      const int rbo = RBO();
      WREAD(0); WREAD(1); WREAD(2);
    }
    BARRIER();
    RW(3); RW(5); RW(6); RW(7);
    WAITV(0);
    STAMP(50);
#undef TW
#undef RW
#undef DMAW
#undef DMAK
#undef DMAV
  } else {
  const __amdgpu_buffer_rsrc_t srdX = __builtin_amdgcn_make_buffer_rsrc((void*)(x + (size_t)(b * SQ + wid * 32) * (DM * 2)), (short)0, 32 * DM * 2, 0x00020000);
  const unsigned xv = ((lane >> 2) & 7) * 4096 + (lane >> 5) * 64 + (lane & 3) * 16;
  u32x4 xr[8][4];
#define QISSUE(c) do { _Pragma("unroll") for (int g_ = 0; g_ < 4; ++g_) BLOAD(POLW, xr[c][g_], xv, srdX, 0u, 1024 * g_ + 128 * (c)); } while (0)
  STAMP(45);
  DMAW2(0); QISSUE(0);
  STAMP(46);
  BARRIER();
  STAMP(47);
  QISSUE(1);
  STAMP(48);
  const int tpp = ((tp & 1) << 1) | (tp >> 1);
  const lds_cptr wrb = (lds_cptr)L + 8192 * hi + 64 * tq + 32 * tblk + 8 * tpp;
  const bool odd1 = lane & 1, odd2 = lane & 2;

  AttnState st;
  f32x16 acc[4];
#pragma unroll
  for (int i = 0; i < 4; ++i) acc[i] = f32x16{};
  s16x4 wlo[16], whi[16];
#define DPP1(v) (unsigned)__builtin_amdgcn_mov_dpp((int)(v), 0xB1, 0xF, 0xF, true)
#define DPP2(v) (unsigned)__builtin_amdgcn_mov_dpp((int)(v), 0x4E, 0xF, 0xF, true)
#define XST(c, e, p, q, r, t, QP, MS, MC) do { unsigned o0_, o1_, o2_, o3_; \
    asm volatile("s_mov_b64 vcc, %9\n\t" \
                 "v_cndmask_b32_dpp %0, %5, %4, vcc quad_perm:" QP " row_mask:0xf bank_mask:0xf\n\t" \
                 "v_cndmask_b32_dpp %2, %7, %6, vcc quad_perm:" QP " row_mask:0xf bank_mask:0xf\n\t" \
                 "s_mov_b64 vcc, %8\n\t" \
                 "v_cndmask_b32_dpp %1, %4, %5, vcc quad_perm:" QP " row_mask:0xf bank_mask:0xf\n\t" \
                 "v_cndmask_b32_dpp %3, %6, %7, vcc quad_perm:" QP " row_mask:0xf bank_mask:0xf" \
                 : "=&v"(o0_), "=&v"(o1_), "=&v"(o2_), "=&v"(o3_) \
                 : "v"(xr[c][p][e]), "v"(xr[c][q][e]), "v"(xr[c][r][e]), "v"(xr[c][t][e]), "s"(MS), "s"(MC) : "vcc"); \
    xr[c][p][e] = o0_; xr[c][q][e] = o1_; xr[c][r][e] = o2_; xr[c][t][e] = o3_; } while (0)
#define XTRA(c, e) XST(c, e, 0, 1, 2, 3, "[1,0,3,2]", 0xAAAAAAAAAAAAAAAAull, 0x5555555555555555ull)
#define XTRB(c, e) XST(c, e, 0, 2, 1, 3, "[2,3,0,1]", 0xCCCCCCCCCCCCCCCCull, 0x3333333333333333ull)
#define XLD(c, g_) BLOAD(POLW, xr[c][g_], xv, srdX, 0u, 1024 * (g_) + 128 * (c))
  lds_cptr wrb2 = wrb + 65536; PIN(wrb2);
#define WRB(c) ((WQB(c) < 4 ? wrb : wrb2) + (WQB(c) & 3) * 16384)
#define WRD(c, m) do { wlo[m] = ldstr(WRB(c) + 2048 * ((m) >> 2) + 256 * ((m) & 3)); whi[m] = ldstr(WRB(c) + 2048 * ((m) >> 2) + 1024 + 256 * ((m) & 3)); } while (0)
  WAITV4(4, xr[0][0], xr[0][1], xr[0][2], xr[0][3]);
  STAMP(49);
  BARRIER();
#pragma unroll
  for (int m = 0; m < PD; ++m) WRD(0, m);
#pragma unroll
  for (int e = 0; e < 4; ++e) { XTRA(0, e); }
#pragma unroll
  for (int e = 0; e < 4; ++e) { XTRB(0, e); }
  asm volatile("s_nop 1" ::: "memory");
  STAMP(1); STAMP(2);
  static_for<128>([&](auto I) __attribute__((always_inline)) {
    constexpr int c = I.value >> 4, mm = I.value & 15;
    constexpr int m = (c == 7) ? (4 * (mm & 3) + (mm >> 2)) : mm;
    acc[m & 3] = MFMA(cat(wlo[m], whi[m]), __builtin_bit_cast(bf16x8, xr[c][m >> 2]), acc[m & 3]);
    if constexpr (c == 0 && mm + PD == 16) { BARRIER(); }
    if constexpr (mm + PD < 16) { constexpr int f = mm + PD, ff = (c == 7) ? (4 * (f & 3) + (f >> 2)) : f; WRD(c, ff); }
    else if constexpr (c < 7) { constexpr int f = mm + PD - 16, ff = (c + 1 == 7) ? (4 * (f & 3) + (f >> 2)) : f; WRD(c + 1, ff); }
    if constexpr (c == 0 && mm < 8) { XLD(2 + (mm >> 2), mm & 3); }
    if constexpr (c == 2 && mm < 8) { XLD(4 + (mm >> 2), mm & 3); }
    if constexpr (c == 3 && mm < 4) { XLD(6, mm); }
    if constexpr (c == 4 && mm < 4) { XLD(7, mm); }
    if constexpr (c < 7) {
      if constexpr (mm == 8) { WAITV4(4 * (c == 0 ? 2 : c == 1 ? 1 : c <= 4 ? 2 : 6 - c), xr[c + 1][0], xr[c + 1][1], xr[c + 1][2], xr[c + 1][3]); }
      if constexpr (mm == 9 && c == 0) { KEEP(pfd0); }
      if constexpr (mm >= 8 && mm < 12) { XTRA(c + 1, mm - 8); }
      if constexpr (mm >= 12) { XTRB(c + 1, mm - 12); }
    }
    if constexpr (c == 5 && mm < 4) { st.o[mm] = f32x16{}; PIN(st.o[mm]); }
    if constexpr (c == 7 && mm >= 6 && !(mm & 1)) {
      constexpr int f = (mm - 6) >> 1, nb = f >> 1, sq = f & 1;
      u32x4 w;
#pragma unroll
      for (int e = 0; e < 4; ++e) w[e] = cvtpk(acc[nb][8 * sq + 2 * e] * QSCALE, acc[nb][8 * sq + 2 * e + 1] * QSCALE);
      st.q[f] = __builtin_bit_cast(bf16x8, w); PIN(st.q[f]);
    }
    SBAR();
    if constexpr (mm == 15) { BARRIER(); STAMP(3 + c); SBAR(); }
  });
  LSTAMP(58);
#undef XTRA
#undef XTRB
#undef XLD
#undef WRD
#undef QISSUE
#pragma unroll
  for (int m = 0; m < PD; ++m) st.kf[m] = ldsrd128((lds_cptr)L + 1024 * (r32 >> 2) + 64 * (r32 & 3) + 16 * ((2 * (m & 1) + hi) ^ ((r32 >> 2) & 3)) + 256 * (m >> 1));
#pragma unroll
  for (int f = 5; f < 8; ++f) {
    u32x4 w;
#pragma unroll
    for (int e = 0; e < 4; ++e) w[e] = cvtpk(acc[f >> 1][8 * (f & 1) + 2 * e] * QSCALE, acc[f >> 1][8 * (f & 1) + 2 * e + 1] * QSCALE);
    st.q[f] = __builtin_bit_cast(bf16x8, w);
  }
  st.negm = f32x16{}; PIN(st.negm);
  st.mhat = 0.f; st.lsum = 0.f;

  const lds_cptr kb0 = (lds_cptr)L + 1024 * (r32 >> 2) + 64 * (r32 & 3) + 16 * ((0 + hi) ^ ((r32 >> 2) & 3));
  const lds_cptr kb1 = (lds_cptr)L + 1024 * (r32 >> 2) + 64 * (r32 & 3) + 16 * ((2 + hi) ^ ((r32 >> 2) & 3));
  const lds_cptr vb = (lds_cptr)L + 16384 + 1024 * hi + 64 * tq + 32 * tblk + 8 * tp;

  f32x16 A0, A1, B0, B1;
  attn_step<true, false>(st, A0, A1, B0, B1, kb0, kb1, vb, kb0 + 32768, kb1 + 32768, wid >= 4);
  STAMP(11);
  BARRIER();
  STAMP(12);
  for (int t = 1; t < 15; t += 2) {
    const int s0 = (t & 3) * 32768, s1 = ((t + 1) & 3) * 32768, s2 = ((t + 2) & 3) * 32768;
    attn_step<false, false>(st, B0, B1, A0, A1, kb0 + s0, kb1 + s0, vb + s0, kb0 + s1, kb1 + s1, wid >= 4);
    STAMP(11 + 2 * t);
    BARRIER();
    STAMP(12 + 2 * t);
    attn_step<false, false>(st, A0, A1, B0, B1, kb0 + s1, kb1 + s1, vb + s1, kb0 + s2, kb1 + s2, wid >= 4);
    STAMP(13 + 2 * t);
    BARRIER();
    STAMP(14 + 2 * t);
  }
  attn_step<false, false>(st, B0, B1, A0, A1, kb0 + 3 * 32768, kb1 + 3 * 32768, vb + 3 * 32768, kb0, kb1, wid >= 4);
  STAMP(41);
  BARRIER();
  STAMP(42); LSTAMP(59);
  {
#define VRD(m) do { st.vlo[m] = ldstr(vb + 4096 * ((m) & 3) + 256 * ((m) >> 2)); st.vhi[m] = ldstr(vb + 4096 * ((m) & 3) + 2048 + 256 * ((m) >> 2)); } while (0)
    const int rbo = RBO();
    WREAD(0); WREAD(1); WREAD(2);
    BARRIER();
#pragma unroll
    for (int m = 0; m < 8; ++m) VRD(m);
    st.lsum += B0[9];
#pragma unroll
    for (int e = 10; e < 16; ++e) { B0[e] = __builtin_amdgcn_exp2f(B0[e]); st.lsum += B0[e]; }
    float l2 = 0.f;
#pragma unroll
    for (int e = 0; e < 16; ++e) { B1[e] = __builtin_amdgcn_exp2f(B1[e]); l2 += B1[e]; }
    st.lsum += l2;
#pragma unroll
    for (int e = 0; e < 4; ++e) { st.pw[1][e] = cvtpk(B0[8 + 2 * e], B0[9 + 2 * e]); st.pw[2][e] = cvtpk(B1[2 * e], B1[2 * e + 1]); st.pw[3][e] = cvtpk(B1[8 + 2 * e], B1[9 + 2 * e]); }
    auto rr = __builtin_amdgcn_permlane32_swap(__float_as_uint(st.lsum), __float_as_uint(st.lsum), false, false);
    const float inv = 1.0f / (__uint_as_float(rr[0]) + __uint_as_float(rr[1]));
    const __amdgpu_buffer_rsrc_t srdOs = __builtin_amdgcn_make_buffer_rsrc((void*)(ows + (size_t)(b * NH + h) * 57344), (short)0, 57344, 0x00020000);
    const int qrow = 32 * wid + r32, vblk = qrow / 28;
    const unsigned osv = vblk * 7168 + (hi * 28 + qrow - 28 * vblk) * 16;
#define OSTORE(db) do { _Pragma("unroll") for (int s_ = 0; s_ < 2; ++s_) { u32x4 w_; \
      _Pragma("unroll") for (int e_ = 0; e_ < 4; ++e_) w_[e_] = cvtpk(st.o[db][8 * s_ + 2 * e_] * inv, st.o[db][8 * s_ + 2 * e_ + 1] * inv); \
      __builtin_amdgcn_raw_buffer_store_b128(w_, srdOs, osv + (2 * (db) + s_) * 896, 0, 16); } } while (0)
    SBAR();
#pragma unroll
    for (int db = 0; db < 4; ++db) {
#pragma unroll
      for (int ks = 0; ks < 4; ++ks) {
        st.o[db] = MFMA(cat(st.vlo[4 * db + ks], st.vhi[4 * db + ks]), __builtin_bit_cast(bf16x8, st.pw[ks]), st.o[db]);
        if (db < 2) { VRD(4 * db + ks + 8); }
      }
      SBAR();
      if (db >= 1) { OSTORE(db - 1); }
      SBAR();
    }
    STAMP(43);
    OSTORE(3);
    STAMP(44);
#undef VRD
#undef OSTORE
    WAITV(0);
    if (lane == 0) {
      const unsigned old = __hip_atomic_fetch_add((__attribute__((address_space(3))) unsigned*)L, 1u, __ATOMIC_RELAXED, __HIP_MEMORY_SCOPE_WORKGROUP);
      if (old == 6) __hip_atomic_store(flags + b * NH + h, *(__attribute__((address_space(3))) unsigned long long*)(L + 8), __ATOMIC_RELAXED, __HIP_MEMORY_SCOPE_AGENT);
    }
    asm volatile("s_waitcnt lgkmcnt(0)" ::: "memory");
    STAMP(50);
  }
  }

  {
    static_assert(NA == 8, "prefetch sequences above are written for NA = 8");
    const int rbo = RBO(); W3();
    const __amdgpu_buffer_rsrc_t srdO = __builtin_amdgcn_make_buffer_rsrc((void*)(ows + (size_t)ob * (NH * 57344)), (short)0, NH * 57344, 0x00020000);
    const unsigned ov0 = (hi * 28 + (r32 < 28 ? r32 : 27)) * 16;
    const unsigned osb = (w3 >> 2) * 57344 + (w3 & 3) * 1792 + oqt * 2 * 7168;
    u32x4 of[3][4];
    f32x16 acc[2][2];
#pragma unroll
    for (int i = 0; i < 2; ++i) { acc[i][0] = f32x16{}; acc[i][1] = f32x16{}; }
#define OLOAD(i) do { const unsigned so_ = osb + (unsigned)(RR(i) * 2 * 57344), so1_ = so_ + 7168u; \
      BLOAD("sc1", of[(i) % 3][0], ov0, srdO, so_, 0); BLOAD("sc1", of[(i) % 3][1], ov0, srdO, so1_, 0); \
      BLOAD("sc1", of[(i) % 3][2], ov0, srdO, so_, 896); BLOAD("sc1", of[(i) % 3][3], ov0, srdO, so1_, 896); } while (0)
    BARRIER();
    LSTAMP(57);
    STAMP(51);
    WDMA(4);
    WREAD(3); WDMA(8); WREAD(5); WDMA(10); WREAD(6); WDMA(11); WREAD(7); WDMA(12);
    WAITV(16); WREAD(4); WDMA(9);
    STAMP(52);
    if (w3 == 7) {
      const unsigned long long* fp = flags + ob * NH + r32;
      for (unsigned spins = 0; spins < (1u << 18); ++spins) {
        const unsigned long long v = __hip_atomic_load(fp, __ATOMIC_RELAXED, __HIP_MEMORY_SCOPE_AGENT);
        if (__all(v == tag)) break;
        __builtin_amdgcn_s_sleep(1);
      }
      KEEP(td);
    }
    STAMP(53);
    BARRIER();
    LSTAMP(60);
    STAMP(54);
    OLOAD(0); OLOAD(1);
#pragma unroll
    for (int i = 0; i < 16; ++i) {
      const int cnt = (i < 15 ? 4 : 0) + ((i - 1 >= 1 && i - 1 <= 3) ? 4 : 0);
      WAITV4(cnt, of[i % 3][0], of[i % 3][1], of[i % 3][2], of[i % 3][3]);
      if (i >= 1 && i <= 3) { WRWAIT(i - 1 + NA); WDMA(i + 12); }
      if (i + 2 < 16) { OLOAD(i + 2); }
      SBAR();
#pragma unroll
      for (int ks = 0; ks < 2; ++ks)
#pragma unroll
        for (int nb = 0; nb < 2; ++nb)
#pragma unroll
          for (int vb = 0; vb < 2; ++vb)
            acc[nb][vb] = MFMA(wf[i % NA][2 * ks + nb], __builtin_bit_cast(bf16x8, of[i % 3][2 * ks + vb]), acc[nb][vb]);
      SBAR();
      if (i + NA < 16) { WREAD_NW(i + NA); }
      SBAR();
    }
    STAMP(55);
#undef WDMA
#undef WREAD
#undef OLOAD
#undef RBP
#pragma unroll
    for (int vb = 0; vb < 2; ++vb)
#pragma unroll
      for (int nb = 0; nb < 2; ++nb)
#pragma unroll
        for (int rg = 0; rg < 4; ++rg) {
          const int q = 32 * vb + r32, slot = 8 * nb + 2 * rg + hi;
          f32x4 v = {acc[nb][vb][4 * rg], acc[nb][vb][4 * rg + 1], acc[nb][vb][4 * rg + 2], acc[nb][vb][4 * rg + 3]};
          *(__attribute__((address_space(3))) f32x4*)(L + (q >> 4) * 32768 + wid * 4096 + (q & 15) * 256 + ((slot ^ (q & 15)) * 16)) = v;
        }
    asm volatile("s_waitcnt lgkmcnt(0)" ::: "memory");
    BARRIER();
    STAMP(56); LSTAMP(61);
    const int q = tid >> 3, pc = tid & 7;
    f32x4 s0 = {0, 0, 0, 0}, s1 = {0, 0, 0, 0};
#pragma unroll
    for (int w = 0; w < 8; ++w) {
      s0 += *(const __attribute__((address_space(3))) f32x4*)(L + (q >> 4) * 32768 + w * 4096 + (q & 15) * 256 + (((2 * pc) ^ (q & 15)) * 16));
      s1 += *(const __attribute__((address_space(3))) f32x4*)(L + (q >> 4) * 32768 + w * 4096 + (q & 15) * 256 + (((2 * pc + 1) ^ (q & 15)) * 16));
    }
    u32x4 w = {cvtpk(s0[0], s0[1]), cvtpk(s0[2], s0[3]), cvtpk(s1[0], s1[1]), cvtpk(s1[2], s1[3])};
    if ((q & 31) < 28)
      *(u32x4*)(out + (size_t)(ob * SQ + 56 * oqt + 28 * (q >> 5) + (q & 31)) * (DM * 2) + ont * 128 + pc * 16) = w;
    if (blockIdx.x == 0 && wid == 7) {
      for (unsigned spins = 0; spins < (1u << 18); ++spins) {
        bool ok = true;
#pragma unroll
        for (int k = 0; k < 4; ++k) ok &= __hip_atomic_load(flags + 64 * k + lane, __ATOMIC_RELAXED, __HIP_MEMORY_SCOPE_AGENT) == tag;
        if (__all(ok)) break;
        __builtin_amdgcn_s_sleep(2);
      }
      if (lane == 0) __hip_atomic_store(epochp, tag, __ATOMIC_RELAXED, __HIP_MEMORY_SCOPE_AGENT);
    }
  }
  STAMP(40); RSTAMP(63);
  STFLUSH(ows + (64 << 20));
}

extern "C" void kernel_launch(void* const* d_in, const int* in_sizes, int n_in,
                              void* d_out, int out_size, void* d_ws, size_t ws_size,
                              hipStream_t stream) {
  (void)in_sizes; (void)n_in; (void)out_size; (void)ws_size;
  (void)hipFuncSetAttribute((const void*)mha_k1, hipFuncAttributeMaxDynamicSharedMemorySize, K1_LDS);
  mha_k1<<<256, 512, K1_LDS, stream>>>((const char*)d_in[0], (const char*)d_in[1], (const char*)d_in[2], (const char*)d_in[3], (const char*)d_in[4], (char*)d_ws, (char*)d_out);
}
```

```cpp
#include <hip/hip_runtime.h>
#include <hip/hip_bf16.h>
#include <stdint.h>
#include <utility>
#include <type_traits>

typedef short    bf16x8 __attribute__((ext_vector_type(8)));
typedef short    s16x4  __attribute__((ext_vector_type(4)));
typedef float    f32x16 __attribute__((ext_vector_type(16)));
typedef float    f32x4  __attribute__((ext_vector_type(4)));
typedef float    f32x2  __attribute__((ext_vector_type(2)));
typedef __bf16   bf16x2 __attribute__((ext_vector_type(2)));
typedef unsigned u32x4  __attribute__((ext_vector_type(4)));
typedef __attribute__((address_space(3))) char* lds_ptr;
typedef __attribute__((address_space(3))) const char* lds_cptr;

#define DI __device__ __forceinline__
#define SBAR() __builtin_amdgcn_sched_barrier(0)
#define PIN(x) asm volatile("" : "+v"(x))
#define MFMA(a, b, c) __builtin_amdgcn_mfma_f32_32x32x16_bf16(a, b, c, 0, 0, 0)
#define BARRIER() asm volatile("s_barrier" ::: "memory")

#define STAMP(i)
#define RSTAMP(i)
#define LSTAMP(i)
#define STFLUSH(dbg)
#define POLT "nt"
#define POLW ""
#define POLWO ""
#define POLT2 ""
#define POLO "sc1"
#define P3NA 8
#define TSCHED 3
#define PRIO_TOGGLE 16
#define EARLYKV 0
#define MAPK1 1
constexpr int NB = 8, SQ = 224, DM = 512, NH = 32, SK = 1024, DH = 128;
constexpr float QSCALE = 0.088388347648318440f * 1.4426950408889634f;
constexpr float THR = 8.0f;

template <int... Is, class F> DI void static_for_(std::integer_sequence<int, Is...>, F&& f) { (f(std::integral_constant<int, Is>{}), ...); }
template <int N, class F> DI void static_for(F&& f) { static_for_(std::make_integer_sequence<int, N>{}, f); }
DI unsigned cvtpk(float lo, float hi) { f32x2 v = {lo, hi}; bf16x2 b = __builtin_convertvector(v, bf16x2); return __builtin_bit_cast(unsigned, b); }
DI bf16x8 ldsrd128(lds_cptr p) { return *(const __attribute__((address_space(3))) bf16x8*)p; }
DI s16x4 ldstr(lds_cptr p) { return __builtin_bit_cast(s16x4, __builtin_amdgcn_ds_read_tr16_b64_v4i16((__attribute__((address_space(3))) s16x4*)p)); }
DI bf16x8 cat(s16x4 a, s16x4 b) { return __builtin_shufflevector(a, b, 0, 1, 2, 3, 4, 5, 6, 7); }
DI void ldswr128(lds_ptr p, u32x4 v) { *(__attribute__((address_space(3))) u32x4*)p = v; }
#define MX3(a, b, c) __builtin_fmaxf(__builtin_fmaxf((a), (b)), (c))

#define DMA4(POL, m0v, v0, v1, v2, v3, srd, soff)                                                     \
  asm volatile("s_mov_b32 m0, %0\n\ts_nop 0\n\t"                                                      \
               "buffer_load_dwordx4 %1, %5, %6 offen " POL " lds\n\t"                                 \
               "buffer_load_dwordx4 %2, %5, %6 offen offset:1024 " POL " lds\n\t"                     \
               "buffer_load_dwordx4 %3, %5, %6 offen offset:2048 " POL " lds\n\t"                     \
               "buffer_load_dwordx4 %4, %5, %6 offen offset:3072 " POL " lds"                         \
               :: "s"(m0v), "v"(v0), "v"(v1), "v"(v2), "v"(v3), "s"(srd), "s"(soff) : "memory")
#define DMA2(POL, m0v, v0, v1, srd, soff)                                                             \
  asm volatile("s_mov_b32 m0, %0\n\ts_nop 0\n\t"                                                      \
               "buffer_load_dwordx4 %1, %3, %4 offen " POL " lds\n\t"                                 \
               "buffer_load_dwordx4 %2, %3, %4 offen offset:1024 " POL " lds"                         \
               :: "s"(m0v), "v"(v0), "v"(v1), "s"(srd), "s"(soff) : "memory")
#define BLOAD(POL, dst, voff, srd, soff, imm)                                                         \
  asm volatile("buffer_load_dwordx4 %0, %1, %2, %3 offen offset:%4 " POL                              \
               : "=v"(dst) : "v"(voff), "s"(srd), "s"(soff), "i"(imm) : "memory")
#define WAITV(n) asm volatile("s_waitcnt vmcnt(%0)" :: "i"(n) : "memory")
#define WAITV4(n, a, b, c, d) asm volatile("s_waitcnt vmcnt(%4)" : "+v"(a), "+v"(b), "+v"(c), "+v"(d) : "i"(n) : "memory")

constexpr int K1_LDS = 163840;
constexpr size_t WS_FLAGS = (size_t)32 << 20, WS_EPOCH = WS_FLAGS + 4096;
DI constexpr int RB(int i) { return (i % 5) * 32768; }
DI constexpr int SLOT(int t) { return ((t + 3) % 5) * 32768; }
constexpr int PUBW = 131072;
DI constexpr int WQB(int c) { return c == 0 ? 6 : c == 7 ? 7 : c - 1; }

struct AttnState {
  f32x16 o[4];
  bf16x8 q[8];
  f32x16 negm;
  float mhat, lsum;
  bf16x8 kf[16];
  s16x4 vlo[16], vhi[16];
  u32x4 pw[4];
};

#define PDV 5
constexpr int PD = PDV;
template <bool FIRST, bool LAST>
DI void attn_step(AttnState& st, f32x16& C0, f32x16& C1, f32x16& P0, f32x16& P1,
                  lds_cptr kp0, lds_cptr kp1, lds_cptr vp, lds_cptr kn0, lds_cptr kn1, const bool young) {
#define KRD(m, b0, b1) st.kf[m] = ldsrd128((((m) & 1) ? (b1) : (b0)) + 8192 * ((m) >> 3) + 256 * (((m) & 7) >> 1))
#define VRD(m) do { st.vlo[m] = ldstr(vp + 4096 * ((m) >> 2) + 256 * ((m) & 3)); st.vhi[m] = ldstr(vp + 4096 * ((m) >> 2) + 2048 + 256 * ((m) & 3)); } while (0)
#define EX(v) __builtin_amdgcn_exp2f(v)
#define PE(e) (((e) < 16) ? P0[(e) & 15] : P1[(e) & 15])
  float ma = 0.f, mb = 0.f, fct = 1.f;
  bool resc = false;
  SBAR();
#pragma unroll
  for (int g = 0; g < 32; ++g) {
    if (g == 0) { if (young) __builtin_amdgcn_s_setprio(1); }
    if (g == PRIO_TOGGLE) { __builtin_amdgcn_s_setprio(0); }
    if (g < 16) {
      if constexpr (!LAST) {
        if (g == 0) C0 = MFMA(st.kf[0], st.q[0], st.negm);
        else if (g < 8) C0 = MFMA(st.kf[g], st.q[g], C0);
        else if (g == 8) C1 = MFMA(st.kf[8], st.q[0], st.negm);
        else C1 = MFMA(st.kf[g], st.q[g - 8], C1);
        if (g < 8) PIN(C0); else PIN(C1);
      }
    } else {
      if constexpr (!FIRST) {
        const int m = g - 16;
        st.o[m & 3] = MFMA(cat(st.vlo[m], st.vhi[m]), __builtin_bit_cast(bf16x8, st.pw[m >> 2]), st.o[m & 3]);
      }
    }
    {
      const int f = g + PD;
      if (f < 16) { if constexpr (!LAST) { KRD(f, kp0, kp1); } }
      else if (f < 32) { if constexpr (!FIRST) { VRD(f - 16); } }
      else { if constexpr (!LAST) { KRD(f - 32, kn0, kn1); } }
    }
    if (g <= 22) {
      if constexpr (!FIRST) {
        st.lsum += PE(9 + g); PIN(st.lsum);
        if ((g & 1) == 0) { const int e = 8 + g; st.pw[e >> 3][(e >> 1) & 3] = cvtpk(PE(e), PE(e + 1)); PIN(st.pw[e >> 3]); }
      }
    } else {
      if constexpr (!LAST) {
        st.lsum += C0[g - 23]; PIN(st.lsum);
        if ((g & 1) == 0) { const int e = g - 24; st.pw[0][e >> 1] = cvtpk(C0[e], C0[e + 1]); PIN(st.pw[0]); }
      }
    }
    if constexpr (!LAST) {
      if (g == 9)  { ma = MX3(C0[0], C0[1], C0[2]); mb = MX3(C0[3], C0[4], C0[5]); PIN(ma); PIN(mb); }
      if (g == 11) { ma = MX3(ma, C0[6], C0[7]); mb = MX3(mb, C0[8], C0[9]); PIN(ma); PIN(mb); }
      if (g == 13) { ma = MX3(ma, C0[10], C0[11]); mb = MX3(mb, C0[12], C0[13]); PIN(ma); PIN(mb); }
      if (g == 15) { ma = MX3(ma, C0[14], C0[15]); PIN(ma); }
      if (g == 17) { ma = MX3(ma, C1[0], C1[1]); mb = MX3(mb, C1[2], C1[3]); PIN(ma); PIN(mb); }
      if (g == 18) { ma = MX3(ma, C1[4], C1[5]); PIN(ma); }
      if (g == 19) { ma = MX3(ma, C1[6], C1[7]); mb = MX3(mb, C1[8], C1[9]); PIN(ma); PIN(mb); }
      if (g == 20) { mb = MX3(mb, C1[10], C1[11]); PIN(mb); }
      if (g == 21) { ma = MX3(ma, C1[12], C1[13]); mb = MX3(mb, C1[14], C1[15]); PIN(ma); PIN(mb); }
      if (g == 22) {
        if (FIRST || __builtin_expect(__any((ma > THR) | (mb > THR)), 0)) {
          ma = __builtin_fmaxf(ma, mb);
          auto rr = __builtin_amdgcn_permlane32_swap(__float_as_uint(ma), __float_as_uint(ma), false, false);
          float rm = __builtin_fmaxf(__uint_as_float(rr[0]), __uint_as_float(rr[1]));
          const float dl = FIRST ? rm : __builtin_fmaxf(rm, 0.f);
          st.mhat += dl;
#pragma unroll
          for (int r = 0; r < 16; ++r) { C0[r] -= dl; C1[r] -= dl; }
#pragma unroll
          for (int r = 0; r < 16; ++r) st.negm[r] = -st.mhat;
          PIN(st.negm);
          if constexpr (!FIRST) { fct = EX(-dl); st.lsum *= fct; resc = true; }
        }
      }
    }
    if (g <= 21) {
      if constexpr (!FIRST) { const int e = 10 + g; if (e < 16) { P0[e] = EX(P0[e]); PIN(P0); } else { P1[e - 16] = EX(P1[e - 16]); PIN(P1); } }
    } else {
      if constexpr (!LAST) { C0[g - 22] = EX(C0[g - 22]); PIN(C0); }
    }
    SBAR();
  }
  if constexpr (!FIRST && !LAST) {
    if (__builtin_expect(resc, 0)) {
#pragma unroll
      for (int d = 0; d < 4; ++d)
#pragma unroll
        for (int r = 0; r < 16; ++r) st.o[d][r] *= fct;
    }
  }
#undef KRD
#undef VRD
#undef EX
#undef PE
}

__global__ __launch_bounds__(512, 2)
void mha_k1(const char* __restrict__ x, const char* __restrict__ kc, const char* __restrict__ vc,
            const char* __restrict__ wq, const char* __restrict__ wo, char* ows, char* __restrict__ out) {
  extern __shared__ __attribute__((aligned(16))) char lds[];
  const int tid = threadIdx.x, lane = tid & 63;
  const int wid = __builtin_amdgcn_readfirstlane(tid >> 6);
  const int b = 4 * (blockIdx.x & 1) + ((blockIdx.x >> 3) & 3), h = 8 * ((blockIdx.x >> 1) & 3) + (blockIdx.x >> 5);
  const unsigned lds0 = (unsigned)(uintptr_t)lds;
  const lds_ptr L = (lds_ptr)lds;
  RSTAMP(62); STAMP(0);

  const char *pfx, *pfw;
  {
    const int j = blockIdx.x >> 3, half = lane >> 5, l5 = lane & 31;
    const int xrow = (b & 4) * SQ + 28 * j + (l5 < 28 ? l5 : 27);
    const int idx = 32 * j + l5;
    pfw = wq + (size_t)(64 * half + (idx >> 4)) * 8192 + (h & 24) * 256 + (idx & 15) * 128;
    pfx = x + (size_t)xrow * (DM * 2) + half * 128;
  }
  unsigned pfd0;
  const char* pfe = wid >= 4 ? x : (wid & 1) ? pfw + (size_t)(wid >> 1) * (128 * 8192) : pfx + (wid >> 1) * 256;
#define TOUCH(d, p) asm volatile("global_load_dword %0, %1, off " POLT : "=v"(d) : "v"(p) : "memory")
#define KEEP(d) asm volatile("" :: "v"(d) : "memory")
  const __amdgpu_buffer_rsrc_t srdW = __builtin_amdgcn_make_buffer_rsrc((void*)(wq + h * 256), (short)0, DM * 8192 - h * 256, 0x00020000);
  const unsigned pl = ((lane >> 2) & 3), dbl = lane >> 4, c4 = lane & 3;
  const unsigned wv0 = pl * 8192 + dbl * 64 + c4 * 16, wv1 = wv0 + 31744;
#define DMAW2(c) DMA2(POLW, lds0 + WQB(c) * 16384 + 2048 * wid, wv0, wv1, srdW, (unsigned)(((c) * 64 + 8 * wid) * 8192))
  TOUCH(pfd0, pfe);
  const int ob = blockIdx.x & 7, oqt = blockIdx.x >> 6, ont = (blockIdx.x >> 3) & 7;
  const __amdgpu_buffer_rsrc_t srdWo = __builtin_amdgcn_make_buffer_rsrc((void*)(wo + ont * 128), (short)0, NH * DH * DM * 2 - ont * 128, 0x00020000);
  const unsigned ow0 = (4 * (lane >> 5) + ((lane >> 2) & 3)) * 1024 + ((lane >> 4) & 1) * 64 + (lane & 3) * 16;
  const unsigned ow1 = ow0 + 7168, ow2 = ow0 + 2 * 7168, ow3 = ow0 + 3 * 7168;
  unsigned long long* const epochp = (unsigned long long*)(ows + WS_EPOCH);
  unsigned long long* const flags = (unsigned long long*)(ows + WS_FLAGS);
  unsigned long long tag = 0;
  unsigned td = 0;
#define ROTM 0
#define ROTA 0
#define RR(i) (((i) + ROTM * ont + ROTA) & 15)
  constexpr int NA = P3NA;
  const int r32 = lane & 31, hi = lane >> 5, tq = (lane >> 2) & 3, tp = lane & 3, tblk = (lane >> 4) & 1;
  bf16x8 wf[NA][4];
#define RBP(i) ((lds_cptr)L + RB(i) + rbo)
#define WDMA(i) DMA4("", lds0 + (unsigned)(RB(i)) + 4096u * w3, ow0, ow1, ow2, ow3, srdWo, 32768u * w3 + (unsigned)(RR(i) * 256 * 1024))
#define W3() unsigned w3 = (unsigned)__builtin_amdgcn_readfirstlane(tid >> 6); asm volatile("s_nop 4" : "+s"(w3))
#define WREAD_NW(i) do { lds_cptr p_ = RBP(i); PIN(p_); _Pragma("unroll") for (int f_ = 0; f_ < 4; ++f_) \
      wf[(i) % NA][f_] = cat(ldstr(p_ + 2048 * (f_ >> 1) + 256 * (f_ & 1)), ldstr(p_ + 2048 * (f_ >> 1) + 1024 + 256 * (f_ & 1))); } while (0)
#define WRWAIT(i) asm volatile("s_waitcnt lgkmcnt(0)" : "+v"(wf[(i) % NA][0]), "+v"(wf[(i) % NA][1]), "+v"(wf[(i) % NA][2]), "+v"(wf[(i) % NA][3]) :: "memory")
#define WREAD(i) do { WREAD_NW(i); WRWAIT(i); } while (0)
#define RBO() (wid * 4096 + 512 * hi + 64 * tq + 32 * tblk + 8 * tp)
  if (wid == 7) {
    const __amdgpu_buffer_rsrc_t srdK = __builtin_amdgcn_make_buffer_rsrc((void*)(kc + (size_t)(b * NH + h) * (SK * DH * 2)), (short)0, SK * DH * 2, 0x00020000);
    const __amdgpu_buffer_rsrc_t srdV = __builtin_amdgcn_make_buffer_rsrc((void*)(vc + (size_t)(b * NH + h) * (SK * DH * 2)), (short)0, SK * DH * 2, 0x00020000);
    const unsigned wv2 = wv0 + 2 * 31744, wv3 = wv0 + 3 * 31744;
    const unsigned vv = pl * 256 + dbl * 64 + c4 * 16;
    const unsigned kv0 = pl * 256 + dbl * 64 + ((c4 ^ 0) * 16), kv1 = pl * 256 + dbl * 64 + ((c4 ^ 1) * 16),
                   kv2 = pl * 256 + dbl * 64 + ((c4 ^ 2) * 16), kv3 = pl * 256 + dbl * 64 + ((c4 ^ 3) * 16);
#define DMAW(c) do { _Pragma("unroll") for (int i_ = 0; i_ < 4; ++i_) DMA4(POLW, lds0 + (unsigned)(WQB(c) * 16384 + 4096 * i_), wv0, wv1, wv2, wv3, srdW, (unsigned)(((c) * 64 + 16 * i_) * 8192)); } while (0)
#define DMAK_(t, so) do { _Pragma("unroll") for (int i_ = 0; i_ < 4; ++i_) DMA4("nt", lds0 + (unsigned)((so) + 4096 * i_), kv0, kv1, kv2, kv3, srdK, (unsigned)((t) * 16384 + 4096 * i_)); } while (0)
#define DMAV_(t, so) do { _Pragma("unroll") for (int i_ = 0; i_ < 4; ++i_) DMA4("nt", lds0 + (unsigned)((so) + 16384 + 4096 * i_), vv, vv, vv, vv, srdV, (unsigned)((t) * 16384 + 4096 * i_)); } while (0)
#define DMAK(t) DMAK_(t, SLOT(t))
#define DMAV(t) DMAV_(t, SLOT((t) + 1))
    const char* tw = wo + (size_t)(blockIdx.x >> 3) * 8192 + lane * 128;
#define TW(r) do { const char* p_ = tw + (size_t)((((r) + ROTM * (lane & 7) + ROTA) & 15) * 262144); asm volatile("global_load_dword %0, %1, off" : "+v"(td) : "v"(p_) : "memory"); } while (0)
    unsigned t4, t5, t6, t7;
    asm volatile("global_load_dwordx2 %0, %1, off sc1" : "=v"(tag) : "v"(epochp) : "memory");
#define RW(i) do { _Pragma("unroll") for (int w_ = 0; w_ < 8; ++w_) DMA4("", lds0 + (unsigned)(RB(i) + 4096 * w_), ow0, ow1, ow2, ow3, srdWo, (unsigned)((RR(i) * 256 + 32 * w_) * 1024)); } while (0)
    DMAW2(0);
    BARRIER();
    DMAW(1);
    WAITV(16); BARRIER();
    DMAW(2); DMAW(3);
    WAITV(32); BARRIER();
    { const char* p4 = pfx + 512; const char* p5 = pfw + (size_t)2 * 128 * 8192; const char* p6 = pfx + 768; const char* p7 = pfw + (size_t)3 * 128 * 8192;
      TOUCH(t4, p4); TOUCH(t5, p5); TOUCH(t6, p6); TOUCH(t7, p7); }
    WAITV(20); BARRIER();
    WAITV(4); BARRIER();
    DMAW(4); DMAW(5);
    WAITV(16); BARRIER();
    KEEP(pfd0); KEEP(t4); KEEP(t5); KEEP(t6); KEEP(t7);
    DMAW(6);
    WAITV(16); BARRIER();
    DMAW(7); DMAK(0);
    WAITV(32); BARRIER();
    DMAK(1); DMAV(0);
    WAITV(48); BARRIER();
    DMAK(2);
    BARRIER();
    DMAV(1);
    WAITV(48); BARRIER();
    DMAK(3); DMAV(2); DMAK(4);
    WAITV(63); BARRIER();
    DMAV(3); DMAV(4);
    DMAK(5);
    WAITV(63); BARRIER();
    {
      int so = SLOT(1);
      for (int t = 1; t < 11; ++t) {
        DMAK_(t + 5, so); DMAV_(t + 4, so);
        so = (so == 131072) ? 0 : so + 32768;
        WAITV(63);
        BARRIER();
      }
    }
    DMAV(15); TW(0); TW(1); TW(2); TW(3);
    asm volatile("" : "+v"(tag));
    tag = (((unsigned long long)(unsigned)__builtin_amdgcn_readfirstlane((unsigned)(tag >> 32)) << 32) | (unsigned)__builtin_amdgcn_readfirstlane((unsigned)tag)) + 1;
    if (lane == 0) { *(__attribute__((address_space(3))) unsigned*)(L + PUBW) = 0u; *(__attribute__((address_space(3))) unsigned long long*)(L + PUBW + 8) = tag; }
    asm volatile("s_waitcnt lgkmcnt(0)" ::: "memory");
    WAITV(63);
    BARRIER();
    TW(5); TW(6); TW(7); TW(4);
    WAITV(40);
    RW(0);
    BARRIER();
    RW(1); TW(8); TW(10); TW(11); TW(12);
    WAITV(63);
    BARRIER();
    RW(2); TW(9); TW(13); TW(14); TW(15);
    WAITV(4);
    BARRIER();
    {
      const int rbo = RBO();
      WREAD(0); WREAD(1); WREAD(2);
    }
    BARRIER();
    RW(3); RW(5); RW(6); RW(7);
    WAITV(0);
#undef TW
#undef RW
#undef DMAW
#undef DMAK
#undef DMAV
  } else {
  const __amdgpu_buffer_rsrc_t srdX = __builtin_amdgcn_make_buffer_rsrc((void*)(x + (size_t)(b * SQ + wid * 32) * (DM * 2)), (short)0, 32 * DM * 2, 0x00020000);
  const unsigned xv = ((lane >> 2) & 7) * 4096 + (lane >> 5) * 64 + (lane & 3) * 16;
  u32x4 xr[8][4];
#define QISSUE(c) do { _Pragma("unroll") for (int g_ = 0; g_ < 4; ++g_) BLOAD(POLW, xr[c][g_], xv, srdX, 0u, 1024 * g_ + 128 * (c)); } while (0)
  STAMP(45);
  DMAW2(0); QISSUE(0);
  STAMP(46);
  BARRIER();
  STAMP(47);
  QISSUE(1);
  STAMP(48);
  const int tpp = ((tp & 1) << 1) | (tp >> 1);
  const lds_cptr wrb = (lds_cptr)L + 8192 * hi + 64 * tq + 32 * tblk + 8 * tpp;
  const bool odd1 = lane & 1, odd2 = lane & 2;

  AttnState st;
  f32x16 acc[4];
#pragma unroll
  for (int i = 0; i < 4; ++i) acc[i] = f32x16{};
  s16x4 wlo[16], whi[16];
#define DPP1(v) (unsigned)__builtin_amdgcn_mov_dpp((int)(v), 0xB1, 0xF, 0xF, true)
#define DPP2(v) (unsigned)__builtin_amdgcn_mov_dpp((int)(v), 0x4E, 0xF, 0xF, true)
#define XST(c, e, p, q, r, t, QP, MS, MC) do { unsigned o0_, o1_, o2_, o3_; \
    asm volatile("s_mov_b64 vcc, %9\n\t" \
                 "v_cndmask_b32_dpp %0, %5, %4, vcc quad_perm:" QP " row_mask:0xf bank_mask:0xf\n\t" \
                 "v_cndmask_b32_dpp %2, %7, %6, vcc quad_perm:" QP " row_mask:0xf bank_mask:0xf\n\t" \
                 "s_mov_b64 vcc, %8\n\t" \
                 "v_cndmask_b32_dpp %1, %4, %5, vcc quad_perm:" QP " row_mask:0xf bank_mask:0xf\n\t" \
                 "v_cndmask_b32_dpp %3, %6, %7, vcc quad_perm:" QP " row_mask:0xf bank_mask:0xf" \
                 : "=&v"(o0_), "=&v"(o1_), "=&v"(o2_), "=&v"(o3_) \
                 : "v"(xr[c][p][e]), "v"(xr[c][q][e]), "v"(xr[c][r][e]), "v"(xr[c][t][e]), "s"(MS), "s"(MC) : "vcc"); \
    xr[c][p][e] = o0_; xr[c][q][e] = o1_; xr[c][r][e] = o2_; xr[c][t][e] = o3_; } while (0)
#define XTRA(c, e) XST(c, e, 0, 1, 2, 3, "[1,0,3,2]", 0xAAAAAAAAAAAAAAAAull, 0x5555555555555555ull)
#define XTRB(c, e) XST(c, e, 0, 2, 1, 3, "[2,3,0,1]", 0xCCCCCCCCCCCCCCCCull, 0x3333333333333333ull)
#define XLD(c, g_) BLOAD(POLW, xr[c][g_], xv, srdX, 0u, 1024 * (g_) + 128 * (c))
  lds_cptr wrb2 = wrb + 65536; PIN(wrb2);
#define WRB(c) ((WQB(c) < 4 ? wrb : wrb2) + (WQB(c) & 3) * 16384)
#define WRD(c, m) do { wlo[m] = ldstr(WRB(c) + 2048 * ((m) >> 2) + 256 * ((m) & 3)); whi[m] = ldstr(WRB(c) + 2048 * ((m) >> 2) + 1024 + 256 * ((m) & 3)); } while (0)
  WAITV4(4, xr[0][0], xr[0][1], xr[0][2], xr[0][3]);
  STAMP(49);
  BARRIER();
#pragma unroll
  for (int m = 0; m < PD; ++m) WRD(0, m);
#pragma unroll
  for (int e = 0; e < 4; ++e) { XTRA(0, e); }
#pragma unroll
  for (int e = 0; e < 4; ++e) { XTRB(0, e); }
  asm volatile("s_nop 1" ::: "memory");
  STAMP(1); STAMP(2);
  static_for<128>([&](auto I) __attribute__((always_inline)) {
    constexpr int c = I.value >> 4, mm = I.value & 15;
    constexpr int m = (c == 7) ? (4 * (mm & 3) + (mm >> 2)) : mm;
    acc[m & 3] = MFMA(cat(wlo[m], whi[m]), __builtin_bit_cast(bf16x8, xr[c][m >> 2]), acc[m & 3]);
    if constexpr (c == 0 && mm + PD == 16) { BARRIER(); }
    if constexpr (mm + PD < 16) { constexpr int f = mm + PD, ff = (c == 7) ? (4 * (f & 3) + (f >> 2)) : f; WRD(c, ff); }
    else if constexpr (c < 7) { constexpr int f = mm + PD - 16, ff = (c + 1 == 7) ? (4 * (f & 3) + (f >> 2)) : f; WRD(c + 1, ff); }
    if constexpr (c == 0 && mm < 8) { XLD(2 + (mm >> 2), mm & 3); }
    if constexpr (c == 2 && mm < 8) { XLD(4 + (mm >> 2), mm & 3); }
    if constexpr (c == 3 && mm < 4) { XLD(6, mm); }
    if constexpr (c == 4 && mm < 4) { XLD(7, mm); }
    if constexpr (c < 7) {
      if constexpr (mm == 8) { WAITV4(4 * (c == 0 ? 2 : c == 1 ? 1 : c <= 4 ? 2 : 6 - c), xr[c + 1][0], xr[c + 1][1], xr[c + 1][2], xr[c + 1][3]); }
      if constexpr (mm == 9 && c == 0) { KEEP(pfd0); }
      if constexpr (mm >= 8 && mm < 12) { XTRA(c + 1, mm - 8); }
      if constexpr (mm >= 12) { XTRB(c + 1, mm - 12); }
    }
    if constexpr (c == 5 && mm < 4) { st.o[mm] = f32x16{}; PIN(st.o[mm]); }
    if constexpr (c == 7 && mm >= 6 && !(mm & 1)) {
      constexpr int f = (mm - 6) >> 1, nb = f >> 1, sq = f & 1;
      u32x4 w;
#pragma unroll
      for (int e = 0; e < 4; ++e) w[e] = cvtpk(acc[nb][8 * sq + 2 * e] * QSCALE, acc[nb][8 * sq + 2 * e + 1] * QSCALE);
      st.q[f] = __builtin_bit_cast(bf16x8, w); PIN(st.q[f]);
    }
    SBAR();
    if constexpr (mm == 15) { BARRIER(); STAMP(3 + c); SBAR(); }
  });
  LSTAMP(58);
#undef XTRA
#undef XTRB
#undef XLD
#undef WRD
#undef QISSUE
#pragma unroll
  for (int m = 0; m < PD; ++m) st.kf[m] = ldsrd128((lds_cptr)L + SLOT(0) + 1024 * (r32 >> 2) + 64 * (r32 & 3) + 16 * ((2 * (m & 1) + hi) ^ ((r32 >> 2) & 3)) + 256 * (m >> 1));
#pragma unroll
  for (int f = 5; f < 8; ++f) {
    u32x4 w;
#pragma unroll
    for (int e = 0; e < 4; ++e) w[e] = cvtpk(acc[f >> 1][8 * (f & 1) + 2 * e] * QSCALE, acc[f >> 1][8 * (f & 1) + 2 * e + 1] * QSCALE);
    st.q[f] = __builtin_bit_cast(bf16x8, w);
  }
  st.negm = f32x16{}; PIN(st.negm);
  st.mhat = 0.f; st.lsum = 0.f;

  const lds_cptr kb0 = (lds_cptr)L + 1024 * (r32 >> 2) + 64 * (r32 & 3) + 16 * ((0 + hi) ^ ((r32 >> 2) & 3));
  const lds_cptr kb1 = (lds_cptr)L + 1024 * (r32 >> 2) + 64 * (r32 & 3) + 16 * ((2 + hi) ^ ((r32 >> 2) & 3));
  const lds_cptr vb = (lds_cptr)L + 16384 + 1024 * hi + 64 * tq + 32 * tblk + 8 * tp;

  f32x16 A0, A1, B0, B1;
  attn_step<true, false>(st, A0, A1, B0, B1, kb0 + SLOT(0), kb1 + SLOT(0), vb + SLOT(0), kb0 + SLOT(1), kb1 + SLOT(1), wid >= 4);
  STAMP(11);
  BARRIER();
  STAMP(12);
  int s0 = SLOT(1);
  for (int t = 1; t < 15; t += 2) {
    const int s1 = (s0 == 131072) ? 0 : s0 + 32768, s2 = (s1 == 131072) ? 0 : s1 + 32768;
    attn_step<false, false>(st, B0, B1, A0, A1, kb0 + s0, kb1 + s0, vb + s0, kb0 + s1, kb1 + s1, wid >= 4);
    STAMP(11 + 2 * t);
    BARRIER();
    STAMP(12 + 2 * t);
    attn_step<false, false>(st, A0, A1, B0, B1, kb0 + s1, kb1 + s1, vb + s1, kb0 + s2, kb1 + s2, wid >= 4);
    STAMP(13 + 2 * t);
    BARRIER();
    STAMP(14 + 2 * t);
    s0 = s2;
  }
  attn_step<false, false>(st, B0, B1, A0, A1, kb0 + SLOT(15), kb1 + SLOT(15), vb + SLOT(15), kb0 + SLOT(15), kb1 + SLOT(15), wid >= 4);
  STAMP(41);
  BARRIER();
  STAMP(42); LSTAMP(59);
  {
    lds_cptr vd = vb + SLOT(16); PIN(vd);
#define VRD(m) do { st.vlo[m] = ldstr(vd + 4096 * ((m) & 3) + 256 * ((m) >> 2)); st.vhi[m] = ldstr(vd + 4096 * ((m) & 3) + 2048 + 256 * ((m) >> 2)); } while (0)
    const int rbo = RBO();
    WREAD(0); WREAD(1); WREAD(2);
    BARRIER();
#pragma unroll
    for (int m = 0; m < 8; ++m) VRD(m);
    st.lsum += B0[9];
#pragma unroll
    for (int e = 10; e < 16; ++e) { B0[e] = __builtin_amdgcn_exp2f(B0[e]); st.lsum += B0[e]; }
    float l2 = 0.f;
#pragma unroll
    for (int e = 0; e < 16; ++e) { B1[e] = __builtin_amdgcn_exp2f(B1[e]); l2 += B1[e]; }
    st.lsum += l2;
#pragma unroll
    for (int e = 0; e < 4; ++e) { st.pw[1][e] = cvtpk(B0[8 + 2 * e], B0[9 + 2 * e]); st.pw[2][e] = cvtpk(B1[2 * e], B1[2 * e + 1]); st.pw[3][e] = cvtpk(B1[8 + 2 * e], B1[9 + 2 * e]); }
    auto rr = __builtin_amdgcn_permlane32_swap(__float_as_uint(st.lsum), __float_as_uint(st.lsum), false, false);
    const float inv = 1.0f / (__uint_as_float(rr[0]) + __uint_as_float(rr[1]));
    const __amdgpu_buffer_rsrc_t srdOs = __builtin_amdgcn_make_buffer_rsrc((void*)(ows + (size_t)(b * NH + h) * 57344), (short)0, 57344, 0x00020000);
    const int qrow = 32 * wid + r32, vblk = qrow / 28;
    const unsigned osv = vblk * 7168 + (hi * 28 + qrow - 28 * vblk) * 16;
#define OSTORE(db) do { _Pragma("unroll") for (int s_ = 0; s_ < 2; ++s_) { u32x4 w_; \
      _Pragma("unroll") for (int e_ = 0; e_ < 4; ++e_) w_[e_] = cvtpk(st.o[db][8 * s_ + 2 * e_] * inv, st.o[db][8 * s_ + 2 * e_ + 1] * inv); \
      __builtin_amdgcn_raw_buffer_store_b128(w_, srdOs, osv + (2 * (db) + s_) * 896, 0, 16); } } while (0)
    SBAR();
#pragma unroll
    for (int db = 0; db < 4; ++db) {
#pragma unroll
      for (int ks = 0; ks < 4; ++ks) {
        st.o[db] = MFMA(cat(st.vlo[4 * db + ks], st.vhi[4 * db + ks]), __builtin_bit_cast(bf16x8, st.pw[ks]), st.o[db]);
        if (db < 2) { VRD(4 * db + ks + 8); }
      }
      SBAR();
      if (db >= 1) { OSTORE(db - 1); }
      SBAR();
    }
    STAMP(43);
    OSTORE(3);
    STAMP(44);
#undef VRD
#undef OSTORE
    WAITV(0);
    if (lane == 0) {
      const unsigned old = __hip_atomic_fetch_add((__attribute__((address_space(3))) unsigned*)(L + PUBW), 1u, __ATOMIC_RELAXED, __HIP_MEMORY_SCOPE_WORKGROUP);
      if (old == 6) __hip_atomic_store(flags + b * NH + h, *(__attribute__((address_space(3))) unsigned long long*)(L + PUBW + 8), __ATOMIC_RELAXED, __HIP_MEMORY_SCOPE_AGENT);
    }
    asm volatile("s_waitcnt lgkmcnt(0)" ::: "memory");
    STAMP(50);
  }
  }

  {
    static_assert(NA == 8, "prefetch sequences above are written for NA = 8");
    const int rbo = RBO(); W3();
    const __amdgpu_buffer_rsrc_t srdO = __builtin_amdgcn_make_buffer_rsrc((void*)(ows + (size_t)ob * (NH * 57344)), (short)0, NH * 57344, 0x00020000);
    const unsigned ov0 = (hi * 28 + (r32 < 28 ? r32 : 27)) * 16;
    const unsigned osb = (w3 >> 2) * 57344 + (w3 & 3) * 1792 + oqt * 2 * 7168;
    u32x4 of[3][4];
    f32x16 acc[2][2];
#pragma unroll
    for (int i = 0; i < 2; ++i) { acc[i][0] = f32x16{}; acc[i][1] = f32x16{}; }
#define OLOAD(i) do { const unsigned so_ = osb + (unsigned)(RR(i) * 2 * 57344), so1_ = so_ + 7168u; \
      BLOAD("sc1", of[(i) % 3][0], ov0, srdO, so_, 0); BLOAD("sc1", of[(i) % 3][1], ov0, srdO, so1_, 0); \
      BLOAD("sc1", of[(i) % 3][2], ov0, srdO, so_, 896); BLOAD("sc1", of[(i) % 3][3], ov0, srdO, so1_, 896); } while (0)
    BARRIER();
    LSTAMP(57);
    STAMP(51);
    WDMA(4);
    WREAD(3); WDMA(8); WREAD(5); WDMA(10); WREAD(6); WDMA(11); WREAD(7); WDMA(12);
    WAITV(16); WREAD(4); WDMA(9);
    STAMP(52);
    if (w3 == 7) {
      const unsigned long long* fp = flags + ob * NH + r32;
      for (unsigned spins = 0; spins < (1u << 18); ++spins) {
        const unsigned long long v = __hip_atomic_load(fp, __ATOMIC_RELAXED, __HIP_MEMORY_SCOPE_AGENT);
        if (__all(v == tag)) break;
        __builtin_amdgcn_s_sleep(1);
      }
      KEEP(td);
    }
    STAMP(53);
    BARRIER();
    LSTAMP(60);
    STAMP(54);
    OLOAD(0); OLOAD(1);
#pragma unroll
    for (int i = 0; i < 16; ++i) {
      const int cnt = (i < 15 ? 4 : 0) + ((i - 1 >= 1 && i - 1 <= 3) ? 4 : 0);
      WAITV4(cnt, of[i % 3][0], of[i % 3][1], of[i % 3][2], of[i % 3][3]);
      if (i >= 1 && i <= 3) { WRWAIT(i - 1 + NA); WDMA(i + 12); }
      if (i + 2 < 16) { OLOAD(i + 2); }
      SBAR();
#pragma unroll
      for (int ks = 0; ks < 2; ++ks)
#pragma unroll
        for (int nb = 0; nb < 2; ++nb)
#pragma unroll
          for (int vb = 0; vb < 2; ++vb)
            acc[nb][vb] = MFMA(wf[i % NA][2 * ks + nb], __builtin_bit_cast(bf16x8, of[i % 3][2 * ks + vb]), acc[nb][vb]);
      SBAR();
      if (i + NA < 16) { WREAD_NW(i + NA); }
      SBAR();
    }
    STAMP(55);
#undef WDMA
#undef WREAD
#undef OLOAD
#undef RBP
#pragma unroll
    for (int vb = 0; vb < 2; ++vb)
#pragma unroll
      for (int nb = 0; nb < 2; ++nb)
#pragma unroll
        for (int rg = 0; rg < 4; ++rg) {
          const int q = 32 * vb + r32, slot = 8 * nb + 2 * rg + hi;
          f32x4 v = {acc[nb][vb][4 * rg], acc[nb][vb][4 * rg + 1], acc[nb][vb][4 * rg + 2], acc[nb][vb][4 * rg + 3]};
          *(__attribute__((address_space(3))) f32x4*)(L + (q >> 4) * 32768 + wid * 4096 + (q & 15) * 256 + ((slot ^ (q & 15)) * 16)) = v;
        }
    asm volatile("s_waitcnt lgkmcnt(0)" ::: "memory");
    BARRIER();
    STAMP(56); LSTAMP(61);
    const int q = tid >> 3, pc = tid & 7;
    f32x4 s0 = {0, 0, 0, 0}, s1 = {0, 0, 0, 0};
#pragma unroll
    for (int w = 0; w < 8; ++w) {
      s0 += *(const __attribute__((address_space(3))) f32x4*)(L + (q >> 4) * 32768 + w * 4096 + (q & 15) * 256 + (((2 * pc) ^ (q & 15)) * 16));
      s1 += *(const __attribute__((address_space(3))) f32x4*)(L + (q >> 4) * 32768 + w * 4096 + (q & 15) * 256 + (((2 * pc + 1) ^ (q & 15)) * 16));
    }
    u32x4 w = {cvtpk(s0[0], s0[1]), cvtpk(s0[2], s0[3]), cvtpk(s1[0], s1[1]), cvtpk(s1[2], s1[3])};
    if ((q & 31) < 28)
      *(u32x4*)(out + (size_t)(ob * SQ + 56 * oqt + 28 * (q >> 5) + (q & 31)) * (DM * 2) + ont * 128 + pc * 16) = w;
    if (blockIdx.x == 0 && wid == 7) {
      for (unsigned spins = 0; spins < (1u << 18); ++spins) {
        bool ok = true;
#pragma unroll
        for (int k = 0; k < 4; ++k) ok &= __hip_atomic_load(flags + 64 * k + lane, __ATOMIC_RELAXED, __HIP_MEMORY_SCOPE_AGENT) == tag;
        if (__all(ok)) break;
        __builtin_amdgcn_s_sleep(2);
      }
      if (lane == 0) __hip_atomic_store(epochp, tag, __ATOMIC_RELAXED, __HIP_MEMORY_SCOPE_AGENT);
    }
  }
  STAMP(40); RSTAMP(63);
  STFLUSH(ows + (64 << 20));
}

extern "C" void kernel_launch(void* const* d_in, const int* in_sizes, int n_in,
                              void* d_out, int out_size, void* d_ws, size_t ws_size,
                              hipStream_t stream) {
  (void)in_sizes; (void)n_in; (void)out_size; (void)ws_size;
  (void)hipFuncSetAttribute((const void*)mha_k1, hipFuncAttributeMaxDynamicSharedMemorySize, K1_LDS);
  mha_k1<<<256, 512, K1_LDS, stream>>>((const char*)d_in[0], (const char*)d_in[1], (const char*)d_in[2], (const char*)d_in[3], (const char*)d_in[4], (char*)d_ws, (char*)d_out);
}
```

```cpp
#include <hip/hip_runtime.h>
#include <hip/hip_bf16.h>
#include <stdint.h>
#include <utility>
#include <type_traits>

typedef short    bf16x8 __attribute__((ext_vector_type(8)));
typedef short    s16x4  __attribute__((ext_vector_type(4)));
typedef float    f32x16 __attribute__((ext_vector_type(16)));
typedef float    f32x4  __attribute__((ext_vector_type(4)));
typedef float    f32x2  __attribute__((ext_vector_type(2)));
typedef __bf16   bf16x2 __attribute__((ext_vector_type(2)));
typedef unsigned u32x4  __attribute__((ext_vector_type(4)));
typedef __attribute__((address_space(3))) char* lds_ptr;
typedef __attribute__((address_space(3))) const char* lds_cptr;

#define DI __device__ __forceinline__
#define SBAR() __builtin_amdgcn_sched_barrier(0)
#define PIN(x) asm volatile("" : "+v"(x))
#define MFMA(a, b, c) __builtin_amdgcn_mfma_f32_32x32x16_bf16(a, b, c, 0, 0, 0)
#define BARRIER() asm volatile("s_barrier" ::: "memory")

#define STAMP(i)
#define RSTAMP(i)
#define LSTAMP(i)
#define STFLUSH(dbg)
#define POLT "nt"
#define POLW ""
#define POLWO ""
#define POLT2 ""
#define POLO "sc1"
#define P3NA 8
#define TSCHED 3
#define PRIO_TOGGLE 16
#define EARLYKV 0
#define MAPK1 1
constexpr int NB = 8, SQ = 224, DM = 512, NH = 32, SK = 1024, DH = 128;
constexpr float QSCALE = 0.088388347648318440f * 1.4426950408889634f;
constexpr float THR = 8.0f;

template <int... Is, class F> DI void static_for_(std::integer_sequence<int, Is...>, F&& f) { (f(std::integral_constant<int, Is>{}), ...); }
template <int N, class F> DI void static_for(F&& f) { static_for_(std::make_integer_sequence<int, N>{}, f); }
DI unsigned cvtpk(float lo, float hi) { f32x2 v = {lo, hi}; bf16x2 b = __builtin_convertvector(v, bf16x2); return __builtin_bit_cast(unsigned, b); }
DI bf16x8 ldsrd128(lds_cptr p) { return *(const __attribute__((address_space(3))) bf16x8*)p; }
DI s16x4 ldstr(lds_cptr p) { return __builtin_bit_cast(s16x4, __builtin_amdgcn_ds_read_tr16_b64_v4i16((__attribute__((address_space(3))) s16x4*)p)); }
DI bf16x8 cat(s16x4 a, s16x4 b) { return __builtin_shufflevector(a, b, 0, 1, 2, 3, 4, 5, 6, 7); }
DI void ldswr128(lds_ptr p, u32x4 v) { *(__attribute__((address_space(3))) u32x4*)p = v; }
#define MX3(a, b, c) __builtin_fmaxf(__builtin_fmaxf((a), (b)), (c))

#define DMA4(POL, m0v, v0, v1, v2, v3, srd, soff)                                                     \
  asm volatile("s_mov_b32 m0, %0\n\ts_nop 0\n\t"                                                      \
               "buffer_load_dwordx4 %1, %5, %6 offen " POL " lds\n\t"                                 \
               "buffer_load_dwordx4 %2, %5, %6 offen offset:1024 " POL " lds\n\t"                     \
               "buffer_load_dwordx4 %3, %5, %6 offen offset:2048 " POL " lds\n\t"                     \
               "buffer_load_dwordx4 %4, %5, %6 offen offset:3072 " POL " lds"                         \
               :: "s"(m0v), "v"(v0), "v"(v1), "v"(v2), "v"(v3), "s"(srd), "s"(soff) : "memory")
#define DMA2(POL, m0v, v0, v1, srd, soff)                                                             \
  asm volatile("s_mov_b32 m0, %0\n\ts_nop 0\n\t"                                                      \
               "buffer_load_dwordx4 %1, %3, %4 offen " POL " lds\n\t"                                 \
               "buffer_load_dwordx4 %2, %3, %4 offen offset:1024 " POL " lds"                         \
               :: "s"(m0v), "v"(v0), "v"(v1), "s"(srd), "s"(soff) : "memory")
#define BLOAD(POL, dst, voff, srd, soff, imm)                                                         \
  asm volatile("buffer_load_dwordx4 %0, %1, %2, %3 offen offset:%4 " POL                              \
               : "=v"(dst) : "v"(voff), "s"(srd), "s"(soff), "i"(imm) : "memory")
#define WAITV(n) asm volatile("s_waitcnt vmcnt(%0)" :: "i"(n) : "memory")
#define WAITV4(n, a, b, c, d) asm volatile("s_waitcnt vmcnt(%4)" : "+v"(a), "+v"(b), "+v"(c), "+v"(d) : "i"(n) : "memory")

constexpr int K1_LDS = 163840;
constexpr size_t WS_FLAGS = (size_t)32 << 20, WS_EPOCH = WS_FLAGS + 4096;
DI constexpr int RB(int i) { return (i % 5) * 32768; }
DI constexpr int SLOT(int t) { return ((t + 3) % 5) * 32768; }
constexpr int PUBW = 131072;
DI constexpr int WQB(int c) { return c == 0 ? 6 : c == 7 ? 7 : c - 1; }

#define MFMA16(a, b, c) __builtin_amdgcn_mfma_f32_16x16x32_bf16(a, b, c, 0, 0, 0)
struct AttnState {
  f32x4 o[2][8];
  bf16x8 q[2][4];
  f32x4 negm[2];
  float mhat[2];
  f32x4 lacc[2];
  bf16x8 ones;
  bf16x8 kf[16];
  s16x4 vlo[16], vhi[16];
  u32x4 pw[2][2];
};
struct STile { f32x4 s[2][4]; };
DI float xmax4(float v) {
  auto a = __builtin_amdgcn_permlane16_swap(__float_as_uint(v), __float_as_uint(v), false, false);
  v = __builtin_fmaxf(__uint_as_float(a[0]), __uint_as_float(a[1]));
  auto c = __builtin_amdgcn_permlane32_swap(__float_as_uint(v), __float_as_uint(v), false, false);
  return __builtin_fmaxf(__uint_as_float(c[0]), __uint_as_float(c[1]));
}
DI float xsum4(float v) {
  auto a = __builtin_amdgcn_permlane16_swap(__float_as_uint(v), __float_as_uint(v), false, false);
  v = __uint_as_float(a[0]) + __uint_as_float(a[1]);
  auto c = __builtin_amdgcn_permlane32_swap(__float_as_uint(v), __float_as_uint(v), false, false);
  return __uint_as_float(c[0]) + __uint_as_float(c[1]);
}

#define PDV 5
constexpr int PD = PDV;
#define EL(T, n) (T).s[((n) >> 3) & 1][2 * ((n) >> 4) + (((n) >> 2) & 1)][(n) & 3]
#define ELV(T, n) (T).s[((n) >> 3) & 1][2 * ((n) >> 4) + (((n) >> 2) & 1)]
#define PWV(n) st.pw[((n) >> 3) & 1][(n) >> 4]
#define EX(v) __builtin_amdgcn_exp2f(v)
template <bool FIRST>
DI void attn_step(AttnState& st, STile& C, STile& P, lds_cptr kp, lds_cptr vp0, lds_cptr vp1, lds_cptr kn, const bool young) {
#define KRD(m, base) st.kf[m] = ldsrd128((base) + 4096 * ((m) >> 2) + 256 * ((m) & 3))
#define VRD(m) do { st.vlo[m] = ldstr((((m) & 1) ? vp1 : vp0) + 8192 * ((m) >> 3) + 256 * (((m) & 7) >> 1)); \
                    st.vhi[m] = ldstr((((m) & 1) ? vp1 : vp0) + 8192 * ((m) >> 3) + 4096 + 256 * (((m) & 7) >> 1)); } while (0)
  float ma = 0.f, mb = 0.f, fct0 = 1.f, fct1 = 1.f;
  bool resc = false;
  SBAR();
  static_for<32>([&](auto I) __attribute__((always_inline)) {
    constexpr int g = I.value;
    if constexpr (g == 0) { if (young) __builtin_amdgcn_s_setprio(1); }
    if constexpr (g == 16) { __builtin_amdgcn_s_setprio(0); }
    if constexpr (g < 16) {
      constexpr int kb = g >> 2, u = g & 3;
      if constexpr (u == 0) C.s[0][kb] = MFMA16(st.kf[g], st.q[0][0], st.negm[0]); else C.s[0][kb] = MFMA16(st.kf[g], st.q[0][u], C.s[0][kb]);
      PIN(C.s[0][kb]);
    } else if constexpr (!FIRST) {
      constexpr int m = g - 16;
      st.o[0][m & 7] = MFMA16(cat(st.vlo[m], st.vhi[m]), __builtin_bit_cast(bf16x8, st.pw[0][m >> 3]), st.o[0][m & 7]);
      PIN(st.o[0][m & 7]);
    }
    SBAR();
    {
      constexpr int f = g + PD;
      if constexpr (f < 16) { KRD(f, kp); }
      else if constexpr (f < 32) { if constexpr (!FIRST) { VRD(f - 16); } }
      else { KRD(f - 32, kn); }
    }
    if constexpr ((g & 1) && g >= 5 && g <= 19) {
      constexpr int kb = (g - 5) >> 2, qb = ((g - 5) >> 1) & 1;
      ma = MX3(ma, C.s[qb][kb][0], C.s[qb][kb][1]); PIN(ma);
    }
    SBAR();
    if constexpr (g < 16) {
      constexpr int kb = g >> 2, u = g & 3;
      if constexpr (u == 0) C.s[1][kb] = MFMA16(st.kf[g], st.q[1][0], st.negm[1]); else C.s[1][kb] = MFMA16(st.kf[g], st.q[1][u], C.s[1][kb]);
      PIN(C.s[1][kb]);
    } else if constexpr (!FIRST) {
      constexpr int m = g - 16;
      st.o[1][m & 7] = MFMA16(cat(st.vlo[m], st.vhi[m]), __builtin_bit_cast(bf16x8, st.pw[1][m >> 3]), st.o[1][m & 7]);
      PIN(st.o[1][m & 7]);
    }
    SBAR();
    if constexpr ((g & 1) == 0) {
      if constexpr (g <= 22) {
        if constexpr (!FIRST) { constexpr int n = 8 + g; PWV(n)[(n >> 1) & 3] = cvtpk(EL(P, n), EL(P, n + 1)); PIN(PWV(n)); }
      } else { constexpr int n = g - 24; PWV(n)[(n >> 1) & 3] = cvtpk(EL(C, n), EL(C, n + 1)); PIN(PWV(n)); }
    }
    if constexpr ((g & 1) && g >= 5 && g <= 19) {
      constexpr int kb = (g - 5) >> 2, qb = ((g - 5) >> 1) & 1;
      mb = MX3(mb, C.s[qb][kb][2], C.s[qb][kb][3]); PIN(mb);
    }
    if constexpr (g == 22) {
      if (FIRST || __builtin_expect(__any((ma > THR) | (mb > THR)), 0)) {
#pragma unroll
        for (int qb = 0; qb < 2; ++qb) {
          float rm = MX3(C.s[qb][0][0], C.s[qb][0][1], __builtin_fmaxf(C.s[qb][0][2], C.s[qb][0][3]));
#pragma unroll
          for (int kb = 1; kb < 4; ++kb) { rm = MX3(rm, C.s[qb][kb][0], C.s[qb][kb][1]); rm = MX3(rm, C.s[qb][kb][2], C.s[qb][kb][3]); }
          rm = xmax4(rm);
          const float dl = FIRST ? rm : __builtin_fmaxf(rm, 0.f);
          st.mhat[qb] += dl;
#pragma unroll
          for (int kb = 0; kb < 4; ++kb)
#pragma unroll
            for (int r = 0; r < 4; ++r) C.s[qb][kb][r] -= dl;
#pragma unroll
          for (int r = 0; r < 4; ++r) st.negm[qb][r] = -st.mhat[qb];
          PIN(st.negm[qb]);
          if constexpr (!FIRST) { const float f_ = EX(-dl); if (qb == 0) fct0 = f_; else fct1 = f_; }
        }
        resc = true;
      }
    }
    if constexpr (g <= 21) {
      if constexpr (!FIRST) { constexpr int n = 10 + g; EL(P, n) = EX(EL(P, n)); PIN(ELV(P, n)); }
    } else { constexpr int n = g - 22; EL(C, n) = EX(EL(C, n)); PIN(ELV(C, n)); }
    SBAR();
    if constexpr (!FIRST && (g == 21 || g == 23 || g == 25 || g == 27)) {
      constexpr int qb = (g >> 1) & 1, w = g >= 24;
      st.lacc[qb] = MFMA16(st.ones, __builtin_bit_cast(bf16x8, st.pw[qb][w]), st.lacc[qb]); PIN(st.lacc[qb]);
      SBAR();
    }
  });
  if constexpr (!FIRST) {
    if (__builtin_expect(resc, 0)) {
#pragma unroll
      for (int d = 0; d < 8; ++d)
#pragma unroll
        for (int r = 0; r < 4; ++r) { st.o[0][d][r] *= fct0; st.o[1][d][r] *= fct1; }
#pragma unroll
      for (int r = 0; r < 4; ++r) { st.lacc[0][r] *= fct0; st.lacc[1][r] *= fct1; }
    }
  }
#undef KRD
#undef VRD
}

__global__ __launch_bounds__(512, 2)
void mha_k1(const char* __restrict__ x, const char* __restrict__ kc, const char* __restrict__ vc,
            const char* __restrict__ wq, const char* __restrict__ wo, char* ows, char* __restrict__ out) {
  extern __shared__ __attribute__((aligned(16))) char lds[];
  const int tid = threadIdx.x, lane = tid & 63;
  const int wid = __builtin_amdgcn_readfirstlane(tid >> 6);
  const int b = 4 * (blockIdx.x & 1) + ((blockIdx.x >> 3) & 3), h = 8 * ((blockIdx.x >> 1) & 3) + (blockIdx.x >> 5);
  const unsigned lds0 = (unsigned)(uintptr_t)lds;
  const lds_ptr L = (lds_ptr)lds;
  RSTAMP(62); STAMP(0);

  const char *pfx, *pfw;
  {
    const int j = blockIdx.x >> 3, half = lane >> 5, l5 = lane & 31;
    const int xrow = (b & 4) * SQ + 28 * j + (l5 < 28 ? l5 : 27);
    const int idx = 32 * j + l5;
    pfw = wq + (size_t)(64 * half + (idx >> 4)) * 8192 + (h & 24) * 256 + (idx & 15) * 128;
    pfx = x + (size_t)xrow * (DM * 2) + half * 128;
  }
  unsigned pfd0;
  const char* pfe = wid >= 4 ? x : (wid & 1) ? pfw + (size_t)(wid >> 1) * (128 * 8192) : pfx + (wid >> 1) * 256;
#define TOUCH(d, p) asm volatile("global_load_dword %0, %1, off " POLT : "=v"(d) : "v"(p) : "memory")
#define KEEP(d) asm volatile("" :: "v"(d) : "memory")
  const __amdgpu_buffer_rsrc_t srdW = __builtin_amdgcn_make_buffer_rsrc((void*)(wq + h * 256), (short)0, DM * 8192 - h * 256, 0x00020000);
  const unsigned pl = ((lane >> 2) & 3), dbl = lane >> 4, c4 = lane & 3;
  const unsigned wv0 = pl * 8192 + dbl * 64 + c4 * 16, wv1 = wv0 + 31744;
#define DMAW2(c) DMA2(POLW, lds0 + WQB(c) * 16384 + 2048 * wid, wv0, wv1, srdW, (unsigned)(((c) * 64 + 8 * wid) * 8192))
  TOUCH(pfd0, pfe);
  const int ob = blockIdx.x & 7, oqt = blockIdx.x >> 6, ont = (blockIdx.x >> 3) & 7;
  const __amdgpu_buffer_rsrc_t srdWo = __builtin_amdgcn_make_buffer_rsrc((void*)(wo + ont * 128), (short)0, NH * DH * DM * 2 - ont * 128, 0x00020000);
  const unsigned ow0 = (4 * (lane >> 5) + ((lane >> 2) & 3)) * 1024 + ((lane >> 4) & 1) * 64 + (lane & 3) * 16;
  const unsigned ow1 = ow0 + 7168, ow2 = ow0 + 2 * 7168, ow3 = ow0 + 3 * 7168;
  unsigned long long* const epochp = (unsigned long long*)(ows + WS_EPOCH);
  unsigned long long* const flags = (unsigned long long*)(ows + WS_FLAGS);
  unsigned long long tag = 0;
  unsigned td = 0;
#define ROTM 0
#define ROTA 0
#define RR(i) (((i) + ROTM * ont + ROTA) & 15)
  constexpr int NA = P3NA;
  const int r32 = lane & 31, hi = lane >> 5, tq = (lane >> 2) & 3, tp = lane & 3, tblk = (lane >> 4) & 1;
  bf16x8 wf[NA][4];
#define RBP(i) ((lds_cptr)L + RB(i) + rbo)
#define WDMA(i) DMA4("", lds0 + (unsigned)(RB(i)) + 4096u * w3, ow0, ow1, ow2, ow3, srdWo, 32768u * w3 + (unsigned)(RR(i) * 256 * 1024))
#define W3() unsigned w3 = (unsigned)__builtin_amdgcn_readfirstlane(tid >> 6); asm volatile("s_nop 4" : "+s"(w3))
#define WREAD_NW(i) do { lds_cptr p_ = RBP(i); PIN(p_); _Pragma("unroll") for (int f_ = 0; f_ < 4; ++f_) \
      wf[(i) % NA][f_] = cat(ldstr(p_ + 1024 * (f_ >> 1) + 256 * (f_ & 1)), ldstr(p_ + 1024 * (f_ >> 1) + 2048 + 256 * (f_ & 1))); } while (0)
#define WRWAIT(i) asm volatile("s_waitcnt lgkmcnt(0)" : "+v"(wf[(i) % NA][0]), "+v"(wf[(i) % NA][1]), "+v"(wf[(i) % NA][2]), "+v"(wf[(i) % NA][3]) :: "memory")
#define WREAD(i) do { WREAD_NW(i); WRWAIT(i); } while (0)
#define RBO() (wid * 4096 + 512 * hi + 64 * tq + 32 * tblk + 8 * tp)
  if (wid == 7) {
    const __amdgpu_buffer_rsrc_t srdK = __builtin_amdgcn_make_buffer_rsrc((void*)(kc + (size_t)(b * NH + h) * (SK * DH * 2)), (short)0, SK * DH * 2, 0x00020000);
    const __amdgpu_buffer_rsrc_t srdV = __builtin_amdgcn_make_buffer_rsrc((void*)(vc + (size_t)(b * NH + h) * (SK * DH * 2)), (short)0, SK * DH * 2, 0x00020000);
    const unsigned wv2 = wv0 + 2 * 31744, wv3 = wv0 + 3 * 31744;
    const unsigned kvA = pl * 256 + dbl * 64 + c4 * 16, kvB = pl * 256 + dbl * 64 + ((c4 ^ 2) * 16);
#define DMAW(c) do { _Pragma("unroll") for (int i_ = 0; i_ < 4; ++i_) DMA4(POLW, lds0 + (unsigned)(WQB(c) * 16384 + 4096 * i_), wv0, wv1, wv2, wv3, srdW, (unsigned)(((c) * 64 + 16 * i_) * 8192)); } while (0)
#define DMAK_(t, so) do { _Pragma("unroll") for (int i_ = 0; i_ < 4; ++i_) DMA4("nt", lds0 + (unsigned)((so) + 4096 * i_), kvA, kvA, kvB, kvB, srdK, (unsigned)((t) * 16384 + 4096 * i_)); } while (0)
#define DMAV_(t, so) do { _Pragma("unroll") for (int i_ = 0; i_ < 4; ++i_) DMA4("nt", lds0 + (unsigned)((so) + 16384 + 4096 * i_), kvA, kvB, kvA, kvB, srdV, (unsigned)((t) * 16384 + 4096 * i_)); } while (0)
#define DMAK(t) DMAK_(t, SLOT(t))
#define DMAV(t) DMAV_(t, SLOT((t) + 1))
    const char* tw = wo + (size_t)(blockIdx.x >> 3) * 8192 + lane * 128;
#define TW(r) do { const char* p_ = tw + (size_t)((((r) + ROTM * (lane & 7) + ROTA) & 15) * 262144); asm volatile("global_load_dword %0, %1, off" : "+v"(td) : "v"(p_) : "memory"); } while (0)
    unsigned t4, t5, t6, t7;
    asm volatile("global_load_dwordx2 %0, %1, off sc1" : "=v"(tag) : "v"(epochp) : "memory");
#define RW(i) do { _Pragma("unroll") for (int w_ = 0; w_ < 8; ++w_) DMA4("", lds0 + (unsigned)(RB(i) + 4096 * w_), ow0, ow1, ow2, ow3, srdWo, (unsigned)((RR(i) * 256 + 32 * w_) * 1024)); } while (0)
    DMAW2(0);
    BARRIER();
    DMAW(1);
    WAITV(16); BARRIER();
    DMAW(2); DMAW(3);
    WAITV(32); BARRIER();
    { const char* p4 = pfx + 512; const char* p5 = pfw + (size_t)2 * 128 * 8192; const char* p6 = pfx + 768; const char* p7 = pfw + (size_t)3 * 128 * 8192;
      TOUCH(t4, p4); TOUCH(t5, p5); TOUCH(t6, p6); TOUCH(t7, p7); }
    WAITV(20); BARRIER();
    WAITV(4); BARRIER();
    DMAW(4); DMAW(5);
    WAITV(16); BARRIER();
    KEEP(pfd0); KEEP(t4); KEEP(t5); KEEP(t6); KEEP(t7);
    DMAW(6);
    WAITV(16); BARRIER();
    DMAW(7); DMAK(0);
    WAITV(32); BARRIER();
    DMAK(1); DMAV(0);
    WAITV(48); BARRIER();
    DMAK(2);
    BARRIER();
    DMAV(1);
    WAITV(48); BARRIER();
    DMAK(3); DMAV(2); DMAK(4);
    WAITV(63); BARRIER();
    DMAV(3); DMAV(4);
    DMAK(5);
    WAITV(63); BARRIER();
    {
      int so = SLOT(1);
      for (int t = 1; t < 11; ++t) {
        DMAK_(t + 5, so); DMAV_(t + 4, so);
        so = (so == 131072) ? 0 : so + 32768;
        WAITV(63);
        BARRIER();
      }
    }
    DMAV(15); TW(0); TW(1); TW(2); TW(3);
    asm volatile("" : "+v"(tag));
    tag = (((unsigned long long)(unsigned)__builtin_amdgcn_readfirstlane((unsigned)(tag >> 32)) << 32) | (unsigned)__builtin_amdgcn_readfirstlane((unsigned)tag)) + 1;
    if (lane == 0) { *(__attribute__((address_space(3))) unsigned*)(L + PUBW) = 0u; *(__attribute__((address_space(3))) unsigned long long*)(L + PUBW + 8) = tag; }
    asm volatile("s_waitcnt lgkmcnt(0)" ::: "memory");
    WAITV(63);
    BARRIER();
    TW(5); TW(6); TW(7); TW(4);
    WAITV(40);
    RW(0);
    BARRIER();
    RW(1); TW(8); TW(10); TW(11); TW(12);
    WAITV(63);
    BARRIER();
    RW(2); TW(9); TW(13); TW(14); TW(15);
    WAITV(4);
    BARRIER();
    {
      const int rbo = RBO();
      WREAD(0); WREAD(1); WREAD(2);
    }
    BARRIER();
    RW(3); RW(5); RW(6); RW(7);
    WAITV(0);
#undef TW
#undef RW
#undef DMAW
#undef DMAK
#undef DMAV
  } else {
  const __amdgpu_buffer_rsrc_t srdX = __builtin_amdgcn_make_buffer_rsrc((void*)(x + (size_t)(b * SQ + wid * 32) * (DM * 2)), (short)0, 32 * DM * 2, 0x00020000);
  const unsigned xv = ((lane >> 2) & 7) * 4096 + (lane >> 5) * 64 + (lane & 3) * 16;
  u32x4 xr[8][4];
#define QISSUE(c) do { _Pragma("unroll") for (int g_ = 0; g_ < 4; ++g_) BLOAD(POLW, xr[c][g_], xv, srdX, 0u, 1024 * g_ + 128 * (c)); } while (0)
  STAMP(45);
  DMAW2(0); QISSUE(0);
  STAMP(46);
  BARRIER();
  STAMP(47);
  QISSUE(1);
  STAMP(48);
  const lds_cptr wrb = (lds_cptr)L + 8192 * hi + 64 * tq + 32 * (tp & 1) + 16 * tblk + 8 * (tp >> 1);
  const bool odd1 = lane & 1, odd2 = lane & 2;

  AttnState st;
  f32x16 acc[4];
#pragma unroll
  for (int i = 0; i < 4; ++i) acc[i] = f32x16{};
  s16x4 wlo[16], whi[16];
#define DPP1(v) (unsigned)__builtin_amdgcn_mov_dpp((int)(v), 0xB1, 0xF, 0xF, true)
#define DPP2(v) (unsigned)__builtin_amdgcn_mov_dpp((int)(v), 0x4E, 0xF, 0xF, true)
#define XST(c, e, p, q, r, t, QP, MS, MC) do { unsigned o0_, o1_, o2_, o3_; \
    asm volatile("s_mov_b64 vcc, %9\n\t" \
                 "v_cndmask_b32_dpp %0, %5, %4, vcc quad_perm:" QP " row_mask:0xf bank_mask:0xf\n\t" \
                 "v_cndmask_b32_dpp %2, %7, %6, vcc quad_perm:" QP " row_mask:0xf bank_mask:0xf\n\t" \
                 "s_mov_b64 vcc, %8\n\t" \
                 "v_cndmask_b32_dpp %1, %4, %5, vcc quad_perm:" QP " row_mask:0xf bank_mask:0xf\n\t" \
                 "v_cndmask_b32_dpp %3, %6, %7, vcc quad_perm:" QP " row_mask:0xf bank_mask:0xf" \
                 : "=&v"(o0_), "=&v"(o1_), "=&v"(o2_), "=&v"(o3_) \
                 : "v"(xr[c][p][e]), "v"(xr[c][q][e]), "v"(xr[c][r][e]), "v"(xr[c][t][e]), "s"(MS), "s"(MC) : "vcc"); \
    xr[c][p][e] = o0_; xr[c][q][e] = o1_; xr[c][r][e] = o2_; xr[c][t][e] = o3_; } while (0)
#define XTRA(c, e) XST(c, e, 0, 1, 2, 3, "[1,0,3,2]", 0xAAAAAAAAAAAAAAAAull, 0x5555555555555555ull)
#define XTRB(c, e) XST(c, e, 0, 2, 1, 3, "[2,3,0,1]", 0xCCCCCCCCCCCCCCCCull, 0x3333333333333333ull)
#define XLD(c, g_) BLOAD(POLW, xr[c][g_], xv, srdX, 0u, 1024 * (g_) + 128 * (c))
  lds_cptr wrb2 = wrb + 65536; PIN(wrb2);
#define WRB(c) ((WQB(c) < 4 ? wrb : wrb2) + (WQB(c) & 3) * 16384)
#define WRD(c, m) do { wlo[m] = ldstr(WRB(c) + 2048 * ((m) >> 2) + 256 * ((m) & 3)); whi[m] = ldstr(WRB(c) + 2048 * ((m) >> 2) + 1024 + 256 * ((m) & 3)); } while (0)
  WAITV4(4, xr[0][0], xr[0][1], xr[0][2], xr[0][3]);
  STAMP(49);
  BARRIER();
#pragma unroll
  for (int m = 0; m < PD; ++m) WRD(0, m);
#pragma unroll
  for (int e = 0; e < 4; ++e) { XTRA(0, e); }
#pragma unroll
  for (int e = 0; e < 4; ++e) { XTRB(0, e); }
  asm volatile("s_nop 1" ::: "memory");
  STAMP(1); STAMP(2);
#define QCVT(nb) do { u32x4 x_, y_; \
    _Pragma("unroll") for (int e_ = 0; e_ < 4; ++e_) { \
      const unsigned lo_ = cvtpk(acc[nb][2 * e_] * QSCALE, acc[nb][2 * e_ + 1] * QSCALE), hi_ = cvtpk(acc[nb][8 + 2 * e_] * QSCALE, acc[nb][9 + 2 * e_] * QSCALE); \
      auto r_ = __builtin_amdgcn_permlane16_swap(lo_, hi_, false, false); x_[e_] = r_[0]; y_[e_] = r_[1]; } \
    st.q[0][nb] = __builtin_bit_cast(bf16x8, x_); st.q[1][nb] = __builtin_bit_cast(bf16x8, y_); PIN(st.q[0][nb]); PIN(st.q[1][nb]); } while (0)
  static_for<128>([&](auto I) __attribute__((always_inline)) {
    constexpr int c = I.value >> 4, mm = I.value & 15;
    constexpr int m = (c == 7) ? (4 * (mm & 3) + (mm >> 2)) : mm;
    acc[m & 3] = MFMA(cat(wlo[m], whi[m]), __builtin_bit_cast(bf16x8, xr[c][m >> 2]), acc[m & 3]);
    if constexpr (c == 0 && mm + PD == 16) { BARRIER(); }
    if constexpr (mm + PD < 16) { constexpr int f = mm + PD, ff = (c == 7) ? (4 * (f & 3) + (f >> 2)) : f; WRD(c, ff); }
    else if constexpr (c < 7) { constexpr int f = mm + PD - 16, ff = (c + 1 == 7) ? (4 * (f & 3) + (f >> 2)) : f; WRD(c + 1, ff); }
    if constexpr (c == 0 && mm < 8) { XLD(2 + (mm >> 2), mm & 3); }
    if constexpr (c == 2 && mm < 8) { XLD(4 + (mm >> 2), mm & 3); }
    if constexpr (c == 3 && mm < 4) { XLD(6, mm); }
    if constexpr (c == 4 && mm < 4) { XLD(7, mm); }
    if constexpr (c < 7) {
      if constexpr (mm == 8) { WAITV4(4 * (c == 0 ? 2 : c == 1 ? 1 : c <= 4 ? 2 : 6 - c), xr[c + 1][0], xr[c + 1][1], xr[c + 1][2], xr[c + 1][3]); }
      if constexpr (mm == 9 && c == 0) { KEEP(pfd0); }
      if constexpr (mm >= 8 && mm < 12) { XTRA(c + 1, mm - 8); }
      if constexpr (mm >= 12) { XTRB(c + 1, mm - 12); }
    }
    if constexpr (c == 5) { st.o[mm >> 3][mm & 7] = f32x4{}; PIN(st.o[mm >> 3][mm & 7]); }
    if constexpr (c == 7 && (mm == 6 || mm == 10 || mm == 14)) { QCVT((mm - 6) >> 2); }
    SBAR();
    if constexpr (mm == 15) { BARRIER(); STAMP(3 + c); SBAR(); }
  });
  LSTAMP(58);
#undef XTRA
#undef XTRB
#undef XLD
#undef WRD
#undef QISSUE
  const int l16 = lane & 15, g4 = lane >> 4;
  const lds_cptr kb = (lds_cptr)L + 1024 * (l16 >> 2) + 64 * (l16 & 3) + 16 * (g4 ^ ((l16 >> 2) & 2));
  const lds_cptr vb0 = (lds_cptr)L + 16384 + 1024 * g4 + 64 * tq + 32 * (g4 & 1) + 8 * tp;
  const lds_cptr vb1 = (lds_cptr)L + 16384 + 1024 * g4 + 64 * tq + 32 * (1 - (g4 & 1)) + 8 * tp;
#pragma unroll
  for (int m = 0; m < PD; ++m) st.kf[m] = ldsrd128(kb + SLOT(0) + 4096 * (m >> 2) + 256 * (m & 3));
  QCVT(3);
#undef QCVT
  st.negm[0] = f32x4{}; st.negm[1] = f32x4{}; PIN(st.negm[0]); PIN(st.negm[1]);
  st.mhat[0] = st.mhat[1] = 0.f; st.lacc[0] = f32x4{}; st.lacc[1] = f32x4{};
  st.ones = bf16x8{0x3F80, 0x3F80, 0x3F80, 0x3F80, 0x3F80, 0x3F80, 0x3F80, 0x3F80}; PIN(st.ones);

  STile A, B;
  attn_step<true>(st, A, B, kb + SLOT(0), vb0 + SLOT(0), vb1 + SLOT(0), kb + SLOT(1), wid >= 4);
  STAMP(11);
  BARRIER();
  STAMP(12);
  int s0 = SLOT(1);
  for (int t = 1; t < 15; t += 2) {
    const int s1 = (s0 == 131072) ? 0 : s0 + 32768, s2 = (s1 == 131072) ? 0 : s1 + 32768;
    attn_step<false>(st, B, A, kb + s0, vb0 + s0, vb1 + s0, kb + s1, wid >= 4);
    STAMP(11 + 2 * t);
    BARRIER();
    STAMP(12 + 2 * t);
    attn_step<false>(st, A, B, kb + s1, vb0 + s1, vb1 + s1, kb + s2, wid >= 4);
    STAMP(13 + 2 * t);
    BARRIER();
    STAMP(14 + 2 * t);
    s0 = s2;
  }
  attn_step<false>(st, B, A, kb + SLOT(15), vb0 + SLOT(15), vb1 + SLOT(15), kb + SLOT(15), wid >= 4);
  STAMP(41);
  BARRIER();
  STAMP(42); LSTAMP(59);
  {
    lds_cptr vd0 = vb0 + SLOT(16), vd1 = vb1 + SLOT(16); PIN(vd0); PIN(vd1);
#define VRD(m) do { st.vlo[m] = ldstr((((m) & 2) ? vd1 : vd0) + 8192 * ((m) & 1) + 256 * ((m) >> 2)); st.vhi[m] = ldstr((((m) & 2) ? vd1 : vd0) + 8192 * ((m) & 1) + 4096 + 256 * ((m) >> 2)); } while (0)
    const int rbo = RBO();
    WREAD(0); WREAD(1); WREAD(2);
    BARRIER();
#pragma unroll
    for (int m = 0; m < 8; ++m) VRD(m);
    static_for<22>([&](auto I) __attribute__((always_inline)) { constexpr int n = 10 + I.value; EL(B, n) = EX(EL(B, n)); });
    static_for<12>([&](auto I) __attribute__((always_inline)) { constexpr int n = 8 + 2 * I.value; PWV(n)[(n >> 1) & 3] = cvtpk(EL(B, n), EL(B, n + 1)); });
#pragma unroll
    for (int w = 0; w < 2; ++w) {
      st.lacc[0] = MFMA16(st.ones, __builtin_bit_cast(bf16x8, st.pw[0][w]), st.lacc[0]);
      st.lacc[1] = MFMA16(st.ones, __builtin_bit_cast(bf16x8, st.pw[1][w]), st.lacc[1]);
    }
    const float inv0 = 1.0f / st.lacc[0][0], inv1 = 1.0f / st.lacc[1][0];
    const __amdgpu_buffer_rsrc_t srdOs = __builtin_amdgcn_make_buffer_rsrc((void*)(ows + (size_t)(b * NH + h) * 57344), (short)0, 57344, 0x00020000);
    const int qr0 = 32 * wid + l16, vk0 = qr0 / 28, qr1 = qr0 + 16, vk1 = qr1 / 28;
    const unsigned osv0 = vk0 * 7168 + (g4 >> 1) * 896 + ((g4 & 1) * 28 + qr0 - 28 * vk0) * 16, osv1 = vk1 * 7168 + (g4 >> 1) * 896 + ((g4 & 1) * 28 + qr1 - 28 * vk1) * 16;
#define OSTORE(u) do { \
      u32x4 w0_ = {cvtpk(st.o[0][2 * (u)][0] * inv0, st.o[0][2 * (u)][1] * inv0), cvtpk(st.o[0][2 * (u)][2] * inv0, st.o[0][2 * (u)][3] * inv0), \
                   cvtpk(st.o[0][2 * (u) + 1][0] * inv0, st.o[0][2 * (u) + 1][1] * inv0), cvtpk(st.o[0][2 * (u) + 1][2] * inv0, st.o[0][2 * (u) + 1][3] * inv0)}; \
      __builtin_amdgcn_raw_buffer_store_b128(w0_, srdOs, osv0 + (u) * 1792, 0, 16); \
      u32x4 w1_ = {cvtpk(st.o[1][2 * (u)][0] * inv1, st.o[1][2 * (u)][1] * inv1), cvtpk(st.o[1][2 * (u)][2] * inv1, st.o[1][2 * (u)][3] * inv1), \
                   cvtpk(st.o[1][2 * (u) + 1][0] * inv1, st.o[1][2 * (u) + 1][1] * inv1), cvtpk(st.o[1][2 * (u) + 1][2] * inv1, st.o[1][2 * (u) + 1][3] * inv1)}; \
      __builtin_amdgcn_raw_buffer_store_b128(w1_, srdOs, osv1 + (u) * 1792, 0, 16); } while (0)
    SBAR();
#pragma unroll
    for (int u = 0; u < 4; ++u) {
#pragma unroll
      for (int k = 0; k < 4; ++k) {
        const int m = 4 * u + k, db = m >> 1, w = m & 1;
        st.o[0][db] = MFMA16(cat(st.vlo[m], st.vhi[m]), __builtin_bit_cast(bf16x8, st.pw[0][w]), st.o[0][db]);
        st.o[1][db] = MFMA16(cat(st.vlo[m], st.vhi[m]), __builtin_bit_cast(bf16x8, st.pw[1][w]), st.o[1][db]);
        if (u < 2) { VRD(m + 8); }
      }
      SBAR();
      if (u >= 1) { OSTORE(u - 1); }
      SBAR();
    }
    STAMP(43);
    OSTORE(3);
    STAMP(44);
#undef VRD
#undef OSTORE
    WAITV(0);
    if (lane == 0) {
      const unsigned old = __hip_atomic_fetch_add((__attribute__((address_space(3))) unsigned*)(L + PUBW), 1u, __ATOMIC_RELAXED, __HIP_MEMORY_SCOPE_WORKGROUP);
      if (old == 6) __hip_atomic_store(flags + b * NH + h, *(__attribute__((address_space(3))) unsigned long long*)(L + PUBW + 8), __ATOMIC_RELAXED, __HIP_MEMORY_SCOPE_AGENT);
    }
    asm volatile("s_waitcnt lgkmcnt(0)" ::: "memory");
    STAMP(50);
  }
  }

  {
    static_assert(NA == 8, "prefetch sequences above are written for NA = 8");
    const int rbo = RBO(); W3();
    const __amdgpu_buffer_rsrc_t srdO = __builtin_amdgcn_make_buffer_rsrc((void*)(ows + (size_t)ob * (NH * 57344)), (short)0, NH * 57344, 0x00020000);
    const unsigned ov0 = (hi * 28 + (r32 < 28 ? r32 : 27)) * 16;
    const unsigned osb = (w3 >> 2) * 57344 + (w3 & 3) * 1792 + oqt * 2 * 7168;
    u32x4 of[3][4];
    f32x16 acc[2][2];
#pragma unroll
    for (int i = 0; i < 2; ++i) { acc[i][0] = f32x16{}; acc[i][1] = f32x16{}; }
#define OLOAD(i) do { const unsigned so_ = osb + (unsigned)(RR(i) * 2 * 57344), so1_ = so_ + 7168u; \
      BLOAD("sc1", of[(i) % 3][0], ov0, srdO, so_, 0); BLOAD("sc1", of[(i) % 3][1], ov0, srdO, so1_, 0); \
      BLOAD("sc1", of[(i) % 3][2], ov0, srdO, so_, 896); BLOAD("sc1", of[(i) % 3][3], ov0, srdO, so1_, 896); } while (0)
    BARRIER();
    LSTAMP(57);
    STAMP(51);
    WDMA(4);
    WREAD(3); WDMA(8); WREAD(5); WDMA(10); WREAD(6); WDMA(11); WREAD(7); WDMA(12);
    WAITV(16); WREAD(4); WDMA(9);
    STAMP(52);
    if (w3 == 7) {
      const unsigned long long* fp = flags + ob * NH + r32;
      for (unsigned spins = 0; spins < (1u << 18); ++spins) {
        const unsigned long long v = __hip_atomic_load(fp, __ATOMIC_RELAXED, __HIP_MEMORY_SCOPE_AGENT);
        if (__all(v == tag)) break;
        __builtin_amdgcn_s_sleep(1);
      }
      KEEP(td);
    }
    STAMP(53);
    BARRIER();
    LSTAMP(60);
    STAMP(54);
    OLOAD(0); OLOAD(1);
#pragma unroll
    for (int i = 0; i < 16; ++i) {
      const int cnt = (i < 15 ? 4 : 0) + ((i - 1 >= 1 && i - 1 <= 3) ? 4 : 0);
      WAITV4(cnt, of[i % 3][0], of[i % 3][1], of[i % 3][2], of[i % 3][3]);
      if (i >= 1 && i <= 3) { WRWAIT(i - 1 + NA); WDMA(i + 12); }
      if (i + 2 < 16) { OLOAD(i + 2); }
      SBAR();
#pragma unroll
      for (int ks = 0; ks < 2; ++ks)
#pragma unroll
        for (int nb = 0; nb < 2; ++nb)
#pragma unroll
          for (int vb = 0; vb < 2; ++vb)
            acc[nb][vb] = MFMA(wf[i % NA][2 * ks + nb], __builtin_bit_cast(bf16x8, of[i % 3][2 * ks + vb]), acc[nb][vb]);
      SBAR();
      if (i + NA < 16) { WREAD_NW(i + NA); }
      SBAR();
    }
    STAMP(55);
#undef WDMA
#undef WREAD
#undef OLOAD
#undef RBP
#pragma unroll
    for (int vb = 0; vb < 2; ++vb)
#pragma unroll
      for (int nb = 0; nb < 2; ++nb)
#pragma unroll
        for (int rg = 0; rg < 4; ++rg) {
          const int q = 32 * vb + r32, slot = 8 * nb + 2 * rg + hi;
          f32x4 v = {acc[nb][vb][4 * rg], acc[nb][vb][4 * rg + 1], acc[nb][vb][4 * rg + 2], acc[nb][vb][4 * rg + 3]};
          *(__attribute__((address_space(3))) f32x4*)(L + (q >> 4) * 32768 + wid * 4096 + (q & 15) * 256 + ((slot ^ (q & 15)) * 16)) = v;
        }
    asm volatile("s_waitcnt lgkmcnt(0)" ::: "memory");
    BARRIER();
    STAMP(56); LSTAMP(61);
    const int q = tid >> 3, pc = tid & 7;
    f32x4 s0 = {0, 0, 0, 0}, s1 = {0, 0, 0, 0};
#pragma unroll
    for (int w = 0; w < 8; ++w) {
      s0 += *(const __attribute__((address_space(3))) f32x4*)(L + (q >> 4) * 32768 + w * 4096 + (q & 15) * 256 + (((2 * pc) ^ (q & 15)) * 16));
      s1 += *(const __attribute__((address_space(3))) f32x4*)(L + (q >> 4) * 32768 + w * 4096 + (q & 15) * 256 + (((2 * pc + 1) ^ (q & 15)) * 16));
    }
    u32x4 w = {cvtpk(s0[0], s0[1]), cvtpk(s0[2], s0[3]), cvtpk(s1[0], s1[1]), cvtpk(s1[2], s1[3])};
    if ((q & 31) < 28)
      *(u32x4*)(out + (size_t)(ob * SQ + 56 * oqt + 28 * (q >> 5) + (q & 31)) * (DM * 2) + ont * 128 + pc * 16) = w;
    if (blockIdx.x == 0 && wid == 7) {
      for (unsigned spins = 0; spins < (1u << 18); ++spins) {
        bool ok = true;
#pragma unroll
        for (int k = 0; k < 4; ++k) ok &= __hip_atomic_load(flags + 64 * k + lane, __ATOMIC_RELAXED, __HIP_MEMORY_SCOPE_AGENT) == tag;
        if (__all(ok)) break;
        __builtin_amdgcn_s_sleep(2);
      }
      if (lane == 0) __hip_atomic_store(epochp, tag, __ATOMIC_RELAXED, __HIP_MEMORY_SCOPE_AGENT);
    }
  }
  STAMP(40); RSTAMP(63);
  STFLUSH(ows + (64 << 20));
}

extern "C" void kernel_launch(void* const* d_in, const int* in_sizes, int n_in,
                              void* d_out, int out_size, void* d_ws, size_t ws_size,
                              hipStream_t stream) {
  (void)in_sizes; (void)n_in; (void)out_size; (void)ws_size;
  (void)hipFuncSetAttribute((const void*)mha_k1, hipFuncAttributeMaxDynamicSharedMemorySize, K1_LDS);
  mha_k1<<<256, 512, K1_LDS, stream>>>((const char*)d_in[0], (const char*)d_in[1], (const char*)d_in[2], (const char*)d_in[3], (const char*)d_in[4], (char*)d_ws, (char*)d_out);
}
```

```cpp
#include <hip/hip_runtime.h>
#include <hip/hip_bf16.h>
#include <stdint.h>
#include <utility>
#include <type_traits>

typedef short    bf16x8 __attribute__((ext_vector_type(8)));
typedef short    s16x4  __attribute__((ext_vector_type(4)));
typedef float    f32x16 __attribute__((ext_vector_type(16)));
typedef float    f32x4  __attribute__((ext_vector_type(4)));
typedef float    f32x2  __attribute__((ext_vector_type(2)));
typedef __bf16   bf16x2 __attribute__((ext_vector_type(2)));
typedef unsigned u32x4  __attribute__((ext_vector_type(4)));
typedef __attribute__((address_space(3))) char* lds_ptr;
typedef __attribute__((address_space(3))) const char* lds_cptr;

#define DI __device__ __forceinline__
#define SBAR() __builtin_amdgcn_sched_barrier(0)
#define PIN(x) asm volatile("" : "+v"(x))
#define MFMA(a, b, c) __builtin_amdgcn_mfma_f32_32x32x16_bf16(a, b, c, 0, 0, 0)
#define BARRIER() asm volatile("s_barrier" ::: "memory")

#define STAMP(i)
#define RSTAMP(i)
#define LSTAMP(i)
#define STFLUSH(dbg)
#define POLT "nt"
#define POLW ""
#define POLWO ""
#define POLT2 ""
#define POLO "sc1"
#define P3NA 8
#define TSCHED 3
#define PRIO_TOGGLE 21
#define EARLYKV 0
#define MAPK1 1
constexpr int NB = 8, SQ = 224, DM = 512, NH = 32, SK = 1024, DH = 128;
constexpr float QSCALE = 0.088388347648318440f * 1.4426950408889634f;
constexpr float THR = 8.0f;

template <int... Is, class F> DI void static_for_(std::integer_sequence<int, Is...>, F&& f) { (f(std::integral_constant<int, Is>{}), ...); }
template <int N, class F> DI void static_for(F&& f) { static_for_(std::make_integer_sequence<int, N>{}, f); }
DI unsigned cvtpk(float lo, float hi) { f32x2 v = {lo, hi}; bf16x2 b = __builtin_convertvector(v, bf16x2); return __builtin_bit_cast(unsigned, b); }
DI bf16x8 ldsrd128(lds_cptr p) { return *(const __attribute__((address_space(3))) bf16x8*)p; }
DI s16x4 ldstr(lds_cptr p) { return __builtin_bit_cast(s16x4, __builtin_amdgcn_ds_read_tr16_b64_v4i16((__attribute__((address_space(3))) s16x4*)p)); }
DI bf16x8 cat(s16x4 a, s16x4 b) { return __builtin_shufflevector(a, b, 0, 1, 2, 3, 4, 5, 6, 7); }
DI void ldswr128(lds_ptr p, u32x4 v) { *(__attribute__((address_space(3))) u32x4*)p = v; }
#define MX3(a, b, c) __builtin_fmaxf(__builtin_fmaxf((a), (b)), (c))

#define DMA4(POL, m0v, v0, v1, v2, v3, srd, soff)                                                     \
  asm volatile("s_mov_b32 m0, %0\n\ts_nop 0\n\t"                                                      \
               "buffer_load_dwordx4 %1, %5, %6 offen " POL " lds\n\t"                                 \
               "buffer_load_dwordx4 %2, %5, %6 offen offset:1024 " POL " lds\n\t"                     \
               "buffer_load_dwordx4 %3, %5, %6 offen offset:2048 " POL " lds\n\t"                     \
               "buffer_load_dwordx4 %4, %5, %6 offen offset:3072 " POL " lds"                         \
               :: "s"(m0v), "v"(v0), "v"(v1), "v"(v2), "v"(v3), "s"(srd), "s"(soff) : "memory")
#define DMA2(POL, m0v, v0, v1, srd, soff)                                                             \
  asm volatile("s_mov_b32 m0, %0\n\ts_nop 0\n\t"                                                      \
               "buffer_load_dwordx4 %1, %3, %4 offen " POL " lds\n\t"                                 \
               "buffer_load_dwordx4 %2, %3, %4 offen offset:1024 " POL " lds"                         \
               :: "s"(m0v), "v"(v0), "v"(v1), "s"(srd), "s"(soff) : "memory")
#define BLOAD(POL, dst, voff, srd, soff, imm)                                                         \
  asm volatile("buffer_load_dwordx4 %0, %1, %2, %3 offen offset:%4 " POL                              \
               : "=v"(dst) : "v"(voff), "s"(srd), "s"(soff), "i"(imm) : "memory")
#define WAITV(n) asm volatile("s_waitcnt vmcnt(%0)" :: "i"(n) : "memory")
#define WAITV4(n, a, b, c, d) asm volatile("s_waitcnt vmcnt(%4)" : "+v"(a), "+v"(b), "+v"(c), "+v"(d) : "i"(n) : "memory")

constexpr int K1_LDS = 163840;
constexpr size_t WS_FLAGS = (size_t)32 << 20, WS_EPOCH = WS_FLAGS + 4096;
DI constexpr int SLOT(int t) { return (t % 5) * 32768; }
DI constexpr int RB(int i) { return ((i + 2) % 5) * 32768; }
constexpr int PUBW = 32768;
DI constexpr int WQO(int c) { return (c & 3) == 0 ? 0 : (c & 3) == 1 ? 16384 : (c & 3) == 2 ? 131072 : 147456; }
DI constexpr int XO(int c) { return (c % 3) == 0 ? 65536 : (c % 3) == 1 ? 98304 : 32768; }

#define MFMA16(a, b, c) __builtin_amdgcn_mfma_f32_16x16x32_bf16(a, b, c, 0, 0, 0)
struct AttnState {
  f32x4 o[2][8];
  bf16x8 q[2][4];
  f32x4 negm[2];
  float mhat[2];
  f32x4 lacc[2];
  bf16x8 ones;
  bf16x8 kf[16];
  s16x4 vlo[16], vhi[16];
  u32x4 pw[2][2];
};
struct STile { f32x4 s[2][4]; };
DI float xmax4(float v) {
  auto a = __builtin_amdgcn_permlane16_swap(__float_as_uint(v), __float_as_uint(v), false, false);
  v = __builtin_fmaxf(__uint_as_float(a[0]), __uint_as_float(a[1]));
  auto c = __builtin_amdgcn_permlane32_swap(__float_as_uint(v), __float_as_uint(v), false, false);
  return __builtin_fmaxf(__uint_as_float(c[0]), __uint_as_float(c[1]));
}
DI float xsum4(float v) {
  auto a = __builtin_amdgcn_permlane16_swap(__float_as_uint(v), __float_as_uint(v), false, false);
  v = __uint_as_float(a[0]) + __uint_as_float(a[1]);
  auto c = __builtin_amdgcn_permlane32_swap(__float_as_uint(v), __float_as_uint(v), false, false);
  return __uint_as_float(c[0]) + __uint_as_float(c[1]);
}

#define PDV 5
constexpr int PD = PDV;
#define EL(T, n) (T).s[((n) >> 3) & 1][2 * ((n) >> 4) + (((n) >> 2) & 1)][(n) & 3]
#define ELV(T, n) (T).s[((n) >> 3) & 1][2 * ((n) >> 4) + (((n) >> 2) & 1)]
#define PWV(n) st.pw[((n) >> 3) & 1][(n) >> 4]
#define EX(v) __builtin_amdgcn_exp2f(v)
template <bool FIRST>
DI void attn_step(AttnState& st, STile& C, STile& P, lds_cptr kp, lds_cptr vp0, lds_cptr vp1, lds_cptr kn, const bool young) {
#define KRD(m, base) st.kf[m] = ldsrd128((base) + 4096 * ((m) >> 2) + 256 * ((m) & 3))
#define VRD(m) do { st.vlo[m] = ldstr((((m) & 1) ? vp1 : vp0) + 8192 * ((m) >> 3) + 256 * (((m) & 7) >> 1)); \
                    st.vhi[m] = ldstr((((m) & 1) ? vp1 : vp0) + 8192 * ((m) >> 3) + 4096 + 256 * (((m) & 7) >> 1)); } while (0)
  float ma = -3.0e38f, mb = -3.0e38f, fct0 = 1.f, fct1 = 1.f;
  bool resc = false;
  SBAR();
  static_for<32>([&](auto I) __attribute__((always_inline)) {
    constexpr int g = I.value;
    if constexpr (g == 0) { if (young) __builtin_amdgcn_s_setprio(1); }
    if constexpr (g == PRIO_TOGGLE) { __builtin_amdgcn_s_setprio(0); }
    if constexpr (g < 16) {
      constexpr int kb = g >> 2, u = g & 3;
      if constexpr (u == 0) C.s[0][kb] = MFMA16(st.kf[g], st.q[0][0], st.negm[0]); else C.s[0][kb] = MFMA16(st.kf[g], st.q[0][u], C.s[0][kb]);
      PIN(C.s[0][kb]);
    } else if constexpr (!FIRST) {
      constexpr int m = g - 16;
      st.o[0][m & 7] = MFMA16(cat(st.vlo[m], st.vhi[m]), __builtin_bit_cast(bf16x8, st.pw[0][m >> 3]), st.o[0][m & 7]);
      PIN(st.o[0][m & 7]);
    }
    SBAR();
    {
      constexpr int f = g + PD;
      if constexpr (f < 16) { KRD(f, kp); }
      else if constexpr (f < 32) { if constexpr (!FIRST) { VRD(f - 16); } }
      else { KRD(f - 32, kn); }
    }
    if constexpr ((g & 1) && g >= 5 && g <= 19) {
      constexpr int kb = (g - 5) >> 2, hf = ((g - 5) >> 1) & 1;
      ma = MX3(ma, C.s[0][kb][2 * hf], C.s[0][kb][2 * hf + 1]); PIN(ma);
    }
    SBAR();
    if constexpr (g < 16) {
      constexpr int kb = g >> 2, u = g & 3;
      if constexpr (u == 0) C.s[1][kb] = MFMA16(st.kf[g], st.q[1][0], st.negm[1]); else C.s[1][kb] = MFMA16(st.kf[g], st.q[1][u], C.s[1][kb]);
      PIN(C.s[1][kb]);
    } else if constexpr (!FIRST) {
      constexpr int m = g - 16;
      st.o[1][m & 7] = MFMA16(cat(st.vlo[m], st.vhi[m]), __builtin_bit_cast(bf16x8, st.pw[1][m >> 3]), st.o[1][m & 7]);
      PIN(st.o[1][m & 7]);
    }
    SBAR();
    if constexpr ((g & 1) == 0) {
      if constexpr (g <= 22) {
        if constexpr (!FIRST) { constexpr int n = 8 + g; PWV(n)[(n >> 1) & 3] = cvtpk(EL(P, n), EL(P, n + 1)); PIN(PWV(n)); }
      } else { constexpr int n = g - 24; PWV(n)[(n >> 1) & 3] = cvtpk(EL(C, n), EL(C, n + 1)); PIN(PWV(n)); }
    }
    if constexpr ((g & 1) && g >= 5 && g <= 19) {
      constexpr int kb = (g - 5) >> 2, hf = ((g - 5) >> 1) & 1;
      mb = MX3(mb, C.s[1][kb][2 * hf], C.s[1][kb][2 * hf + 1]); PIN(mb);
    }
    if constexpr (g == 22) {
      const bool ok = (ma <= THR) & (mb <= THR) & (!FIRST | ((ma >= -THR) & (mb >= -THR)));
      if (__builtin_expect(!__all(ok), 0)) {
#pragma unroll
        for (int qb = 0; qb < 2; ++qb) {
          float rm = qb ? mb : ma;
          rm = xmax4(rm);
          const float dl = FIRST ? rm : __builtin_fmaxf(rm, 0.f);
          st.mhat[qb] += dl;
#pragma unroll
          for (int kb = 0; kb < 4; ++kb)
#pragma unroll
            for (int r = 0; r < 4; ++r) C.s[qb][kb][r] -= dl;
#pragma unroll
          for (int r = 0; r < 4; ++r) st.negm[qb][r] = -st.mhat[qb];
          PIN(st.negm[qb]);
          if constexpr (!FIRST) { const float f_ = EX(-dl); if (qb == 0) fct0 = f_; else fct1 = f_; }
        }
        resc = true;
      }
    }
    if constexpr (g <= 21) {
      if constexpr (!FIRST) { constexpr int n = 10 + g; EL(P, n) = EX(EL(P, n)); PIN(ELV(P, n)); }
    } else { constexpr int n = g - 22; EL(C, n) = EX(EL(C, n)); PIN(ELV(C, n)); }
    SBAR();
    if constexpr (!FIRST && (g == 21 || g == 23 || g == 25 || g == 27)) {
      constexpr int qb = (g >> 1) & 1, w = g >= 24;
      st.lacc[qb] = MFMA16(st.ones, __builtin_bit_cast(bf16x8, st.pw[qb][w]), st.lacc[qb]); PIN(st.lacc[qb]);
      SBAR();
    }
  });
  if constexpr (!FIRST) {
    if (__builtin_expect(resc, 0)) {
#pragma unroll
      for (int d = 0; d < 8; ++d)
#pragma unroll
        for (int r = 0; r < 4; ++r) { st.o[0][d][r] *= fct0; st.o[1][d][r] *= fct1; }
#pragma unroll
      for (int r = 0; r < 4; ++r) { st.lacc[0][r] *= fct0; st.lacc[1][r] *= fct1; }
    }
  }
#undef KRD
#undef VRD
}

__global__ __launch_bounds__(512, 2)
void mha_k1(const char* __restrict__ x, const char* __restrict__ kc, const char* __restrict__ vc,
            const char* __restrict__ wq, const char* __restrict__ wo, char* ows, char* __restrict__ out) {
  extern __shared__ __attribute__((aligned(16))) char lds[];
  const int tid = threadIdx.x, lane = tid & 63;
  const int wid = __builtin_amdgcn_readfirstlane(tid >> 6);
  const int b = 4 * (blockIdx.x & 1) + ((blockIdx.x >> 3) & 3), h = 8 * ((blockIdx.x >> 1) & 3) + (blockIdx.x >> 5);
  const unsigned lds0 = (unsigned)(uintptr_t)lds;
  const lds_ptr L = (lds_ptr)lds;
  RSTAMP(62); STAMP(0);

  const char *pfx, *pfw;
  {
    const int j = blockIdx.x >> 3, half = lane >> 5, l5 = lane & 31;
    const int xrow = (b & 4) * SQ + 28 * j + (l5 < 28 ? l5 : 27);
    const int idx = 32 * j + l5;
    pfw = wq + (size_t)(64 * half + (idx >> 4)) * 8192 + (h & 24) * 256 + (idx & 15) * 128;
    pfx = x + (size_t)xrow * (DM * 2) + half * 128;
  }
  unsigned pfd0;
  const char* pfe = wid >= 4 ? x : (wid & 1) ? pfw + (size_t)(wid >> 1) * (128 * 8192) : pfx + (wid >> 1) * 256;
#define TOUCH(d, p) asm volatile("global_load_dword %0, %1, off " POLT : "=v"(d) : "v"(p) : "memory")
#define KEEP(d) asm volatile("" :: "v"(d) : "memory")
  const __amdgpu_buffer_rsrc_t srdW = __builtin_amdgcn_make_buffer_rsrc((void*)(wq + h * 256), (short)0, DM * 8192 - h * 256, 0x00020000);
  const unsigned pl = ((lane >> 2) & 3), dbl = lane >> 4, c4 = lane & 3;
  const unsigned wv0 = pl * 8192 + dbl * 64 + c4 * 16, wv1 = wv0 + 31744;
#define DMAW2(c) DMA2(POLW, lds0 + WQO(c) + 2048 * wid, wv0, wv1, srdW, (unsigned)(((c) * 64 + 8 * wid) * 8192))
  TOUCH(pfd0, pfe);
  const int ob = blockIdx.x & 7, oqt = blockIdx.x >> 6, ont = (blockIdx.x >> 3) & 7;
  const __amdgpu_buffer_rsrc_t srdWo = __builtin_amdgcn_make_buffer_rsrc((void*)(wo + ont * 128), (short)0, NH * DH * DM * 2 - ont * 128, 0x00020000);
  const unsigned ow0 = (4 * (lane >> 5) + ((lane >> 2) & 3)) * 1024 + ((lane >> 4) & 1) * 64 + (lane & 3) * 16;
  const unsigned ow1 = ow0 + 7168, ow2 = ow0 + 2 * 7168, ow3 = ow0 + 3 * 7168;
  unsigned long long* const epochp = (unsigned long long*)(ows + WS_EPOCH);
  unsigned long long* const flags = (unsigned long long*)(ows + WS_FLAGS);
  unsigned long long tag = 0;
  unsigned td = 0;
#define ROTM 0
#define ROTA 0
#define RR(i) (((i) + ROTM * ont + ROTA) & 15)
  constexpr int NA = P3NA;
  const int r32 = lane & 31, hi = lane >> 5, tq = (lane >> 2) & 3, tp = lane & 3, tblk = (lane >> 4) & 1;
  bf16x8 wf[NA][4];
#define RBP(i) ((lds_cptr)L + RB(i) + rbo)
#define WDMA(i) DMA4("", lds0 + (unsigned)(RB(i)) + 4096u * w3, ow0, ow1, ow2, ow3, srdWo, 32768u * w3 + (unsigned)(RR(i) * 256 * 1024))
#define W3() unsigned w3 = (unsigned)__builtin_amdgcn_readfirstlane(tid >> 6); asm volatile("s_nop 4" : "+s"(w3))
#define WREAD_NW(i) do { lds_cptr p_ = RBP(i); PIN(p_); _Pragma("unroll") for (int f_ = 0; f_ < 4; ++f_) \
      wf[(i) % NA][f_] = cat(ldstr(p_ + 1024 * (f_ >> 1) + 256 * (f_ & 1)), ldstr(p_ + 1024 * (f_ >> 1) + 2048 + 256 * (f_ & 1))); } while (0)
#define WRWAIT(i) asm volatile("s_waitcnt lgkmcnt(0)" : "+v"(wf[(i) % NA][0]), "+v"(wf[(i) % NA][1]), "+v"(wf[(i) % NA][2]), "+v"(wf[(i) % NA][3]) :: "memory")
#define WREAD(i) do { WREAD_NW(i); WRWAIT(i); } while (0)
#define RBO() (wid * 4096 + 512 * hi + 64 * tq + 32 * tblk + 8 * tp)
  if (wid == 7) {
    const __amdgpu_buffer_rsrc_t srdK = __builtin_amdgcn_make_buffer_rsrc((void*)(kc + (size_t)(b * NH + h) * (SK * DH * 2)), (short)0, SK * DH * 2, 0x00020000);
    const __amdgpu_buffer_rsrc_t srdV = __builtin_amdgcn_make_buffer_rsrc((void*)(vc + (size_t)(b * NH + h) * (SK * DH * 2)), (short)0, SK * DH * 2, 0x00020000);
    const unsigned wv2 = wv0 + 2 * 31744, wv3 = wv0 + 3 * 31744;
    const unsigned kvA = pl * 256 + dbl * 64 + c4 * 16, kvB = pl * 256 + dbl * 64 + ((c4 ^ 2) * 16);
#define DMAW(c) do { _Pragma("unroll") for (int i_ = 0; i_ < 4; ++i_) DMA4(POLW, lds0 + (unsigned)(WQO(c) + 4096 * i_), wv0, wv1, wv2, wv3, srdW, (unsigned)(((c) * 64 + 16 * i_) * 8192)); } while (0)
#define DMAK_(t, so) do { _Pragma("unroll") for (int i_ = 0; i_ < 4; ++i_) DMA4("nt", lds0 + (unsigned)((so) + 4096 * i_), kvA, kvA, kvB, kvB, srdK, (unsigned)((t) * 16384 + 4096 * i_)); } while (0)
#define DMAV_(t, so) do { _Pragma("unroll") for (int i_ = 0; i_ < 4; ++i_) DMA4("nt", lds0 + (unsigned)((so) + 16384 + 4096 * i_), kvA, kvB, kvA, kvB, srdV, (unsigned)((t) * 16384 + 4096 * i_)); } while (0)
#define DMAK(t) DMAK_(t, SLOT(t))
#define DMAV(t) DMAV_(t, SLOT((t) + 1))
    const char* tw = wo + (size_t)(blockIdx.x >> 3) * 8192 + lane * 128;
#define TW(r) do { const char* p_ = tw + (size_t)((((r) + ROTM * (lane & 7) + ROTA) & 15) * 262144); asm volatile("global_load_dword %0, %1, off" : "+v"(td) : "v"(p_) : "memory"); } while (0)
    unsigned t4, t5, t6, t7;
    asm volatile("global_load_dwordx2 %0, %1, off sc1" : "=v"(tag) : "v"(epochp) : "memory");
#define RW(i) do { _Pragma("unroll") for (int w_ = 0; w_ < 8; ++w_) DMA4("", lds0 + (unsigned)(RB(i) + 4096 * w_), ow0, ow1, ow2, ow3, srdWo, (unsigned)((RR(i) * 256 + 32 * w_) * 1024)); } while (0)
    DMAW2(0);
    BARRIER();
    DMAW(1);
    WAITV(16); BARRIER();
    DMAW(2);
    WAITV(16); BARRIER();
    DMAW(3);
    { const char* p4 = pfx + 512; const char* p5 = pfw + (size_t)2 * 128 * 8192; const char* p6 = pfx + 768; const char* p7 = pfw + (size_t)3 * 128 * 8192;
      TOUCH(t4, p4); TOUCH(t5, p5); TOUCH(t6, p6); TOUCH(t7, p7); }
    WAITV(20); BARRIER();
    WAITV(4); BARRIER();
    DMAW(4); DMAW(5);
    WAITV(16); BARRIER();
    KEEP(pfd0); KEEP(t4); KEEP(t5); KEEP(t6); KEEP(t7);
    DMAW(6);
    WAITV(16); BARRIER();
    DMAW(7);
    WAITV(16); BARRIER();
    DMAK(0); DMAK(1); DMAV(0);
    WAITV(48); BARRIER();
    BARRIER();
    DMAK(2);
    WAITV(32); BARRIER();
    DMAV(1); DMAK(3); DMAV(2);
    WAITV(48); BARRIER();
    DMAK(4); DMAV(3); DMAK(5); DMAV(4);
    WAITV(63); BARRIER();
    {
      int so = SLOT(1);
      for (int t = 1; t < 11; ++t) {
        DMAK_(t + 5, so); DMAV_(t + 4, so);
        so = (so == 131072) ? 0 : so + 32768;
        WAITV(63);
        BARRIER();
      }
    }
    DMAV(15); TW(0); TW(1); TW(2); TW(3);
    asm volatile("" : "+v"(tag));
    tag = (((unsigned long long)(unsigned)__builtin_amdgcn_readfirstlane((unsigned)(tag >> 32)) << 32) | (unsigned)__builtin_amdgcn_readfirstlane((unsigned)tag)) + 1;
    if (lane == 0) { *(__attribute__((address_space(3))) unsigned*)(L + PUBW) = 0u; *(__attribute__((address_space(3))) unsigned long long*)(L + PUBW + 8) = tag; }
    asm volatile("s_waitcnt lgkmcnt(0)" ::: "memory");
    WAITV(63);
    BARRIER();
    TW(5); TW(6); TW(7); TW(4);
    WAITV(40);
    RW(0);
    BARRIER();
    RW(1); TW(8); TW(10); TW(11); TW(12);
    WAITV(63);
    BARRIER();
    RW(2); TW(9); TW(13); TW(14); TW(15);
    WAITV(4);
    BARRIER();
    {
      const int rbo = RBO();
      WREAD(0); WREAD(1); WREAD(2);
    }
    BARRIER();
    RW(3); RW(5); RW(6); RW(7);
    WAITV(0);
#undef TW
#undef RW
#undef DMAW
#undef DMAK
#undef DMAV
  } else {
  const __amdgpu_buffer_rsrc_t srdX = __builtin_amdgcn_make_buffer_rsrc((void*)(x + (size_t)(b * SQ + wid * 32) * (DM * 2)), (short)0, 32 * DM * 2, 0x00020000);
  const unsigned xs = (lane >> 3) * 1024 + (((lane & 7) ^ (lane >> 4)) * 16);
  const unsigned xv0 = xs, xv1 = (xs ^ 64) + 7168, xv2 = xs + 2 * 7168, xv3 = (xs ^ 64) + 3 * 7168;
#define XDMA(c) DMA4(POLW, lds0 + (unsigned)XO(c) + 4096u * wid, xv0, xv1, xv2, xv3, srdX, (unsigned)(128 * (c)))
  const unsigned xrb = (unsigned)(wid * 4096 + r32 * 128 + ((hi ^ ((r32 >> 1) & 7)) * 16));
  bf16x8 xf[2][4];
#define XRD(c, k_) xf[(c) & 1][k_] = ldsrd128((lds_cptr)L + ((xrb ^ (unsigned)((k_) << 5)) + XO(c)))
  DMAW2(0); XDMA(0);
  BARRIER();
  XDMA(1);
  const lds_cptr wrb = (lds_cptr)L + 2048 * hi + 64 * tq + 32 * (tp & 1) + 16 * tblk + 8 * (tp >> 1);
  lds_cptr wrb2 = wrb + 131072; PIN(wrb2);
#define WRB(c) ((WQO(c) < 65536 ? wrb : wrb2) + (WQO(c) & 65535))
#define WRD(c, m) do { wlo[m] = ldstr(WRB(c) + 4096 * ((m) >> 2) + 256 * ((m) & 3)); whi[m] = ldstr(WRB(c) + 4096 * ((m) >> 2) + 1024 + 256 * ((m) & 3)); } while (0)
  AttnState st;
  f32x16 acc[4];
#pragma unroll
  for (int i = 0; i < 4; ++i) acc[i] = f32x16{};
  s16x4 wlo[16], whi[16];
  WAITV(4);
  XRD(0, 0); XRD(0, 1); XRD(0, 2); XRD(0, 3);
  STAMP(1);
  BARRIER();
  STAMP(2);
#pragma unroll
  for (int m = 0; m < PD; ++m) WRD(0, m);
#define QCVT(nb) do { u32x4 x_, y_; \
    _Pragma("unroll") for (int e_ = 0; e_ < 4; ++e_) { \
      const unsigned lo_ = cvtpk(acc[nb][2 * e_] * QSCALE, acc[nb][2 * e_ + 1] * QSCALE), hi_ = cvtpk(acc[nb][8 + 2 * e_] * QSCALE, acc[nb][9 + 2 * e_] * QSCALE); \
      auto r_ = __builtin_amdgcn_permlane16_swap(lo_, hi_, false, false); x_[e_] = r_[0]; y_[e_] = r_[1]; } \
    st.q[0][nb] = __builtin_bit_cast(bf16x8, x_); st.q[1][nb] = __builtin_bit_cast(bf16x8, y_); PIN(st.q[0][nb]); PIN(st.q[1][nb]); } while (0)
  static_for<128>([&](auto I) __attribute__((always_inline)) {
    constexpr int c = I.value >> 4, mm = I.value & 15;
    constexpr int m = (c == 7) ? (4 * (mm & 3) + (mm >> 2)) : mm;
    acc[m & 3] = MFMA(cat(wlo[m], whi[m]), xf[c & 1][m >> 2], acc[m & 3]);
    if constexpr (c == 0 && mm + PD == 16) { BARRIER(); }
    if constexpr (mm + PD < 16) { constexpr int f = mm + PD, ff = (c == 7) ? (4 * (f & 3) + (f >> 2)) : f; WRD(c, ff); }
    else if constexpr (c < 7) { constexpr int f = mm + PD - 16, ff = (c + 1 == 7) ? (4 * (f & 3) + (f >> 2)) : f; WRD(c + 1, ff); }
    if constexpr (c == 0 && mm == 0) { XDMA(2); }
    if constexpr (c == 0 && mm == 2) { XDMA(3); }
    if constexpr (c == 2 && mm == 8) { XDMA(4); }
    if constexpr (c == 3 && mm == 0) { XDMA(5); }
    if constexpr (c == 3 && mm == 8) { XDMA(6); }
    if constexpr (c == 4 && mm == 8) { XDMA(7); }
    if constexpr (c < 7) {
      if constexpr (mm == 4) { WAITV(c == 0 ? 8 : (c == 2 || c == 6) ? 0 : 4); }
      if constexpr (mm == 5 && c == 1) { KEEP(pfd0); }
      if constexpr (mm >= 4 && mm < 8) { XRD(c + 1, mm - 4); }
    }
    if constexpr (c == 5) { st.o[mm >> 3][mm & 7] = f32x4{}; PIN(st.o[mm >> 3][mm & 7]); }
    if constexpr (c == 7 && (mm == 6 || mm == 10 || mm == 14)) { QCVT((mm - 6) >> 2); }
    SBAR();
    if constexpr (mm == 15) { BARRIER(); STAMP(3 + c); SBAR(); }
  });
  LSTAMP(58);
#undef XDMA
#undef XRD
#undef WRD
  const int l16 = lane & 15, g4 = lane >> 4;
  const lds_cptr kb = (lds_cptr)L + 1024 * (l16 >> 2) + 64 * (l16 & 3) + 16 * (g4 ^ ((l16 >> 2) & 2));
  const lds_cptr vb0 = (lds_cptr)L + 16384 + 1024 * g4 + 64 * tq + 32 * (g4 & 1) + 8 * tp;
  const lds_cptr vb1 = (lds_cptr)L + 16384 + 1024 * g4 + 64 * tq + 32 * (1 - (g4 & 1)) + 8 * tp;
#pragma unroll
  for (int m = 0; m < PD; ++m) st.kf[m] = ldsrd128(kb + SLOT(0) + 4096 * (m >> 2) + 256 * (m & 3));
  QCVT(3);
#undef QCVT
  st.negm[0] = f32x4{}; st.negm[1] = f32x4{}; PIN(st.negm[0]); PIN(st.negm[1]);
  st.mhat[0] = st.mhat[1] = 0.f; st.lacc[0] = f32x4{}; st.lacc[1] = f32x4{};
  st.ones = bf16x8{0x3F80, 0x3F80, 0x3F80, 0x3F80, 0x3F80, 0x3F80, 0x3F80, 0x3F80}; PIN(st.ones);

  STile A, B;
  attn_step<true>(st, A, B, kb + SLOT(0), vb0 + SLOT(0), vb1 + SLOT(0), kb + SLOT(1), wid >= 4);
  STAMP(11);
  BARRIER();
  STAMP(12);
  int s0 = SLOT(1);
  for (int t = 1; t < 15; t += 2) {
    const int s1 = (s0 == 131072) ? 0 : s0 + 32768, s2 = (s1 == 131072) ? 0 : s1 + 32768;
    attn_step<false>(st, B, A, kb + s0, vb0 + s0, vb1 + s0, kb + s1, wid >= 4);
    STAMP(11 + 2 * t);
    BARRIER();
    STAMP(12 + 2 * t);
    attn_step<false>(st, A, B, kb + s1, vb0 + s1, vb1 + s1, kb + s2, wid >= 4);
    STAMP(13 + 2 * t);
    BARRIER();
    STAMP(14 + 2 * t);
    s0 = s2;
  }
  attn_step<false>(st, B, A, kb + SLOT(15), vb0 + SLOT(15), vb1 + SLOT(15), kb + SLOT(15), wid >= 4);
  STAMP(41);
  BARRIER();
  STAMP(42); LSTAMP(59);
  {
    lds_cptr vd0 = vb0 + SLOT(16), vd1 = vb1 + SLOT(16); PIN(vd0); PIN(vd1);
#define VRD(m) do { st.vlo[m] = ldstr((((m) & 2) ? vd1 : vd0) + 8192 * ((m) & 1) + 256 * ((m) >> 2)); st.vhi[m] = ldstr((((m) & 2) ? vd1 : vd0) + 8192 * ((m) & 1) + 4096 + 256 * ((m) >> 2)); } while (0)
    const int rbo = RBO();
    WREAD(0); WREAD(1); WREAD(2);
    BARRIER();
#pragma unroll
    for (int m = 0; m < 8; ++m) VRD(m);
    static_for<22>([&](auto I) __attribute__((always_inline)) { constexpr int n = 10 + I.value; EL(B, n) = EX(EL(B, n)); });
    static_for<12>([&](auto I) __attribute__((always_inline)) { constexpr int n = 8 + 2 * I.value; PWV(n)[(n >> 1) & 3] = cvtpk(EL(B, n), EL(B, n + 1)); });
#pragma unroll
    for (int w = 0; w < 2; ++w) {
      st.lacc[0] = MFMA16(st.ones, __builtin_bit_cast(bf16x8, st.pw[0][w]), st.lacc[0]);
      st.lacc[1] = MFMA16(st.ones, __builtin_bit_cast(bf16x8, st.pw[1][w]), st.lacc[1]);
    }
    const float inv0 = 1.0f / st.lacc[0][0], inv1 = 1.0f / st.lacc[1][0];
    const __amdgpu_buffer_rsrc_t srdOs = __builtin_amdgcn_make_buffer_rsrc((void*)(ows + (size_t)(b * NH + h) * 57344), (short)0, 57344, 0x00020000);
    const int qr0 = 32 * wid + l16, vk0 = qr0 / 28, qr1 = qr0 + 16, vk1 = qr1 / 28;
    const unsigned osv0 = vk0 * 7168 + (g4 >> 1) * 896 + ((g4 & 1) * 28 + qr0 - 28 * vk0) * 16, osv1 = vk1 * 7168 + (g4 >> 1) * 896 + ((g4 & 1) * 28 + qr1 - 28 * vk1) * 16;
#define OSTORE(u) do { \
      u32x4 w0_ = {cvtpk(st.o[0][2 * (u)][0] * inv0, st.o[0][2 * (u)][1] * inv0), cvtpk(st.o[0][2 * (u)][2] * inv0, st.o[0][2 * (u)][3] * inv0), \
                   cvtpk(st.o[0][2 * (u) + 1][0] * inv0, st.o[0][2 * (u) + 1][1] * inv0), cvtpk(st.o[0][2 * (u) + 1][2] * inv0, st.o[0][2 * (u) + 1][3] * inv0)}; \
      __builtin_amdgcn_raw_buffer_store_b128(w0_, srdOs, osv0 + (u) * 1792, 0, 16); \
      u32x4 w1_ = {cvtpk(st.o[1][2 * (u)][0] * inv1, st.o[1][2 * (u)][1] * inv1), cvtpk(st.o[1][2 * (u)][2] * inv1, st.o[1][2 * (u)][3] * inv1), \
                   cvtpk(st.o[1][2 * (u) + 1][0] * inv1, st.o[1][2 * (u) + 1][1] * inv1), cvtpk(st.o[1][2 * (u) + 1][2] * inv1, st.o[1][2 * (u) + 1][3] * inv1)}; \
      __builtin_amdgcn_raw_buffer_store_b128(w1_, srdOs, osv1 + (u) * 1792, 0, 16); } while (0)
    SBAR();
#pragma unroll
    for (int u = 0; u < 4; ++u) {
#pragma unroll
      for (int k = 0; k < 4; ++k) {
        const int m = 4 * u + k, db = m >> 1, w = m & 1;
        st.o[0][db] = MFMA16(cat(st.vlo[m], st.vhi[m]), __builtin_bit_cast(bf16x8, st.pw[0][w]), st.o[0][db]);
        st.o[1][db] = MFMA16(cat(st.vlo[m], st.vhi[m]), __builtin_bit_cast(bf16x8, st.pw[1][w]), st.o[1][db]);
        if (u < 2) { VRD(m + 8); }
      }
      SBAR();
      if (u >= 1) { OSTORE(u - 1); }
      SBAR();
    }
    STAMP(43);
    OSTORE(3);
    STAMP(44);
#undef VRD
#undef OSTORE
    WAITV(0);
    if (lane == 0) {
      const unsigned old = __hip_atomic_fetch_add((__attribute__((address_space(3))) unsigned*)(L + PUBW), 1u, __ATOMIC_RELAXED, __HIP_MEMORY_SCOPE_WORKGROUP);
      if (old == 6) __hip_atomic_store(flags + b * NH + h, *(__attribute__((address_space(3))) unsigned long long*)(L + PUBW + 8), __ATOMIC_RELAXED, __HIP_MEMORY_SCOPE_AGENT);
    }
    asm volatile("s_waitcnt lgkmcnt(0)" ::: "memory");
    STAMP(50);
  }
  }

  {
    static_assert(NA == 8, "prefetch sequences above are written for NA = 8");
    const int rbo = RBO(); W3();
    const __amdgpu_buffer_rsrc_t srdO = __builtin_amdgcn_make_buffer_rsrc((void*)(ows + (size_t)ob * (NH * 57344)), (short)0, NH * 57344, 0x00020000);
    const unsigned ov0 = (hi * 28 + (r32 < 28 ? r32 : 27)) * 16;
    const unsigned osb = (w3 >> 2) * 57344 + (w3 & 3) * 1792 + oqt * 2 * 7168;
    u32x4 of[3][4];
    f32x16 acc[2][2];
#pragma unroll
    for (int i = 0; i < 2; ++i) { acc[i][0] = f32x16{}; acc[i][1] = f32x16{}; }
#define OLOAD(i) do { const unsigned so_ = osb + (unsigned)(RR(i) * 2 * 57344), so1_ = so_ + 7168u; \
      BLOAD("sc1", of[(i) % 3][0], ov0, srdO, so_, 0); BLOAD("sc1", of[(i) % 3][1], ov0, srdO, so1_, 0); \
      BLOAD("sc1", of[(i) % 3][2], ov0, srdO, so_, 896); BLOAD("sc1", of[(i) % 3][3], ov0, srdO, so1_, 896); } while (0)
    BARRIER();
    LSTAMP(57);
    STAMP(51);
    WDMA(4);
    WREAD(3); WDMA(8); WREAD(5); WDMA(10); WREAD(6); WDMA(11); WREAD(7); WDMA(12);
    WAITV(16); WREAD(4); WDMA(9);
    STAMP(52);
    if (w3 == 7) {
      const unsigned long long* fp = flags + ob * NH + r32;
      for (unsigned spins = 0; spins < (1u << 18); ++spins) {
        const unsigned long long v = __hip_atomic_load(fp, __ATOMIC_RELAXED, __HIP_MEMORY_SCOPE_AGENT);
        if (__all(v == tag)) break;
        __builtin_amdgcn_s_sleep(1);
      }
      KEEP(td);
    }
    STAMP(53);
    BARRIER();
    LSTAMP(60);
    STAMP(54);
    OLOAD(0); OLOAD(1);
#pragma unroll
    for (int i = 0; i < 16; ++i) {
      const int cnt = (i < 15 ? 4 : 0) + ((i - 1 >= 1 && i - 1 <= 3) ? 4 : 0);
      WAITV4(cnt, of[i % 3][0], of[i % 3][1], of[i % 3][2], of[i % 3][3]);
      if (i >= 1 && i <= 3) { WRWAIT(i - 1 + NA); WDMA(i + 12); }
      if (i + 2 < 16) { OLOAD(i + 2); }
      SBAR();
#pragma unroll
      for (int ks = 0; ks < 2; ++ks)
#pragma unroll
        for (int nb = 0; nb < 2; ++nb)
#pragma unroll
          for (int vb = 0; vb < 2; ++vb)
            acc[nb][vb] = MFMA(wf[i % NA][2 * ks + nb], __builtin_bit_cast(bf16x8, of[i % 3][2 * ks + vb]), acc[nb][vb]);
      SBAR();
      if (i + NA < 16) { WREAD_NW(i + NA); }
      SBAR();
    }
    STAMP(55);
#undef WDMA
#undef WREAD
#undef OLOAD
#undef RBP
#pragma unroll
    for (int vb = 0; vb < 2; ++vb)
#pragma unroll
      for (int nb = 0; nb < 2; ++nb)
#pragma unroll
        for (int rg = 0; rg < 4; ++rg) {
          const int q = 32 * vb + r32, slot = 8 * nb + 2 * rg + hi;
          f32x4 v = {acc[nb][vb][4 * rg], acc[nb][vb][4 * rg + 1], acc[nb][vb][4 * rg + 2], acc[nb][vb][4 * rg + 3]};
          *(__attribute__((address_space(3))) f32x4*)(L + (q >> 4) * 32768 + wid * 4096 + (q & 15) * 256 + ((slot ^ (q & 15)) * 16)) = v;
        }
    asm volatile("s_waitcnt lgkmcnt(0)" ::: "memory");
    BARRIER();
    STAMP(56); LSTAMP(61);
    const int q = tid >> 3, pc = tid & 7;
    f32x4 s0 = {0, 0, 0, 0}, s1 = {0, 0, 0, 0};
#pragma unroll
    for (int w = 0; w < 8; ++w) {
      s0 += *(const __attribute__((address_space(3))) f32x4*)(L + (q >> 4) * 32768 + w * 4096 + (q & 15) * 256 + (((2 * pc) ^ (q & 15)) * 16));
      s1 += *(const __attribute__((address_space(3))) f32x4*)(L + (q >> 4) * 32768 + w * 4096 + (q & 15) * 256 + (((2 * pc + 1) ^ (q & 15)) * 16));
    }
    u32x4 w = {cvtpk(s0[0], s0[1]), cvtpk(s0[2], s0[3]), cvtpk(s1[0], s1[1]), cvtpk(s1[2], s1[3])};
    if ((q & 31) < 28)
      *(u32x4*)(out + (size_t)(ob * SQ + 56 * oqt + 28 * (q >> 5) + (q & 31)) * (DM * 2) + ont * 128 + pc * 16) = w;
    if (blockIdx.x == 0 && wid == 7) {
      for (unsigned spins = 0; spins < (1u << 18); ++spins) {
        bool ok = true;
#pragma unroll
        for (int k = 0; k < 4; ++k) ok &= __hip_atomic_load(flags + 64 * k + lane, __ATOMIC_RELAXED, __HIP_MEMORY_SCOPE_AGENT) == tag;
        if (__all(ok)) break;
        __builtin_amdgcn_s_sleep(2);
      }
      if (lane == 0) __hip_atomic_store(epochp, tag, __ATOMIC_RELAXED, __HIP_MEMORY_SCOPE_AGENT);
    }
  }
  STAMP(40); RSTAMP(63);
  STFLUSH(ows + (64 << 20));
}

extern "C" void kernel_launch(void* const* d_in, const int* in_sizes, int n_in,
                              void* d_out, int out_size, void* d_ws, size_t ws_size,
                              hipStream_t stream) {
  (void)in_sizes; (void)n_in; (void)out_size; (void)ws_size;
  (void)hipFuncSetAttribute((const void*)mha_k1, hipFuncAttributeMaxDynamicSharedMemorySize, K1_LDS);
  mha_k1<<<256, 512, K1_LDS, stream>>>((const char*)d_in[0], (const char*)d_in[1], (const char*)d_in[2], (const char*)d_in[3], (const char*)d_in[4], (char*)d_ws, (char*)d_out);
}
```

```cpp
#include <hip/hip_runtime.h>
#include <hip/hip_bf16.h>
#include <stdint.h>
#include <utility>
#include <type_traits>

typedef short    bf16x8 __attribute__((ext_vector_type(8)));
typedef short    s16x4  __attribute__((ext_vector_type(4)));
typedef float    f32x16 __attribute__((ext_vector_type(16)));
typedef float    f32x4  __attribute__((ext_vector_type(4)));
typedef float    f32x2  __attribute__((ext_vector_type(2)));
typedef __bf16   bf16x2 __attribute__((ext_vector_type(2)));
typedef unsigned u32x4  __attribute__((ext_vector_type(4)));
typedef __attribute__((address_space(3))) char* lds_ptr;
typedef __attribute__((address_space(3))) const char* lds_cptr;

#define DI __device__ __forceinline__
#define SBAR() __builtin_amdgcn_sched_barrier(0)
#define PIN(x) asm volatile("" : "+v"(x))
#define MFMA(a, b, c) __builtin_amdgcn_mfma_f32_32x32x16_bf16(a, b, c, 0, 0, 0)
#define BARRIER() asm volatile("s_barrier" ::: "memory")

#define STAMP(i)
#define RSTAMP(i)
#define LSTAMP(i)
#define STFLUSH(dbg)
#define POLT "nt"
#define POLW ""
#define POLWO ""
#define POLT2 ""
#define POLO "sc1"
#define P3NA 8
#define TSCHED 3
#define PRIO_TOGGLE 21
#define EARLYKV 0
#define TLBW 1
#define MAPK1 1
constexpr int NB = 8, SQ = 224, DM = 512, NH = 32, SK = 1024, DH = 128;
constexpr float QSCALE = 0.088388347648318440f * 1.4426950408889634f;
constexpr float THR = 8.0f;

template <int... Is, class F> DI void static_for_(std::integer_sequence<int, Is...>, F&& f) { (f(std::integral_constant<int, Is>{}), ...); }
template <int N, class F> DI void static_for(F&& f) { static_for_(std::make_integer_sequence<int, N>{}, f); }
DI unsigned cvtpk(float lo, float hi) { f32x2 v = {lo, hi}; bf16x2 b = __builtin_convertvector(v, bf16x2); return __builtin_bit_cast(unsigned, b); }
DI bf16x8 ldsrd128(lds_cptr p) { return *(const __attribute__((address_space(3))) bf16x8*)p; }
DI s16x4 ldstr(lds_cptr p) { return __builtin_bit_cast(s16x4, __builtin_amdgcn_ds_read_tr16_b64_v4i16((__attribute__((address_space(3))) s16x4*)p)); }
DI bf16x8 cat(s16x4 a, s16x4 b) { return __builtin_shufflevector(a, b, 0, 1, 2, 3, 4, 5, 6, 7); }
DI void ldswr128(lds_ptr p, u32x4 v) { *(__attribute__((address_space(3))) u32x4*)p = v; }
#define MX3(a, b, c) __builtin_fmaxf(__builtin_fmaxf((a), (b)), (c))

#define DMA4(POL, m0v, v0, v1, v2, v3, srd, soff)                                                     \
  asm volatile("s_mov_b32 m0, %0\n\ts_nop 0\n\t"                                                      \
               "buffer_load_dwordx4 %1, %5, %6 offen " POL " lds\n\t"                                 \
               "buffer_load_dwordx4 %2, %5, %6 offen offset:1024 " POL " lds\n\t"                     \
               "buffer_load_dwordx4 %3, %5, %6 offen offset:2048 " POL " lds\n\t"                     \
               "buffer_load_dwordx4 %4, %5, %6 offen offset:3072 " POL " lds"                         \
               :: "s"(m0v), "v"(v0), "v"(v1), "v"(v2), "v"(v3), "s"(srd), "s"(soff) : "memory")
#define DMA2(POL, m0v, v0, v1, srd, soff)                                                             \
  asm volatile("s_mov_b32 m0, %0\n\ts_nop 0\n\t"                                                      \
               "buffer_load_dwordx4 %1, %3, %4 offen " POL " lds\n\t"                                 \
               "buffer_load_dwordx4 %2, %3, %4 offen offset:1024 " POL " lds"                         \
               :: "s"(m0v), "v"(v0), "v"(v1), "s"(srd), "s"(soff) : "memory")
#define BLOAD(POL, dst, voff, srd, soff, imm)                                                         \
  asm volatile("buffer_load_dwordx4 %0, %1, %2, %3 offen offset:%4 " POL                              \
               : "=v"(dst) : "v"(voff), "s"(srd), "s"(soff), "i"(imm) : "memory")
#define WAITV(n) asm volatile("s_waitcnt vmcnt(%0)" :: "i"(n) : "memory")
#define WAITV4(n, a, b, c, d) asm volatile("s_waitcnt vmcnt(%4)" : "+v"(a), "+v"(b), "+v"(c), "+v"(d) : "i"(n) : "memory")

constexpr int K1_LDS = 163840;
constexpr int OHS = 57344 + 128;
constexpr size_t WS_FLAGS = (size_t)32 << 20, WS_EPOCH = WS_FLAGS + 4096;
DI constexpr int SLOT(int t) { return (t % 5) * 32768; }
DI constexpr int RB(int i) { return ((i + 2) % 5) * 32768; }
constexpr int PUBW = 32768;
DI constexpr int WQO(int c) { return (c & 3) == 0 ? 0 : (c & 3) == 1 ? 16384 : (c & 3) == 2 ? 131072 : 147456; }
DI constexpr int XO(int c) { return (c % 3) == 0 ? 65536 : (c % 3) == 1 ? 98304 : 32768; }

#define MFMA16(a, b, c) __builtin_amdgcn_mfma_f32_16x16x32_bf16(a, b, c, 0, 0, 0)
struct AttnState {
  f32x4 o[2][8];
  bf16x8 q[2][4];
  f32x4 negm[2];
  float mhat[2];
  f32x4 lacc[2];
  bf16x8 ones;
  bf16x8 kf[16];
  s16x4 vlo[16], vhi[16];
  u32x4 pw[2][2];
};
struct STile { f32x4 s[2][4]; };
DI float xmax4(float v) {
  auto a = __builtin_amdgcn_permlane16_swap(__float_as_uint(v), __float_as_uint(v), false, false);
  v = __builtin_fmaxf(__uint_as_float(a[0]), __uint_as_float(a[1]));
  auto c = __builtin_amdgcn_permlane32_swap(__float_as_uint(v), __float_as_uint(v), false, false);
  return __builtin_fmaxf(__uint_as_float(c[0]), __uint_as_float(c[1]));
}
DI float xsum4(float v) {
  auto a = __builtin_amdgcn_permlane16_swap(__float_as_uint(v), __float_as_uint(v), false, false);
  v = __uint_as_float(a[0]) + __uint_as_float(a[1]);
  auto c = __builtin_amdgcn_permlane32_swap(__float_as_uint(v), __float_as_uint(v), false, false);
  return __uint_as_float(c[0]) + __uint_as_float(c[1]);
}

#define PDV 5
constexpr int PD = PDV;
#define EL(T, n) (T).s[((n) >> 3) & 1][2 * ((n) >> 4) + (((n) >> 2) & 1)][(n) & 3]
#define ELV(T, n) (T).s[((n) >> 3) & 1][2 * ((n) >> 4) + (((n) >> 2) & 1)]
#define PWV(n) st.pw[((n) >> 3) & 1][(n) >> 4]
#define EX(v) __builtin_amdgcn_exp2f(v)
template <bool FIRST>
DI void attn_step(AttnState& st, STile& C, STile& P, lds_cptr kp, lds_cptr vp0, lds_cptr vp1, lds_cptr kn, const bool young) {
#define KRD(m, base) st.kf[m] = ldsrd128((base) + 4096 * ((m) >> 2) + 256 * ((m) & 3))
#define VRD(m) do { st.vlo[m] = ldstr((((m) & 1) ? vp1 : vp0) + 8192 * ((m) >> 3) + 256 * (((m) & 7) >> 1)); \
                    st.vhi[m] = ldstr((((m) & 1) ? vp1 : vp0) + 8192 * ((m) >> 3) + 4096 + 256 * (((m) & 7) >> 1)); } while (0)
  float ma = -3.0e38f, mb = -3.0e38f, fct0 = 1.f, fct1 = 1.f;
  bool resc = false;
  SBAR();
  static_for<32>([&](auto I) __attribute__((always_inline)) {
    constexpr int g = I.value;
    if constexpr (g == 0) { if (young) __builtin_amdgcn_s_setprio(1); }
    if constexpr (g == PRIO_TOGGLE) { __builtin_amdgcn_s_setprio(0); }
    if constexpr (g < 16) {
      constexpr int kb = g >> 2, u = g & 3;
      if constexpr (u == 0) C.s[0][kb] = MFMA16(st.kf[g], st.q[0][0], st.negm[0]); else C.s[0][kb] = MFMA16(st.kf[g], st.q[0][u], C.s[0][kb]);
      PIN(C.s[0][kb]);
    } else if constexpr (!FIRST) {
      constexpr int m = g - 16;
      st.o[0][m & 7] = MFMA16(cat(st.vlo[m], st.vhi[m]), __builtin_bit_cast(bf16x8, st.pw[0][m >> 3]), st.o[0][m & 7]);
      PIN(st.o[0][m & 7]);
    }
    SBAR();
    {
      constexpr int f = g + PD;
      if constexpr (f < 16) { KRD(f, kp); }
      else if constexpr (f < 32) { if constexpr (!FIRST) { VRD(f - 16); } }
      else { KRD(f - 32, kn); }
    }
    if constexpr ((g & 1) && g >= 5 && g <= 19) {
      constexpr int kb = (g - 5) >> 2, hf = ((g - 5) >> 1) & 1;
      ma = MX3(ma, C.s[0][kb][2 * hf], C.s[0][kb][2 * hf + 1]); PIN(ma);
    }
    SBAR();
    if constexpr (g < 16) {
      constexpr int kb = g >> 2, u = g & 3;
      if constexpr (u == 0) C.s[1][kb] = MFMA16(st.kf[g], st.q[1][0], st.negm[1]); else C.s[1][kb] = MFMA16(st.kf[g], st.q[1][u], C.s[1][kb]);
      PIN(C.s[1][kb]);
    } else if constexpr (!FIRST) {
      constexpr int m = g - 16;
      st.o[1][m & 7] = MFMA16(cat(st.vlo[m], st.vhi[m]), __builtin_bit_cast(bf16x8, st.pw[1][m >> 3]), st.o[1][m & 7]);
      PIN(st.o[1][m & 7]);
    }
    SBAR();
    if constexpr ((g & 1) == 0) {
      if constexpr (g <= 22) {
        if constexpr (!FIRST) { constexpr int n = 8 + g; PWV(n)[(n >> 1) & 3] = cvtpk(EL(P, n), EL(P, n + 1)); PIN(PWV(n)); }
      } else { constexpr int n = g - 24; PWV(n)[(n >> 1) & 3] = cvtpk(EL(C, n), EL(C, n + 1)); PIN(PWV(n)); }
    }
    if constexpr ((g & 1) && g >= 5 && g <= 19) {
      constexpr int kb = (g - 5) >> 2, hf = ((g - 5) >> 1) & 1;
      mb = MX3(mb, C.s[1][kb][2 * hf], C.s[1][kb][2 * hf + 1]); PIN(mb);
    }
    if constexpr (g == 22) {
      const bool ok = (ma <= THR) & (mb <= THR) & (!FIRST | ((ma >= -THR) & (mb >= -THR)));
      if (__builtin_expect(!__all(ok), 0)) {
#pragma unroll
        for (int qb = 0; qb < 2; ++qb) {
          float rm = qb ? mb : ma;
          rm = xmax4(rm);
          const float dl = FIRST ? rm : __builtin_fmaxf(rm, 0.f);
          st.mhat[qb] += dl;
#pragma unroll
          for (int kb = 0; kb < 4; ++kb)
#pragma unroll
            for (int r = 0; r < 4; ++r) C.s[qb][kb][r] -= dl;
#pragma unroll
          for (int r = 0; r < 4; ++r) st.negm[qb][r] = -st.mhat[qb];
          PIN(st.negm[qb]);
          if constexpr (!FIRST) { const float f_ = EX(-dl); if (qb == 0) fct0 = f_; else fct1 = f_; }
        }
        resc = true;
      }
    }
    if constexpr (g <= 21) {
      if constexpr (!FIRST) { constexpr int n = 10 + g; EL(P, n) = EX(EL(P, n)); PIN(ELV(P, n)); }
    } else { constexpr int n = g - 22; EL(C, n) = EX(EL(C, n)); PIN(ELV(C, n)); }
    SBAR();
    if constexpr (!FIRST && (g == 21 || g == 23 || g == 25 || g == 27)) {
      constexpr int qb = (g >> 1) & 1, w = g >= 24;
      st.lacc[qb] = MFMA16(st.ones, __builtin_bit_cast(bf16x8, st.pw[qb][w]), st.lacc[qb]); PIN(st.lacc[qb]);
      SBAR();
    }
  });
  if constexpr (!FIRST) {
    if (__builtin_expect(resc, 0)) {
#pragma unroll
      for (int d = 0; d < 8; ++d)
#pragma unroll
        for (int r = 0; r < 4; ++r) { st.o[0][d][r] *= fct0; st.o[1][d][r] *= fct1; }
#pragma unroll
      for (int r = 0; r < 4; ++r) { st.lacc[0][r] *= fct0; st.lacc[1][r] *= fct1; }
    }
  }
#undef KRD
#undef VRD
}

__global__ __launch_bounds__(512, 2)
void mha_k1(const char* __restrict__ x, const char* __restrict__ kc, const char* __restrict__ vc,
            const char* __restrict__ wq, const char* __restrict__ wo, char* ows, char* __restrict__ out) {
  extern __shared__ __attribute__((aligned(16))) char lds[];
  const int tid = threadIdx.x, lane = tid & 63;
  const int wid = __builtin_amdgcn_readfirstlane(tid >> 6);
  const int b = 4 * (blockIdx.x & 1) + ((blockIdx.x >> 3) & 3), h = 8 * ((blockIdx.x >> 1) & 3) + (blockIdx.x >> 5);
  const unsigned lds0 = (unsigned)(uintptr_t)lds;
  const lds_ptr L = (lds_ptr)lds;
  RSTAMP(62); STAMP(0);

  const char *pfx, *pfw;
  {
    const int j = blockIdx.x >> 3, half = lane >> 5, l5 = lane & 31;
    const int xrow = (b & 4) * SQ + 28 * j + (l5 < 28 ? l5 : 27);
    const int idx = 32 * j + l5;
    pfw = wq + (size_t)(64 * half + (idx >> 4)) * 8192 + (h & 24) * 256 + (idx & 15) * 128;
    pfx = x + (size_t)xrow * (DM * 2) + half * 128;
  }
  unsigned pfd0;
  const char* pfe = wid >= 4 ? x : (wid & 1) ? pfw + (size_t)(wid >> 1) * (128 * 8192) : pfx + (wid >> 1) * 256;
#define TOUCH(d, p) asm volatile("global_load_dword %0, %1, off " POLT : "=v"(d) : "v"(p) : "memory")
#define KEEP(d) asm volatile("" :: "v"(d) : "memory")
  const __amdgpu_buffer_rsrc_t srdW = __builtin_amdgcn_make_buffer_rsrc((void*)(wq + h * 256), (short)0, DM * 8192 - h * 256, 0x00020000);
  const unsigned pl = ((lane >> 2) & 3), dbl = lane >> 4, c4 = lane & 3;
  const unsigned wv0 = pl * 8192 + dbl * 64 + c4 * 16, wv1 = wv0 + 31744;
#define DMAW2(c) DMA2(POLW, lds0 + WQO(c) + 2048 * wid, wv0, wv1, srdW, (unsigned)(((c) * 64 + 8 * wid) * 8192))
  TOUCH(pfd0, pfe);
  const int ob = blockIdx.x & 7, oqt = blockIdx.x >> 6, ont = (blockIdx.x >> 3) & 7;
  const __amdgpu_buffer_rsrc_t srdWo = __builtin_amdgcn_make_buffer_rsrc((void*)(wo + ont * 128), (short)0, NH * DH * DM * 2 - ont * 128, 0x00020000);
  const unsigned ow0 = (4 * (lane >> 5) + ((lane >> 2) & 3)) * 1024 + ((lane >> 4) & 1) * 64 + (lane & 3) * 16;
  const unsigned ow1 = ow0 + 7168, ow2 = ow0 + 2 * 7168, ow3 = ow0 + 3 * 7168;
  unsigned long long* const epochp = (unsigned long long*)(ows + WS_EPOCH);
  unsigned long long* const flags = (unsigned long long*)(ows + WS_FLAGS);
  unsigned long long tag = 0;
  unsigned td = 0;
#define ROTM 0
#define ROTA 0
#define RR(i) (((i) + ROTM * ont + ROTA) & 15)
  constexpr int NA = P3NA;
  const int r32 = lane & 31, hi = lane >> 5, tq = (lane >> 2) & 3, tp = lane & 3, tblk = (lane >> 4) & 1;
  bf16x8 wf[NA][4];
#define RBP(i) ((lds_cptr)L + RB(i) + rbo)
#define WDMA(i) DMA4("", lds0 + (unsigned)(RB(i)) + 4096u * w3, ow0, ow1, ow2, ow3, srdWo, 32768u * w3 + (unsigned)(RR(i) * 256 * 1024))
#define W3() unsigned w3 = (unsigned)__builtin_amdgcn_readfirstlane(tid >> 6); asm volatile("s_nop 4" : "+s"(w3))
#define WREAD_NW(i) do { lds_cptr p_ = RBP(i); PIN(p_); _Pragma("unroll") for (int f_ = 0; f_ < 4; ++f_) \
      wf[(i) % NA][f_] = cat(ldstr(p_ + 1024 * (f_ >> 1) + 256 * (f_ & 1)), ldstr(p_ + 1024 * (f_ >> 1) + 2048 + 256 * (f_ & 1))); } while (0)
#define WRWAIT(i) asm volatile("s_waitcnt lgkmcnt(0)" : "+v"(wf[(i) % NA][0]), "+v"(wf[(i) % NA][1]), "+v"(wf[(i) % NA][2]), "+v"(wf[(i) % NA][3]) :: "memory")
#define WREAD(i) do { WREAD_NW(i); WRWAIT(i); } while (0)
#define RBO() (wid * 4096 + 512 * hi + 64 * tq + 32 * tblk + 8 * tp)
  if (wid == 7) {
    const __amdgpu_buffer_rsrc_t srdK = __builtin_amdgcn_make_buffer_rsrc((void*)(kc + (size_t)(b * NH + h) * (SK * DH * 2)), (short)0, SK * DH * 2, 0x00020000);
    const __amdgpu_buffer_rsrc_t srdV = __builtin_amdgcn_make_buffer_rsrc((void*)(vc + (size_t)(b * NH + h) * (SK * DH * 2)), (short)0, SK * DH * 2, 0x00020000);
    const unsigned wv2 = wv0 + 2 * 31744, wv3 = wv0 + 3 * 31744;
    const unsigned kvA = pl * 256 + dbl * 64 + c4 * 16, kvB = pl * 256 + dbl * 64 + ((c4 ^ 2) * 16);
#define DMAW(c) do { _Pragma("unroll") for (int i_ = 0; i_ < 4; ++i_) DMA4(POLW, lds0 + (unsigned)(WQO(c) + 4096 * i_), wv0, wv1, wv2, wv3, srdW, (unsigned)(((c) * 64 + 16 * i_) * 8192)); } while (0)
#define DMAK_(t, so) do { _Pragma("unroll") for (int i_ = 0; i_ < 4; ++i_) DMA4("nt", lds0 + (unsigned)((so) + 4096 * i_), kvA, kvA, kvB, kvB, srdK, (unsigned)((t) * 16384 + 4096 * i_)); } while (0)
#define DMAV_(t, so) do { _Pragma("unroll") for (int i_ = 0; i_ < 4; ++i_) DMA4("nt", lds0 + (unsigned)((so) + 16384 + 4096 * i_), kvA, kvB, kvA, kvB, srdV, (unsigned)((t) * 16384 + 4096 * i_)); } while (0)
#define DMAK(t) DMAK_(t, SLOT(t))
#define DMAV(t) DMAV_(t, SLOT((t) + 1))
    const char* tw = wo + (size_t)(blockIdx.x >> 3) * 8192 + lane * 128;
#define TW(r) do { const char* p_ = tw + (size_t)((((r) + ROTM * (lane & 7) + ROTA) & 15) * 262144); asm volatile("global_load_dword %0, %1, off" : "+v"(td) : "v"(p_) : "memory"); } while (0)
    unsigned t4, t5, t6, t7;
    asm volatile("global_load_dwordx2 %0, %1, off sc1" : "=v"(tag) : "v"(epochp) : "memory");
#define RW(i) do { _Pragma("unroll") for (int w_ = 0; w_ < 8; ++w_) DMA4("", lds0 + (unsigned)(RB(i) + 4096 * w_), ow0, ow1, ow2, ow3, srdWo, (unsigned)((RR(i) * 256 + 32 * w_) * 1024)); } while (0)
    DMAW2(0);
    BARRIER();
    DMAW(1);
    WAITV(16); BARRIER();
    DMAW(2);
    WAITV(16); BARRIER();
    DMAW(3);
    { const char* p4 = pfx + 512; const char* p5 = pfw + (size_t)2 * 128 * 8192; const char* p6 = pfx + 768; const char* p7 = pfw + (size_t)3 * 128 * 8192;
      TOUCH(t4, p4); TOUCH(t5, p5); TOUCH(t6, p6); TOUCH(t7, p7); }
    WAITV(20); BARRIER();
    WAITV(4); BARRIER();
    DMAW(4); DMAW(5);
    WAITV(16); BARRIER();
    KEEP(pfd0); KEEP(t4); KEEP(t5); KEEP(t6); KEEP(t7);
    DMAW(6);
    WAITV(16); BARRIER();
    DMAW(7);
    WAITV(16); BARRIER();
    DMAK(0); DMAK(1); DMAV(0);
    WAITV(48); BARRIER();
    BARRIER();
    DMAK(2);
    WAITV(32); BARRIER();
    DMAV(1); DMAK(3); DMAV(2);
    WAITV(48); BARRIER();
    DMAK(4); DMAV(3); DMAK(5); DMAV(4);
    WAITV(63); BARRIER();
    {
      const char* pa = lane < 32 ? ows + (size_t)(ob * NH + lane) * OHS + 57344
                     : lane == 32 ? ows + (size_t)(b * NH + h) * OHS + 57344
                     : lane == 33 ? ows + (size_t)(b * NH + h > 0 ? b * NH + h - 1 : 0) * OHS + 57344
                     : out + (size_t)(ob * SQ + 56 * oqt + (lane & 1) * 55) * (DM * 2) + ont * 128;
      const char* pb = wo + (size_t)lane * 65536 + (blockIdx.x >> 3) * 128;
      asm volatile("global_load_dword %0, %1, off nt\n\tglobal_load_dword %0, %2, off nt" : "+v"(td) : "v"(pa), "v"(pb) : "memory");
    }
    {
      int so = SLOT(1);
      for (int t = 1; t < 11; ++t) {
        DMAK_(t + 5, so); DMAV_(t + 4, so);
        so = (so == 131072) ? 0 : so + 32768;
        WAITV(63);
        BARRIER();
      }
    }
    DMAV(15); TW(0); TW(1); TW(2); TW(3);
    asm volatile("" : "+v"(tag));
    tag = (((unsigned long long)(unsigned)__builtin_amdgcn_readfirstlane((unsigned)(tag >> 32)) << 32) | (unsigned)__builtin_amdgcn_readfirstlane((unsigned)tag)) + 1;
    if (lane == 0) { *(__attribute__((address_space(3))) unsigned*)(L + PUBW) = 0u; *(__attribute__((address_space(3))) unsigned long long*)(L + PUBW + 8) = tag; }
    asm volatile("s_waitcnt lgkmcnt(0)" ::: "memory");
    WAITV(63);
    BARRIER();
    TW(5); TW(6); TW(7); TW(4);
    WAITV(40);
    RW(0);
    BARRIER();
    RW(1); TW(8); TW(10); TW(11); TW(12);
    WAITV(63);
    BARRIER();
    RW(2); TW(9); TW(13); TW(14); TW(15);
    WAITV(4);
    BARRIER();
    {
      const int rbo = RBO();
      WREAD(0); WREAD(1); WREAD(2);
    }
    BARRIER();
    RW(3); RW(5); RW(6); RW(7);
    WAITV(0);
#undef TW
#undef RW
#undef DMAW
#undef DMAK
#undef DMAV
  } else {
  const __amdgpu_buffer_rsrc_t srdX = __builtin_amdgcn_make_buffer_rsrc((void*)(x + (size_t)(b * SQ + wid * 32) * (DM * 2)), (short)0, 32 * DM * 2, 0x00020000);
  const unsigned xs = (lane >> 3) * 1024 + (((lane & 7) ^ (lane >> 4)) * 16);
  const unsigned xv0 = xs, xv1 = (xs ^ 64) + 7168, xv2 = xs + 2 * 7168, xv3 = (xs ^ 64) + 3 * 7168;
#define XDMA(c) DMA4(POLW, lds0 + (unsigned)XO(c) + 4096u * wid, xv0, xv1, xv2, xv3, srdX, (unsigned)(128 * (c)))
  const unsigned xrb = (unsigned)(wid * 4096 + r32 * 128 + ((hi ^ ((r32 >> 1) & 7)) * 16));
  bf16x8 xf[2][4];
#define XRD(c, k_) xf[(c) & 1][k_] = ldsrd128((lds_cptr)L + ((xrb ^ (unsigned)((k_) << 5)) + XO(c)))
  DMAW2(0); XDMA(0);
  BARRIER();
  XDMA(1);
  const lds_cptr wrb = (lds_cptr)L + 2048 * hi + 64 * tq + 32 * (tp & 1) + 16 * tblk + 8 * (tp >> 1);
  lds_cptr wrb2 = wrb + 131072; PIN(wrb2);
#define WRB(c) ((WQO(c) < 65536 ? wrb : wrb2) + (WQO(c) & 65535))
#define WRD(c, m) do { wlo[m] = ldstr(WRB(c) + 4096 * ((m) >> 2) + 256 * ((m) & 3)); whi[m] = ldstr(WRB(c) + 4096 * ((m) >> 2) + 1024 + 256 * ((m) & 3)); } while (0)
  AttnState st;
  f32x16 acc[4];
#pragma unroll
  for (int i = 0; i < 4; ++i) acc[i] = f32x16{};
  s16x4 wlo[16], whi[16];
  WAITV(4);
  XRD(0, 0); XRD(0, 1); XRD(0, 2); XRD(0, 3);
  STAMP(1);
  BARRIER();
  STAMP(2);
#pragma unroll
  for (int m = 0; m < PD; ++m) WRD(0, m);
#define QCVT(nb) do { u32x4 x_, y_; \
    _Pragma("unroll") for (int e_ = 0; e_ < 4; ++e_) { \
      const unsigned lo_ = cvtpk(acc[nb][2 * e_] * QSCALE, acc[nb][2 * e_ + 1] * QSCALE), hi_ = cvtpk(acc[nb][8 + 2 * e_] * QSCALE, acc[nb][9 + 2 * e_] * QSCALE); \
      auto r_ = __builtin_amdgcn_permlane16_swap(lo_, hi_, false, false); x_[e_] = r_[0]; y_[e_] = r_[1]; } \
    st.q[0][nb] = __builtin_bit_cast(bf16x8, x_); st.q[1][nb] = __builtin_bit_cast(bf16x8, y_); PIN(st.q[0][nb]); PIN(st.q[1][nb]); } while (0)
  static_for<128>([&](auto I) __attribute__((always_inline)) {
    constexpr int c = I.value >> 4, mm = I.value & 15;
    constexpr int m = (c == 7) ? (4 * (mm & 3) + (mm >> 2)) : mm;
    acc[m & 3] = MFMA(cat(wlo[m], whi[m]), xf[c & 1][m >> 2], acc[m & 3]);
    if constexpr (c == 0 && mm + PD == 16) { BARRIER(); }
    if constexpr (mm + PD < 16) { constexpr int f = mm + PD, ff = (c == 7) ? (4 * (f & 3) + (f >> 2)) : f; WRD(c, ff); }
    else if constexpr (c < 7) { constexpr int f = mm + PD - 16, ff = (c + 1 == 7) ? (4 * (f & 3) + (f >> 2)) : f; WRD(c + 1, ff); }
    if constexpr (c == 0 && mm == 0) { XDMA(2); }
    if constexpr (c == 0 && mm == 2) { XDMA(3); }
    if constexpr (c == 2 && mm == 8) { XDMA(4); }
    if constexpr (c == 3 && mm == 0) { XDMA(5); }
    if constexpr (c == 3 && mm == 8) { XDMA(6); }
    if constexpr (c == 4 && mm == 8) { XDMA(7); }
    if constexpr (c < 7) {
      if constexpr (mm == 4) { WAITV(c == 0 ? 8 : (c == 2 || c == 6) ? 0 : 4); }
      if constexpr (mm == 5 && c == 1) { KEEP(pfd0); }
      if constexpr (mm >= 4 && mm < 8) { XRD(c + 1, mm - 4); }
    }
    if constexpr (c == 5) { st.o[mm >> 3][mm & 7] = f32x4{}; PIN(st.o[mm >> 3][mm & 7]); }
    if constexpr (c == 7 && (mm == 6 || mm == 10 || mm == 14)) { QCVT((mm - 6) >> 2); }
    SBAR();
    if constexpr (mm == 15) { BARRIER(); STAMP(3 + c); SBAR(); }
  });
  LSTAMP(58);
#undef XDMA
#undef XRD
#undef WRD
  const int l16 = lane & 15, g4 = lane >> 4;
  const lds_cptr kb = (lds_cptr)L + 1024 * (l16 >> 2) + 64 * (l16 & 3) + 16 * (g4 ^ ((l16 >> 2) & 2));
  const lds_cptr vb0 = (lds_cptr)L + 16384 + 1024 * g4 + 64 * tq + 32 * (g4 & 1) + 8 * tp;
  const lds_cptr vb1 = (lds_cptr)L + 16384 + 1024 * g4 + 64 * tq + 32 * (1 - (g4 & 1)) + 8 * tp;
#pragma unroll
  for (int m = 0; m < PD; ++m) st.kf[m] = ldsrd128(kb + SLOT(0) + 4096 * (m >> 2) + 256 * (m & 3));
  QCVT(3);
#undef QCVT
  st.negm[0] = f32x4{}; st.negm[1] = f32x4{}; PIN(st.negm[0]); PIN(st.negm[1]);
  st.mhat[0] = st.mhat[1] = 0.f; st.lacc[0] = f32x4{}; st.lacc[1] = f32x4{};
  st.ones = bf16x8{0x3F80, 0x3F80, 0x3F80, 0x3F80, 0x3F80, 0x3F80, 0x3F80, 0x3F80}; PIN(st.ones);

  STile A, B;
  attn_step<true>(st, A, B, kb + SLOT(0), vb0 + SLOT(0), vb1 + SLOT(0), kb + SLOT(1), wid >= 4);
  STAMP(11);
  BARRIER();
  STAMP(12);
  int s0 = SLOT(1);
  for (int t = 1; t < 15; t += 2) {
    const int s1 = (s0 == 131072) ? 0 : s0 + 32768, s2 = (s1 == 131072) ? 0 : s1 + 32768;
    attn_step<false>(st, B, A, kb + s0, vb0 + s0, vb1 + s0, kb + s1, wid >= 4);
    STAMP(11 + 2 * t);
    BARRIER();
    STAMP(12 + 2 * t);
    attn_step<false>(st, A, B, kb + s1, vb0 + s1, vb1 + s1, kb + s2, wid >= 4);
    STAMP(13 + 2 * t);
    BARRIER();
    STAMP(14 + 2 * t);
    s0 = s2;
  }
  attn_step<false>(st, B, A, kb + SLOT(15), vb0 + SLOT(15), vb1 + SLOT(15), kb + SLOT(15), wid >= 4);
  STAMP(41);
  BARRIER();
  STAMP(42); LSTAMP(59);
  {
    lds_cptr vd0 = vb0 + SLOT(16), vd1 = vb1 + SLOT(16); PIN(vd0); PIN(vd1);
#define VRD(m) do { st.vlo[m] = ldstr((((m) & 2) ? vd1 : vd0) + 8192 * ((m) & 1) + 256 * ((m) >> 2)); st.vhi[m] = ldstr((((m) & 2) ? vd1 : vd0) + 8192 * ((m) & 1) + 4096 + 256 * ((m) >> 2)); } while (0)
    const int rbo = RBO();
    WREAD(0); WREAD(1); WREAD(2);
    BARRIER();
#pragma unroll
    for (int m = 0; m < 8; ++m) VRD(m);
    static_for<22>([&](auto I) __attribute__((always_inline)) { constexpr int n = 10 + I.value; EL(B, n) = EX(EL(B, n)); });
    static_for<12>([&](auto I) __attribute__((always_inline)) { constexpr int n = 8 + 2 * I.value; PWV(n)[(n >> 1) & 3] = cvtpk(EL(B, n), EL(B, n + 1)); });
#pragma unroll
    for (int w = 0; w < 2; ++w) {
      st.lacc[0] = MFMA16(st.ones, __builtin_bit_cast(bf16x8, st.pw[0][w]), st.lacc[0]);
      st.lacc[1] = MFMA16(st.ones, __builtin_bit_cast(bf16x8, st.pw[1][w]), st.lacc[1]);
    }
    const float inv0 = 1.0f / st.lacc[0][0], inv1 = 1.0f / st.lacc[1][0];
    const __amdgpu_buffer_rsrc_t srdOs = __builtin_amdgcn_make_buffer_rsrc((void*)(ows + (size_t)(b * NH + h) * OHS), (short)0, 57344, 0x00020000);
    const int qr0 = 32 * wid + l16, vk0 = qr0 / 28, qr1 = qr0 + 16, vk1 = qr1 / 28;
    const unsigned osv0 = vk0 * 7168 + (g4 >> 1) * 896 + ((g4 & 1) * 28 + qr0 - 28 * vk0) * 16, osv1 = vk1 * 7168 + (g4 >> 1) * 896 + ((g4 & 1) * 28 + qr1 - 28 * vk1) * 16;
#define OSTORE(u) do { \
      u32x4 w0_ = {cvtpk(st.o[0][2 * (u)][0] * inv0, st.o[0][2 * (u)][1] * inv0), cvtpk(st.o[0][2 * (u)][2] * inv0, st.o[0][2 * (u)][3] * inv0), \
                   cvtpk(st.o[0][2 * (u) + 1][0] * inv0, st.o[0][2 * (u) + 1][1] * inv0), cvtpk(st.o[0][2 * (u) + 1][2] * inv0, st.o[0][2 * (u) + 1][3] * inv0)}; \
      __builtin_amdgcn_raw_buffer_store_b128(w0_, srdOs, osv0 + (u) * 1792, 0, 16); \
      u32x4 w1_ = {cvtpk(st.o[1][2 * (u)][0] * inv1, st.o[1][2 * (u)][1] * inv1), cvtpk(st.o[1][2 * (u)][2] * inv1, st.o[1][2 * (u)][3] * inv1), \
                   cvtpk(st.o[1][2 * (u) + 1][0] * inv1, st.o[1][2 * (u) + 1][1] * inv1), cvtpk(st.o[1][2 * (u) + 1][2] * inv1, st.o[1][2 * (u) + 1][3] * inv1)}; \
      __builtin_amdgcn_raw_buffer_store_b128(w1_, srdOs, osv1 + (u) * 1792, 0, 16); } while (0)
    SBAR();
#pragma unroll
    for (int u = 0; u < 4; ++u) {
#pragma unroll
      for (int k = 0; k < 4; ++k) {
        const int m = 4 * u + k, db = m >> 1, w = m & 1;
        st.o[0][db] = MFMA16(cat(st.vlo[m], st.vhi[m]), __builtin_bit_cast(bf16x8, st.pw[0][w]), st.o[0][db]);
        st.o[1][db] = MFMA16(cat(st.vlo[m], st.vhi[m]), __builtin_bit_cast(bf16x8, st.pw[1][w]), st.o[1][db]);
        if (u < 2) { VRD(m + 8); }
      }
      SBAR();
      if (u >= 1) { OSTORE(u - 1); }
      SBAR();
    }
    STAMP(43);
    OSTORE(3);
    STAMP(44);
#undef VRD
#undef OSTORE
    WAITV(0);
    if (lane == 0) {
      const unsigned old = __hip_atomic_fetch_add((__attribute__((address_space(3))) unsigned*)(L + PUBW), 1u, __ATOMIC_RELAXED, __HIP_MEMORY_SCOPE_WORKGROUP);
      if (old == 6) __hip_atomic_store(flags + b * NH + h, *(__attribute__((address_space(3))) unsigned long long*)(L + PUBW + 8), __ATOMIC_RELAXED, __HIP_MEMORY_SCOPE_AGENT);
    }
    asm volatile("s_waitcnt lgkmcnt(0)" ::: "memory");
    STAMP(50);
  }
  }

  {
    static_assert(NA == 8, "prefetch sequences above are written for NA = 8");
    const int rbo = RBO(); W3();
    const __amdgpu_buffer_rsrc_t srdO = __builtin_amdgcn_make_buffer_rsrc((void*)(ows + (size_t)ob * (NH * OHS)), (short)0, NH * OHS, 0x00020000);
    const unsigned ov0 = (hi * 28 + (r32 < 28 ? r32 : 27)) * 16;
    const unsigned osb = (w3 >> 2) * OHS + (w3 & 3) * 1792 + oqt * 2 * 7168;
    u32x4 of[3][4];
    f32x16 acc[2][2];
#pragma unroll
    for (int i = 0; i < 2; ++i) { acc[i][0] = f32x16{}; acc[i][1] = f32x16{}; }
#define OLOAD(i) do { const unsigned so_ = osb + (unsigned)(RR(i) * 2 * OHS), so1_ = so_ + 7168u; \
      BLOAD("sc1", of[(i) % 3][0], ov0, srdO, so_, 0); BLOAD("sc1", of[(i) % 3][1], ov0, srdO, so1_, 0); \
      BLOAD("sc1", of[(i) % 3][2], ov0, srdO, so_, 896); BLOAD("sc1", of[(i) % 3][3], ov0, srdO, so1_, 896); } while (0)
    BARRIER();
    LSTAMP(57);
    STAMP(51);
    WDMA(4);
    WREAD(3); WDMA(8); WREAD(5); WDMA(10); WREAD(6); WDMA(11); WREAD(7); WDMA(12);
    WAITV(16); WREAD(4); WDMA(9);
    STAMP(52);
    if (w3 == 7) {
      const unsigned long long* fp = flags + ob * NH + r32;
      for (unsigned spins = 0; spins < (1u << 18); ++spins) {
        const unsigned long long v = __hip_atomic_load(fp, __ATOMIC_RELAXED, __HIP_MEMORY_SCOPE_AGENT);
        if (__all(v == tag)) break;
        __builtin_amdgcn_s_sleep(1);
      }
      KEEP(td);
    }
    STAMP(53);
    BARRIER();
    LSTAMP(60);
    STAMP(54);
    OLOAD(0); OLOAD(1);
#pragma unroll
    for (int i = 0; i < 16; ++i) {
      const int cnt = (i < 15 ? 4 : 0) + ((i - 1 >= 1 && i - 1 <= 3) ? 4 : 0);
      WAITV4(cnt, of[i % 3][0], of[i % 3][1], of[i % 3][2], of[i % 3][3]);
      if (i >= 1 && i <= 3) { WRWAIT(i - 1 + NA); WDMA(i + 12); }
      if (i + 2 < 16) { OLOAD(i + 2); }
      SBAR();
#pragma unroll
      for (int ks = 0; ks < 2; ++ks)
#pragma unroll
        for (int nb = 0; nb < 2; ++nb)
#pragma unroll
          for (int vb = 0; vb < 2; ++vb)
            acc[nb][vb] = MFMA(wf[i % NA][2 * ks + nb], __builtin_bit_cast(bf16x8, of[i % 3][2 * ks + vb]), acc[nb][vb]);
      SBAR();
      if (i + NA < 16) { WREAD_NW(i + NA); }
      SBAR();
    }
    STAMP(55);
#undef WDMA
#undef WREAD
#undef OLOAD
#undef RBP
#pragma unroll
    for (int vb = 0; vb < 2; ++vb)
#pragma unroll
      for (int nb = 0; nb < 2; ++nb)
#pragma unroll
        for (int rg = 0; rg < 4; ++rg) {
          const int q = 32 * vb + r32, slot = 8 * nb + 2 * rg + hi;
          f32x4 v = {acc[nb][vb][4 * rg], acc[nb][vb][4 * rg + 1], acc[nb][vb][4 * rg + 2], acc[nb][vb][4 * rg + 3]};
          *(__attribute__((address_space(3))) f32x4*)(L + (q >> 4) * 32768 + wid * 4096 + (q & 15) * 256 + ((slot ^ (q & 15)) * 16)) = v;
        }
    asm volatile("s_waitcnt lgkmcnt(0)" ::: "memory");
    BARRIER();
    STAMP(56); LSTAMP(61);
    const int q = tid >> 3, pc = tid & 7;
    f32x4 s0 = {0, 0, 0, 0}, s1 = {0, 0, 0, 0};
#pragma unroll
    for (int w = 0; w < 8; ++w) {
      s0 += *(const __attribute__((address_space(3))) f32x4*)(L + (q >> 4) * 32768 + w * 4096 + (q & 15) * 256 + (((2 * pc) ^ (q & 15)) * 16));
      s1 += *(const __attribute__((address_space(3))) f32x4*)(L + (q >> 4) * 32768 + w * 4096 + (q & 15) * 256 + (((2 * pc + 1) ^ (q & 15)) * 16));
    }
    u32x4 w = {cvtpk(s0[0], s0[1]), cvtpk(s0[2], s0[3]), cvtpk(s1[0], s1[1]), cvtpk(s1[2], s1[3])};
    if ((q & 31) < 28)
      *(u32x4*)(out + (size_t)(ob * SQ + 56 * oqt + 28 * (q >> 5) + (q & 31)) * (DM * 2) + ont * 128 + pc * 16) = w;
    if (blockIdx.x == 0 && wid == 7) {
      for (unsigned spins = 0; spins < (1u << 18); ++spins) {
        bool ok = true;
#pragma unroll
        for (int k = 0; k < 4; ++k) ok &= __hip_atomic_load(flags + 64 * k + lane, __ATOMIC_RELAXED, __HIP_MEMORY_SCOPE_AGENT) == tag;
        if (__all(ok)) break;
        __builtin_amdgcn_s_sleep(2);
      }
      if (lane == 0) __hip_atomic_store(epochp, tag, __ATOMIC_RELAXED, __HIP_MEMORY_SCOPE_AGENT);
    }
  }
  STAMP(40); RSTAMP(63);
  STFLUSH(ows + (64 << 20));
}

extern "C" void kernel_launch(void* const* d_in, const int* in_sizes, int n_in,
                              void* d_out, int out_size, void* d_ws, size_t ws_size,
                              hipStream_t stream) {
  (void)in_sizes; (void)n_in; (void)out_size; (void)ws_size;
  (void)hipFuncSetAttribute((const void*)mha_k1, hipFuncAttributeMaxDynamicSharedMemorySize, K1_LDS);
  mha_k1<<<256, 512, K1_LDS, stream>>>((const char*)d_in[0], (const char*)d_in[1], (const char*)d_in[2], (const char*)d_in[3], (const char*)d_in[4], (char*)d_ws, (char*)d_out);
}
```

```cpp
#include <hip/hip_runtime.h>
#include <hip/hip_bf16.h>
#include <stdint.h>
#include <utility>
#include <type_traits>

typedef short    bf16x8 __attribute__((ext_vector_type(8)));
typedef short    s16x4  __attribute__((ext_vector_type(4)));
typedef float    f32x16 __attribute__((ext_vector_type(16)));
typedef float    f32x4  __attribute__((ext_vector_type(4)));
typedef float    f32x2  __attribute__((ext_vector_type(2)));
typedef __bf16   bf16x2 __attribute__((ext_vector_type(2)));
typedef unsigned u32x4  __attribute__((ext_vector_type(4)));
typedef __attribute__((address_space(3))) char* lds_ptr;
typedef __attribute__((address_space(3))) const char* lds_cptr;

#define DI __device__ __forceinline__
#define SBAR() __builtin_amdgcn_sched_barrier(0)
#define PIN(x) asm volatile("" : "+v"(x))
#define MFMA(a, b, c) __builtin_amdgcn_mfma_f32_32x32x16_bf16(a, b, c, 0, 0, 0)
#define BARRIER() asm volatile("s_barrier" ::: "memory")

#define STAMP(i)
#define RSTAMP(i)
#define LSTAMP(i)
#define STFLUSH(dbg)
#define POLT "nt"
#define POLW ""
#define POLWO ""
#define POLT2 ""
#define POLO "sc1"
#define P3NA 8
#define TSCHED 3
#define PRIO_TOGGLE 21
#define EARLYKV 0
#define MAPK1 1
constexpr int NB = 8, SQ = 224, DM = 512, NH = 32, SK = 1024, DH = 128;
constexpr float QSCALE = 0.088388347648318440f * 1.4426950408889634f;
constexpr float THR = 8.0f;

template <int... Is, class F> DI void static_for_(std::integer_sequence<int, Is...>, F&& f) { (f(std::integral_constant<int, Is>{}), ...); }
template <int N, class F> DI void static_for(F&& f) { static_for_(std::make_integer_sequence<int, N>{}, f); }
DI unsigned cvtpk(float lo, float hi) { f32x2 v = {lo, hi}; bf16x2 b = __builtin_convertvector(v, bf16x2); return __builtin_bit_cast(unsigned, b); }
DI bf16x8 ldsrd128(lds_cptr p) { return *(const __attribute__((address_space(3))) bf16x8*)p; }
DI s16x4 ldstr(lds_cptr p) { return __builtin_bit_cast(s16x4, __builtin_amdgcn_ds_read_tr16_b64_v4i16((__attribute__((address_space(3))) s16x4*)p)); }
DI bf16x8 cat(s16x4 a, s16x4 b) { return __builtin_shufflevector(a, b, 0, 1, 2, 3, 4, 5, 6, 7); }
DI void ldswr128(lds_ptr p, u32x4 v) { *(__attribute__((address_space(3))) u32x4*)p = v; }
#define MX3(a, b, c) __builtin_fmaxf(__builtin_fmaxf((a), (b)), (c))

#define DMA4(POL, m0v, v0, v1, v2, v3, srd, soff)                                                     \
  asm volatile("s_mov_b32 m0, %0\n\ts_nop 0\n\t"                                                      \
               "buffer_load_dwordx4 %1, %5, %6 offen " POL " lds\n\t"                                 \
               "buffer_load_dwordx4 %2, %5, %6 offen offset:1024 " POL " lds\n\t"                     \
               "buffer_load_dwordx4 %3, %5, %6 offen offset:2048 " POL " lds\n\t"                     \
               "buffer_load_dwordx4 %4, %5, %6 offen offset:3072 " POL " lds"                         \
               :: "s"(m0v), "v"(v0), "v"(v1), "v"(v2), "v"(v3), "s"(srd), "s"(soff) : "memory")
#define DMA2(POL, m0v, v0, v1, srd, soff)                                                             \
  asm volatile("s_mov_b32 m0, %0\n\ts_nop 0\n\t"                                                      \
               "buffer_load_dwordx4 %1, %3, %4 offen " POL " lds\n\t"                                 \
               "buffer_load_dwordx4 %2, %3, %4 offen offset:1024 " POL " lds"                         \
               :: "s"(m0v), "v"(v0), "v"(v1), "s"(srd), "s"(soff) : "memory")
#define BLOAD(POL, dst, voff, srd, soff, imm)                                                         \
  asm volatile("buffer_load_dwordx4 %0, %1, %2, %3 offen offset:%4 " POL                              \
               : "=v"(dst) : "v"(voff), "s"(srd), "s"(soff), "i"(imm) : "memory")
#define WAITV(n) asm volatile("s_waitcnt vmcnt(%0)" :: "i"(n) : "memory")
#define WAITV4(n, a, b, c, d) asm volatile("s_waitcnt vmcnt(%4)" : "+v"(a), "+v"(b), "+v"(c), "+v"(d) : "i"(n) : "memory")

constexpr int K1_LDS = 163840;
constexpr size_t WS_FLAGS = (size_t)32 << 20, WS_EPOCH = WS_FLAGS + 4096;
DI constexpr int SLOT(int t) { return (t % 5) * 32768; }
DI constexpr int RB(int i) { return ((i + 2) % 5) * 32768; }
constexpr int PUBW = 32768;
DI constexpr int WQO(int c) { return (c & 3) == 0 ? 0 : (c & 3) == 1 ? 16384 : (c & 3) == 2 ? 131072 : 147456; }
DI constexpr int XO(int c) { return (c % 3) == 0 ? 65536 : (c % 3) == 1 ? 98304 : 32768; }

#define MFMA16(a, b, c) __builtin_amdgcn_mfma_f32_16x16x32_bf16(a, b, c, 0, 0, 0)
struct AttnState {
  f32x4 o[2][8];
  bf16x8 q[2][4];
  f32x4 negm[2];
  float mhat[2];
  f32x4 lacc[2];
  bf16x8 ones;
  bf16x8 kf[16];
  s16x4 vlo[16], vhi[16];
  u32x4 pw[2][2];
};
struct STile { f32x4 s[2][4]; };
DI float xmax4(float v) {
  auto a = __builtin_amdgcn_permlane16_swap(__float_as_uint(v), __float_as_uint(v), false, false);
  v = __builtin_fmaxf(__uint_as_float(a[0]), __uint_as_float(a[1]));
  auto c = __builtin_amdgcn_permlane32_swap(__float_as_uint(v), __float_as_uint(v), false, false);
  return __builtin_fmaxf(__uint_as_float(c[0]), __uint_as_float(c[1]));
}
DI float xsum4(float v) {
  auto a = __builtin_amdgcn_permlane16_swap(__float_as_uint(v), __float_as_uint(v), false, false);
  v = __uint_as_float(a[0]) + __uint_as_float(a[1]);
  auto c = __builtin_amdgcn_permlane32_swap(__float_as_uint(v), __float_as_uint(v), false, false);
  return __uint_as_float(c[0]) + __uint_as_float(c[1]);
}

#define PDV 5
constexpr int PD = PDV;
#define EL(T, n) (T).s[((n) >> 3) & 1][2 * ((n) >> 4) + (((n) >> 2) & 1)][(n) & 3]
#define ELV(T, n) (T).s[((n) >> 3) & 1][2 * ((n) >> 4) + (((n) >> 2) & 1)]
#define PWV(n) st.pw[((n) >> 3) & 1][(n) >> 4]
#define EX(v) __builtin_amdgcn_exp2f(v)
template <bool FIRST>
DI void attn_step(AttnState& st, STile& C, STile& P, lds_cptr kp, lds_cptr vp0, lds_cptr vp1, lds_cptr kn, const bool young) {
#define KRD(m, base) st.kf[m] = ldsrd128((base) + 4096 * ((m) >> 2) + 256 * ((m) & 3))
#define VRD(m) do { st.vlo[m] = ldstr((((m) & 1) ? vp1 : vp0) + 8192 * ((m) >> 3) + 256 * (((m) & 7) >> 1)); \
                    st.vhi[m] = ldstr((((m) & 1) ? vp1 : vp0) + 8192 * ((m) >> 3) + 4096 + 256 * (((m) & 7) >> 1)); } while (0)
  float ma = -3.0e38f, mb = -3.0e38f, fct0 = 1.f, fct1 = 1.f;
  bool resc = false;
  SBAR();
  static_for<32>([&](auto I) __attribute__((always_inline)) {
    constexpr int g = I.value;
    if constexpr (g == 0) { if (young) __builtin_amdgcn_s_setprio(1); }
    if constexpr (g == PRIO_TOGGLE) { __builtin_amdgcn_s_setprio(0); }
    if constexpr (g < 16) {
      constexpr int kb = g >> 2, u = g & 3;
      if constexpr (u == 0) C.s[0][kb] = MFMA16(st.kf[g], st.q[0][0], st.negm[0]); else C.s[0][kb] = MFMA16(st.kf[g], st.q[0][u], C.s[0][kb]);
      PIN(C.s[0][kb]);
    } else if constexpr (!FIRST) {
      constexpr int m = g - 16;
      st.o[0][m & 7] = MFMA16(cat(st.vlo[m], st.vhi[m]), __builtin_bit_cast(bf16x8, st.pw[0][m >> 3]), st.o[0][m & 7]);
      PIN(st.o[0][m & 7]);
    }
    SBAR();
    {
      constexpr int f = g + PD;
      if constexpr (f < 16) { KRD(f, kp); }
      else if constexpr (f < 32) { if constexpr (!FIRST) { VRD(f - 16); } }
      else { KRD(f - 32, kn); }
    }
    if constexpr ((g & 1) && g >= 5 && g <= 19) {
      constexpr int kb = (g - 5) >> 2, hf = ((g - 5) >> 1) & 1;
      ma = MX3(ma, C.s[0][kb][2 * hf], C.s[0][kb][2 * hf + 1]); PIN(ma);
    }
    SBAR();
    if constexpr (g < 16) {
      constexpr int kb = g >> 2, u = g & 3;
      if constexpr (u == 0) C.s[1][kb] = MFMA16(st.kf[g], st.q[1][0], st.negm[1]); else C.s[1][kb] = MFMA16(st.kf[g], st.q[1][u], C.s[1][kb]);
      PIN(C.s[1][kb]);
    } else if constexpr (!FIRST) {
      constexpr int m = g - 16;
      st.o[1][m & 7] = MFMA16(cat(st.vlo[m], st.vhi[m]), __builtin_bit_cast(bf16x8, st.pw[1][m >> 3]), st.o[1][m & 7]);
      PIN(st.o[1][m & 7]);
    }
    SBAR();
    if constexpr ((g & 1) == 0) {
      if constexpr (g <= 22) {
        if constexpr (!FIRST) { constexpr int n = 8 + g; PWV(n)[(n >> 1) & 3] = cvtpk(EL(P, n), EL(P, n + 1)); PIN(PWV(n)); }
      } else { constexpr int n = g - 24; PWV(n)[(n >> 1) & 3] = cvtpk(EL(C, n), EL(C, n + 1)); PIN(PWV(n)); }
    }
    if constexpr ((g & 1) && g >= 5 && g <= 19) {
      constexpr int kb = (g - 5) >> 2, hf = ((g - 5) >> 1) & 1;
      mb = MX3(mb, C.s[1][kb][2 * hf], C.s[1][kb][2 * hf + 1]); PIN(mb);
    }
    if constexpr (g == 22) {
      const bool ok = (ma <= THR) & (mb <= THR) & (!FIRST | ((ma >= -THR) & (mb >= -THR)));
      if (__builtin_expect(!__all(ok), 0)) {
#pragma unroll
        for (int qb = 0; qb < 2; ++qb) {
          float rm = qb ? mb : ma;
          rm = xmax4(rm);
          const float dl = FIRST ? rm : __builtin_fmaxf(rm, 0.f);
          st.mhat[qb] += dl;
#pragma unroll
          for (int kb = 0; kb < 4; ++kb)
#pragma unroll
            for (int r = 0; r < 4; ++r) C.s[qb][kb][r] -= dl;
#pragma unroll
          for (int r = 0; r < 4; ++r) st.negm[qb][r] = -st.mhat[qb];
          PIN(st.negm[qb]);
          if constexpr (!FIRST) { const float f_ = EX(-dl); if (qb == 0) fct0 = f_; else fct1 = f_; }
        }
        resc = true;
      }
    }
    if constexpr (g <= 21) {
      if constexpr (!FIRST) { constexpr int n = 10 + g; EL(P, n) = EX(EL(P, n)); PIN(ELV(P, n)); }
    } else { constexpr int n = g - 22; EL(C, n) = EX(EL(C, n)); PIN(ELV(C, n)); }
    SBAR();
    if constexpr (!FIRST && (g == 21 || g == 23 || g == 25 || g == 27)) {
      constexpr int qb = (g >> 1) & 1, w = g >= 24;
      st.lacc[qb] = MFMA16(st.ones, __builtin_bit_cast(bf16x8, st.pw[qb][w]), st.lacc[qb]); PIN(st.lacc[qb]);
      SBAR();
    }
  });
  if constexpr (!FIRST) {
    if (__builtin_expect(resc, 0)) {
#pragma unroll
      for (int d = 0; d < 8; ++d)
#pragma unroll
        for (int r = 0; r < 4; ++r) { st.o[0][d][r] *= fct0; st.o[1][d][r] *= fct1; }
#pragma unroll
      for (int r = 0; r < 4; ++r) { st.lacc[0][r] *= fct0; st.lacc[1][r] *= fct1; }
    }
  }
#undef KRD
#undef VRD
}

__global__ __launch_bounds__(512, 2)
void mha_k1(const char* __restrict__ x, const char* __restrict__ kc, const char* __restrict__ vc,
            const char* __restrict__ wq, const char* __restrict__ wo, char* ows, char* __restrict__ out) {
  extern __shared__ __attribute__((aligned(16))) char lds[];
  const int tid = threadIdx.x, lane = tid & 63;
  const int wid = __builtin_amdgcn_readfirstlane(tid >> 6);
  const int b = 4 * (blockIdx.x & 1) + ((blockIdx.x >> 3) & 3), h = 8 * ((blockIdx.x >> 1) & 3) + (blockIdx.x >> 5);
  const unsigned lds0 = (unsigned)(uintptr_t)lds;
  const lds_ptr L = (lds_ptr)lds;
  RSTAMP(62); STAMP(0);

  const char *pfx, *pfw;
  {
    const int j = blockIdx.x >> 3, half = lane >> 5, l5 = lane & 31;
    const int xrow = (b & 4) * SQ + 28 * j + (l5 < 28 ? l5 : 27);
    const int idx = 32 * j + l5;
    pfw = wq + (size_t)(64 * half + (idx >> 4)) * 8192 + (h & 24) * 256 + (idx & 15) * 128;
    pfx = x + (size_t)xrow * (DM * 2) + half * 128;
  }
  unsigned pfd0;
  const char* pfe = wid >= 4 ? x : (wid & 1) ? pfw + (size_t)(wid >> 1) * (128 * 8192) : pfx + (wid >> 1) * 256;
#define TOUCH(d, p) asm volatile("global_load_dword %0, %1, off " POLT : "=v"(d) : "v"(p) : "memory")
#define KEEP(d) asm volatile("" :: "v"(d) : "memory")
  const __amdgpu_buffer_rsrc_t srdW = __builtin_amdgcn_make_buffer_rsrc((void*)(wq + h * 256), (short)0, DM * 8192 - h * 256, 0x00020000);
  const unsigned pl = ((lane >> 2) & 3), dbl = lane >> 4, c4 = lane & 3;
  const unsigned wv0 = pl * 8192 + dbl * 64 + c4 * 16, wv1 = wv0 + 31744;
#define DMAW2(c) DMA2(POLW, lds0 + WQO(c) + 2048 * wid, wv0, wv1, srdW, (unsigned)(((c) * 64 + 8 * wid) * 8192))
  TOUCH(pfd0, pfe);
  const int ob = blockIdx.x & 7, oqt = blockIdx.x >> 6, ont = (blockIdx.x >> 3) & 7;
  const __amdgpu_buffer_rsrc_t srdWo = __builtin_amdgcn_make_buffer_rsrc((void*)(wo + ont * 128), (short)0, NH * DH * DM * 2 - ont * 128, 0x00020000);
  const unsigned ow0 = (4 * (lane >> 5) + ((lane >> 2) & 3)) * 1024 + ((lane >> 4) & 1) * 64 + (lane & 3) * 16;
  const unsigned ow1 = ow0 + 7168, ow2 = ow0 + 2 * 7168, ow3 = ow0 + 3 * 7168;
  unsigned long long* const epochp = (unsigned long long*)(ows + WS_EPOCH);
  unsigned long long* const flags = (unsigned long long*)(ows + WS_FLAGS);
  unsigned long long tag = 0;
  unsigned td = 0;
#define ROTM 0
#define ROTA 0
#define RR(i) (((i) + ROTM * ont + ROTA) & 15)
  constexpr int NA = P3NA;
  const int r32 = lane & 31, hi = lane >> 5, tq = (lane >> 2) & 3, tp = lane & 3, tblk = (lane >> 4) & 1;
  bf16x8 wf[NA][4];
#define RBP(i) ((lds_cptr)L + RB(i) + rbo)
#define WDMA(i) DMA4("", lds0 + (unsigned)(RB(i)) + 4096u * w3, ow0, ow1, ow2, ow3, srdWo, 32768u * w3 + (unsigned)(RR(i) * 256 * 1024))
#define W3() unsigned w3 = (unsigned)__builtin_amdgcn_readfirstlane(tid >> 6); asm volatile("s_nop 4" : "+s"(w3))
#define WREAD_NW(i) do { lds_cptr p_ = RBP(i); PIN(p_); _Pragma("unroll") for (int f_ = 0; f_ < 4; ++f_) \
      wf[(i) % NA][f_] = cat(ldstr(p_ + 1024 * (f_ >> 1) + 256 * (f_ & 1)), ldstr(p_ + 1024 * (f_ >> 1) + 2048 + 256 * (f_ & 1))); } while (0)
#define WRWAIT(i) asm volatile("s_waitcnt lgkmcnt(0)" : "+v"(wf[(i) % NA][0]), "+v"(wf[(i) % NA][1]), "+v"(wf[(i) % NA][2]), "+v"(wf[(i) % NA][3]) :: "memory")
#define WREAD(i) do { WREAD_NW(i); WRWAIT(i); } while (0)
#define RBO() (wid * 4096 + 512 * hi + 64 * tq + 32 * tblk + 8 * tp)
  if (wid == 7) {
    const __amdgpu_buffer_rsrc_t srdK = __builtin_amdgcn_make_buffer_rsrc((void*)(kc + (size_t)(b * NH + h) * (SK * DH * 2)), (short)0, SK * DH * 2, 0x00020000);
    const __amdgpu_buffer_rsrc_t srdV = __builtin_amdgcn_make_buffer_rsrc((void*)(vc + (size_t)(b * NH + h) * (SK * DH * 2)), (short)0, SK * DH * 2, 0x00020000);
    const unsigned wv2 = wv0 + 2 * 31744, wv3 = wv0 + 3 * 31744;
    const unsigned kvA = pl * 256 + dbl * 64 + c4 * 16, kvB = pl * 256 + dbl * 64 + ((c4 ^ 2) * 16);
#define DMAW(c) do { _Pragma("unroll") for (int i_ = 0; i_ < 4; ++i_) DMA4(POLW, lds0 + (unsigned)(WQO(c) + 4096 * i_), wv0, wv1, wv2, wv3, srdW, (unsigned)(((c) * 64 + 16 * i_) * 8192)); } while (0)
#define DMAK_(t, so) do { _Pragma("unroll") for (int i_ = 0; i_ < 4; ++i_) DMA4("nt", lds0 + (unsigned)((so) + 4096 * i_), kvA, kvA, kvB, kvB, srdK, (unsigned)((t) * 16384 + 4096 * i_)); } while (0)
#define DMAV_(t, so) do { _Pragma("unroll") for (int i_ = 0; i_ < 4; ++i_) DMA4("nt", lds0 + (unsigned)((so) + 16384 + 4096 * i_), kvA, kvB, kvA, kvB, srdV, (unsigned)((t) * 16384 + 4096 * i_)); } while (0)
#define DMAK(t) DMAK_(t, SLOT(t))
#define DMAV(t) DMAV_(t, SLOT((t) + 1))
    const char* tw = wo + (size_t)(blockIdx.x >> 3) * 8192 + lane * 128;
#define TW(r) do { const char* p_ = tw + (size_t)((((r) + ROTM * (lane & 7) + ROTA) & 15) * 262144); asm volatile("global_load_dword %0, %1, off" : "+v"(td) : "v"(p_) : "memory"); } while (0)
    unsigned t4, t5, t6, t7;
    asm volatile("global_load_dwordx2 %0, %1, off sc1" : "=v"(tag) : "v"(epochp) : "memory");
#define RW(i) do { _Pragma("unroll") for (int w_ = 0; w_ < 8; ++w_) DMA4("", lds0 + (unsigned)(RB(i) + 4096 * w_), ow0, ow1, ow2, ow3, srdWo, (unsigned)((RR(i) * 256 + 32 * w_) * 1024)); } while (0)
    DMAW2(0);
    BARRIER();
    DMAW(1);
    WAITV(16); BARRIER();
    DMAW(2);
    WAITV(16); BARRIER();
    DMAW(3);
    { const char* p4 = pfx + 512; const char* p5 = pfw + (size_t)2 * 128 * 8192; const char* p6 = pfx + 768; const char* p7 = pfw + (size_t)3 * 128 * 8192;
      TOUCH(t4, p4); TOUCH(t5, p5); TOUCH(t6, p6); TOUCH(t7, p7); }
    WAITV(20); BARRIER();
    WAITV(4); BARRIER();
    DMAW(4); DMAW(5);
    WAITV(16); BARRIER();
    KEEP(pfd0); KEEP(t4); KEEP(t5); KEEP(t6); KEEP(t7);
    DMAW(6);
    WAITV(16); BARRIER();
    DMAW(7);
    WAITV(16); BARRIER();
    DMAK(0); DMAK(1); DMAV(0);
    WAITV(48); BARRIER();
    BARRIER();
    DMAK(2);
    WAITV(32); BARRIER();
    DMAV(1); DMAK(3); DMAV(2);
    WAITV(48); BARRIER();
    DMAK(4); DMAV(3); DMAK(5); DMAV(4);
    WAITV(63); BARRIER();
    {
      int so = SLOT(1);
      for (int t = 1; t < 11; ++t) {
        DMAK_(t + 5, so); DMAV_(t + 4, so);
        so = (so == 131072) ? 0 : so + 32768;
        WAITV(63);
        BARRIER();
      }
    }
    DMAV(15); TW(0); TW(1); TW(2); TW(3);
    asm volatile("" : "+v"(tag));
    tag = (((unsigned long long)(unsigned)__builtin_amdgcn_readfirstlane((unsigned)(tag >> 32)) << 32) | (unsigned)__builtin_amdgcn_readfirstlane((unsigned)tag)) + 1;
    if (lane == 0) { *(__attribute__((address_space(3))) unsigned*)(L + PUBW) = 0u; *(__attribute__((address_space(3))) unsigned long long*)(L + PUBW + 8) = tag; }
    asm volatile("s_waitcnt lgkmcnt(0)" ::: "memory");
    WAITV(63);
    BARRIER();
    TW(5); TW(6); TW(7); TW(4);
    WAITV(40);
    RW(0);
    BARRIER();
    RW(1); TW(8); TW(10); TW(11); TW(12);
    WAITV(63);
    BARRIER();
    RW(2); TW(9); TW(13); TW(14); TW(15);
    WAITV(4);
    BARRIER();
    {
      const int rbo = RBO();
      WREAD(0); WREAD(1); WREAD(2);
    }
    BARRIER();
    RW(3); RW(5); RW(6); RW(7);
    WAITV(0);
#undef TW
#undef RW
#undef DMAW
#undef DMAK
#undef DMAV
  } else {
  const __amdgpu_buffer_rsrc_t srdX = __builtin_amdgcn_make_buffer_rsrc((void*)(x + (size_t)(b * SQ + wid * 32) * (DM * 2)), (short)0, 32 * DM * 2, 0x00020000);
  const unsigned xs = (lane >> 3) * 1024 + (((lane & 7) ^ (lane >> 4)) * 16);
  const unsigned xv0 = xs, xv1 = (xs ^ 64) + 7168, xv2 = xs + 2 * 7168, xv3 = (xs ^ 64) + 3 * 7168;
#define XDMA(c) DMA4(POLW, lds0 + (unsigned)XO(c) + 4096u * wid, xv0, xv1, xv2, xv3, srdX, (unsigned)(128 * (c)))
  const unsigned xrb = (unsigned)(wid * 4096 + r32 * 128 + ((hi ^ ((r32 >> 1) & 7)) * 16));
  bf16x8 xf[2][4];
#define XRD(c, k_) xf[(c) & 1][k_] = ldsrd128((lds_cptr)L + ((xrb ^ (unsigned)((k_) << 5)) + XO(c)))
  DMAW2(0); XDMA(0);
  BARRIER();
  XDMA(1);
  const lds_cptr wrb = (lds_cptr)L + 2048 * hi + 64 * tq + 32 * (tp & 1) + 16 * tblk + 8 * (tp >> 1);
  lds_cptr wrb2 = wrb + 131072; PIN(wrb2);
#define WRB(c) ((WQO(c) < 65536 ? wrb : wrb2) + (WQO(c) & 65535))
#define WRD(c, m) do { wlo[m] = ldstr(WRB(c) + 4096 * ((m) >> 2) + 256 * ((m) & 3)); whi[m] = ldstr(WRB(c) + 4096 * ((m) >> 2) + 1024 + 256 * ((m) & 3)); } while (0)
  AttnState st;
  f32x16 acc[4];
#pragma unroll
  for (int i = 0; i < 4; ++i) acc[i] = f32x16{};
  s16x4 wlo[16], whi[16];
  WAITV(4);
  XRD(0, 0); XRD(0, 1); XRD(0, 2); XRD(0, 3);
  STAMP(1);
  BARRIER();
  STAMP(2);
#pragma unroll
  for (int m = 0; m < PD; ++m) WRD(0, m);
  u32x4 qx[4], qy[4];
#define QCV1(nb, e_) do { \
      const unsigned lo_ = cvtpk(acc[nb][2 * (e_)] * QSCALE, acc[nb][2 * (e_) + 1] * QSCALE), hi_ = cvtpk(acc[nb][8 + 2 * (e_)] * QSCALE, acc[nb][9 + 2 * (e_)] * QSCALE); \
      auto r_ = __builtin_amdgcn_permlane16_swap(lo_, hi_, false, false); qx[nb][e_] = r_[0]; qy[nb][e_] = r_[1]; PIN(qx[nb]); PIN(qy[nb]); } while (0)
  static_for<128>([&](auto I) __attribute__((always_inline)) {
    constexpr int c = I.value >> 4, mm = I.value & 15;
    constexpr int m = (c == 7) ? (4 * (mm & 3) + (mm >> 2)) : mm;
    acc[m & 3] = MFMA(cat(wlo[m], whi[m]), xf[c & 1][m >> 2], acc[m & 3]);
    if constexpr (c == 0 && mm + PD == 16) { BARRIER(); }
    if constexpr (mm + PD < 16) { constexpr int f = mm + PD, ff = (c == 7) ? (4 * (f & 3) + (f >> 2)) : f; WRD(c, ff); }
    else if constexpr (c < 7) { constexpr int f = mm + PD - 16, ff = (c + 1 == 7) ? (4 * (f & 3) + (f >> 2)) : f; WRD(c + 1, ff); }
    if constexpr (c == 0 && mm == 0) { XDMA(2); }
    if constexpr (c == 0 && mm == 2) { XDMA(3); }
    if constexpr (c == 2 && mm == 8) { XDMA(4); }
    if constexpr (c == 3 && mm == 0) { XDMA(5); }
    if constexpr (c == 3 && mm == 8) { XDMA(6); }
    if constexpr (c == 4 && mm == 8) { XDMA(7); }
    if constexpr (c < 7) {
      if constexpr (mm == 4) { WAITV(c == 0 ? 8 : (c == 2 || c == 6) ? 0 : 4); }
      if constexpr (mm == 5 && c == 1) { KEEP(pfd0); }
      if constexpr (mm >= 4 && mm < 8) { XRD(c + 1, mm - 4); }
    }
    if constexpr (c == 5) { st.o[mm >> 3][mm & 7] = f32x4{}; PIN(st.o[mm >> 3][mm & 7]); }
    if constexpr (c == 7 && mm >= 5) { QCV1((mm - 5) >> 2, (mm - 5) & 3); }
    SBAR();
    if constexpr (mm == 15) { BARRIER(); STAMP(3 + c); SBAR(); }
  });
  LSTAMP(58);
#undef XDMA
#undef XRD
#undef WRD
  const int l16 = lane & 15, g4 = lane >> 4;
  const lds_cptr kb = (lds_cptr)L + 1024 * (l16 >> 2) + 64 * (l16 & 3) + 16 * (g4 ^ ((l16 >> 2) & 2));
  const lds_cptr vb0 = (lds_cptr)L + 16384 + 1024 * g4 + 64 * tq + 32 * (g4 & 1) + 8 * tp;
  const lds_cptr vb1 = (lds_cptr)L + 16384 + 1024 * g4 + 64 * tq + 32 * (1 - (g4 & 1)) + 8 * tp;
#pragma unroll
  for (int m = 0; m < PD; ++m) st.kf[m] = ldsrd128(kb + SLOT(0) + 4096 * (m >> 2) + 256 * (m & 3));
  QCV1(2, 3); QCV1(3, 0); QCV1(3, 1); QCV1(3, 2); QCV1(3, 3);
#undef QCV1
#pragma unroll
  for (int nb = 0; nb < 4; ++nb) { st.q[0][nb] = __builtin_bit_cast(bf16x8, qx[nb]); st.q[1][nb] = __builtin_bit_cast(bf16x8, qy[nb]); }
  st.negm[0] = f32x4{}; st.negm[1] = f32x4{}; PIN(st.negm[0]); PIN(st.negm[1]);
  st.mhat[0] = st.mhat[1] = 0.f; st.lacc[0] = f32x4{}; st.lacc[1] = f32x4{};
  st.ones = bf16x8{0x3F80, 0x3F80, 0x3F80, 0x3F80, 0x3F80, 0x3F80, 0x3F80, 0x3F80}; PIN(st.ones);

  STile A, B;
  attn_step<true>(st, A, B, kb + SLOT(0), vb0 + SLOT(0), vb1 + SLOT(0), kb + SLOT(1), wid >= 4);
  STAMP(11);
  BARRIER();
  STAMP(12);
  int s0 = SLOT(1);
  for (int t = 1; t < 15; t += 2) {
    const int s1 = (s0 == 131072) ? 0 : s0 + 32768, s2 = (s1 == 131072) ? 0 : s1 + 32768;
    attn_step<false>(st, B, A, kb + s0, vb0 + s0, vb1 + s0, kb + s1, wid >= 4);
    STAMP(11 + 2 * t);
    BARRIER();
    STAMP(12 + 2 * t);
    attn_step<false>(st, A, B, kb + s1, vb0 + s1, vb1 + s1, kb + s2, wid >= 4);
    STAMP(13 + 2 * t);
    BARRIER();
    STAMP(14 + 2 * t);
    s0 = s2;
  }
  attn_step<false>(st, B, A, kb + SLOT(15), vb0 + SLOT(15), vb1 + SLOT(15), kb + SLOT(15), wid >= 4);
  STAMP(41);
  BARRIER();
  STAMP(42); LSTAMP(59);
  {
    lds_cptr vd0 = vb0 + SLOT(16), vd1 = vb1 + SLOT(16); PIN(vd0); PIN(vd1);
#define VRD(m) do { st.vlo[m] = ldstr((((m) & 2) ? vd1 : vd0) + 8192 * ((m) & 1) + 256 * ((m) >> 2)); st.vhi[m] = ldstr((((m) & 2) ? vd1 : vd0) + 8192 * ((m) & 1) + 4096 + 256 * ((m) >> 2)); } while (0)
    const int rbo = RBO();
    WREAD(0); WREAD(1); WREAD(2);
    BARRIER();
#pragma unroll
    for (int m = 0; m < 8; ++m) VRD(m);
    static_for<22>([&](auto I) __attribute__((always_inline)) { constexpr int n = 10 + I.value; EL(B, n) = EX(EL(B, n)); });
    static_for<12>([&](auto I) __attribute__((always_inline)) { constexpr int n = 8 + 2 * I.value; PWV(n)[(n >> 1) & 3] = cvtpk(EL(B, n), EL(B, n + 1)); });
#pragma unroll
    for (int w = 0; w < 2; ++w) {
      st.lacc[0] = MFMA16(st.ones, __builtin_bit_cast(bf16x8, st.pw[0][w]), st.lacc[0]);
      st.lacc[1] = MFMA16(st.ones, __builtin_bit_cast(bf16x8, st.pw[1][w]), st.lacc[1]);
    }
    const float inv0 = 1.0f / st.lacc[0][0], inv1 = 1.0f / st.lacc[1][0];
    const __amdgpu_buffer_rsrc_t srdOs = __builtin_amdgcn_make_buffer_rsrc((void*)(ows + (size_t)(b * NH + h) * 57344), (short)0, 57344, 0x00020000);
    const int qr0 = 32 * wid + l16, vk0 = qr0 / 28, qr1 = qr0 + 16, vk1 = qr1 / 28;
    const unsigned osv0 = vk0 * 7168 + (g4 >> 1) * 896 + ((g4 & 1) * 28 + qr0 - 28 * vk0) * 16, osv1 = vk1 * 7168 + (g4 >> 1) * 896 + ((g4 & 1) * 28 + qr1 - 28 * vk1) * 16;
#define OSTORE(u) do { \
      u32x4 w0_ = {cvtpk(st.o[0][2 * (u)][0] * inv0, st.o[0][2 * (u)][1] * inv0), cvtpk(st.o[0][2 * (u)][2] * inv0, st.o[0][2 * (u)][3] * inv0), \
                   cvtpk(st.o[0][2 * (u) + 1][0] * inv0, st.o[0][2 * (u) + 1][1] * inv0), cvtpk(st.o[0][2 * (u) + 1][2] * inv0, st.o[0][2 * (u) + 1][3] * inv0)}; \
      __builtin_amdgcn_raw_buffer_store_b128(w0_, srdOs, osv0 + (u) * 1792, 0, 16); \
      u32x4 w1_ = {cvtpk(st.o[1][2 * (u)][0] * inv1, st.o[1][2 * (u)][1] * inv1), cvtpk(st.o[1][2 * (u)][2] * inv1, st.o[1][2 * (u)][3] * inv1), \
                   cvtpk(st.o[1][2 * (u) + 1][0] * inv1, st.o[1][2 * (u) + 1][1] * inv1), cvtpk(st.o[1][2 * (u) + 1][2] * inv1, st.o[1][2 * (u) + 1][3] * inv1)}; \
      __builtin_amdgcn_raw_buffer_store_b128(w1_, srdOs, osv1 + (u) * 1792, 0, 16); } while (0)
    SBAR();
#pragma unroll
    for (int u = 0; u < 4; ++u) {
#pragma unroll
      for (int k = 0; k < 4; ++k) {
        const int m = 4 * u + k, db = m >> 1, w = m & 1;
        st.o[0][db] = MFMA16(cat(st.vlo[m], st.vhi[m]), __builtin_bit_cast(bf16x8, st.pw[0][w]), st.o[0][db]);
        st.o[1][db] = MFMA16(cat(st.vlo[m], st.vhi[m]), __builtin_bit_cast(bf16x8, st.pw[1][w]), st.o[1][db]);
        if (u < 2) { VRD(m + 8); }
      }
      SBAR();
      if (u >= 1) { OSTORE(u - 1); }
      SBAR();
    }
    STAMP(43);
    OSTORE(3);
    STAMP(44);
#undef VRD
#undef OSTORE
    WAITV(0);
    if (lane == 0) {
      const unsigned old = __hip_atomic_fetch_add((__attribute__((address_space(3))) unsigned*)(L + PUBW), 1u, __ATOMIC_RELAXED, __HIP_MEMORY_SCOPE_WORKGROUP);
      if (old == 6) __hip_atomic_store(flags + b * NH + h, *(__attribute__((address_space(3))) unsigned long long*)(L + PUBW + 8), __ATOMIC_RELAXED, __HIP_MEMORY_SCOPE_AGENT);
    }
    asm volatile("s_waitcnt lgkmcnt(0)" ::: "memory");
    STAMP(50);
  }
  }

  {
    static_assert(NA == 8, "prefetch sequences above are written for NA = 8");
    const int rbo = RBO(); W3();
    const __amdgpu_buffer_rsrc_t srdO = __builtin_amdgcn_make_buffer_rsrc((void*)(ows + (size_t)ob * (NH * 57344)), (short)0, NH * 57344, 0x00020000);
    const unsigned ov0 = (hi * 28 + (r32 < 28 ? r32 : 27)) * 16;
    const unsigned osb = (w3 >> 2) * 57344 + (w3 & 3) * 1792 + oqt * 2 * 7168;
    u32x4 of[3][4];
    f32x16 acc[2][2];
#pragma unroll
    for (int i = 0; i < 2; ++i) { acc[i][0] = f32x16{}; acc[i][1] = f32x16{}; }
#define OLOAD(i) do { const unsigned so_ = osb + (unsigned)(RR(i) * 2 * 57344), so1_ = so_ + 7168u; \
      BLOAD("sc1", of[(i) % 3][0], ov0, srdO, so_, 0); BLOAD("sc1", of[(i) % 3][1], ov0, srdO, so1_, 0); \
      BLOAD("sc1", of[(i) % 3][2], ov0, srdO, so_, 896); BLOAD("sc1", of[(i) % 3][3], ov0, srdO, so1_, 896); } while (0)
    BARRIER();
    LSTAMP(57);
    STAMP(51);
    WDMA(4);
    WREAD(3); WDMA(8); WREAD(5); WDMA(10); WREAD(6); WDMA(11); WREAD(7); WDMA(12);
    WAITV(16); WREAD(4); WDMA(9);
    STAMP(52);
    if (w3 == 7) {
      const unsigned long long* fp = flags + ob * NH + r32;
      for (unsigned spins = 0; spins < (1u << 18); ++spins) {
        const unsigned long long v = __hip_atomic_load(fp, __ATOMIC_RELAXED, __HIP_MEMORY_SCOPE_AGENT);
        if (__all(v == tag)) break;
        __builtin_amdgcn_s_sleep(1);
      }
      KEEP(td);
    }
    STAMP(53);
    BARRIER();
    LSTAMP(60);
    STAMP(54);
    OLOAD(0); OLOAD(1);
#pragma unroll
    for (int i = 0; i < 16; ++i) {
      const int cnt = (i < 15 ? 4 : 0) + ((i - 1 >= 1 && i - 1 <= 3) ? 4 : 0);
      WAITV4(cnt, of[i % 3][0], of[i % 3][1], of[i % 3][2], of[i % 3][3]);
      if (i >= 1 && i <= 3) { WRWAIT(i - 1 + NA); WDMA(i + 12); }
      if (i + 2 < 16) { OLOAD(i + 2); }
      SBAR();
#pragma unroll
      for (int ks = 0; ks < 2; ++ks)
#pragma unroll
        for (int nb = 0; nb < 2; ++nb)
#pragma unroll
          for (int vb = 0; vb < 2; ++vb)
            acc[nb][vb] = MFMA(wf[i % NA][2 * ks + nb], __builtin_bit_cast(bf16x8, of[i % 3][2 * ks + vb]), acc[nb][vb]);
      SBAR();
      if (i + NA < 16) { WREAD_NW(i + NA); }
      SBAR();
    }
    STAMP(55);
#undef WDMA
#undef WREAD
#undef OLOAD
#undef RBP
#pragma unroll
    for (int vb = 0; vb < 2; ++vb)
#pragma unroll
      for (int nb = 0; nb < 2; ++nb)
#pragma unroll
        for (int rg = 0; rg < 4; ++rg) {
          const int q = 32 * vb + r32, slot = 8 * nb + 2 * rg + hi;
          f32x4 v = {acc[nb][vb][4 * rg], acc[nb][vb][4 * rg + 1], acc[nb][vb][4 * rg + 2], acc[nb][vb][4 * rg + 3]};
          *(__attribute__((address_space(3))) f32x4*)(L + (q >> 4) * 32768 + wid * 4096 + (q & 15) * 256 + ((slot ^ (q & 15)) * 16)) = v;
        }
    asm volatile("s_waitcnt lgkmcnt(0)" ::: "memory");
    BARRIER();
    STAMP(56); LSTAMP(61);
    const int q = tid >> 3, pc = tid & 7;
    f32x4 s0 = {0, 0, 0, 0}, s1 = {0, 0, 0, 0};
#pragma unroll
    for (int w = 0; w < 8; ++w) {
      s0 += *(const __attribute__((address_space(3))) f32x4*)(L + (q >> 4) * 32768 + w * 4096 + (q & 15) * 256 + (((2 * pc) ^ (q & 15)) * 16));
      s1 += *(const __attribute__((address_space(3))) f32x4*)(L + (q >> 4) * 32768 + w * 4096 + (q & 15) * 256 + (((2 * pc + 1) ^ (q & 15)) * 16));
    }
    u32x4 w = {cvtpk(s0[0], s0[1]), cvtpk(s0[2], s0[3]), cvtpk(s1[0], s1[1]), cvtpk(s1[2], s1[3])};
    if ((q & 31) < 28)
      *(u32x4*)(out + (size_t)(ob * SQ + 56 * oqt + 28 * (q >> 5) + (q & 31)) * (DM * 2) + ont * 128 + pc * 16) = w;
    if (blockIdx.x == 0 && wid == 7) {
      for (unsigned spins = 0; spins < (1u << 18); ++spins) {
        bool ok = true;
#pragma unroll
        for (int k = 0; k < 4; ++k) ok &= __hip_atomic_load(flags + 64 * k + lane, __ATOMIC_RELAXED, __HIP_MEMORY_SCOPE_AGENT) == tag;
        if (__all(ok)) break;
        __builtin_amdgcn_s_sleep(2);
      }
      if (lane == 0) __hip_atomic_store(epochp, tag, __ATOMIC_RELAXED, __HIP_MEMORY_SCOPE_AGENT);
    }
  }
  STAMP(40); RSTAMP(63);
  STFLUSH(ows + (64 << 20));
}

extern "C" void kernel_launch(void* const* d_in, const int* in_sizes, int n_in,
                              void* d_out, int out_size, void* d_ws, size_t ws_size,
                              hipStream_t stream) {
  (void)in_sizes; (void)n_in; (void)out_size; (void)ws_size;
  (void)hipFuncSetAttribute((const void*)mha_k1, hipFuncAttributeMaxDynamicSharedMemorySize, K1_LDS);
  mha_k1<<<256, 512, K1_LDS, stream>>>((const char*)d_in[0], (const char*)d_in[1], (const char*)d_in[2], (const char*)d_in[3], (const char*)d_in[4], (char*)d_ws, (char*)d_out);
}
```

```cpp
#include <hip/hip_runtime.h>
#include <hip/hip_bf16.h>
#include <stdint.h>
#include <utility>
#include <type_traits>

typedef short    bf16x8 __attribute__((ext_vector_type(8)));
typedef short    s16x4  __attribute__((ext_vector_type(4)));
typedef float    f32x16 __attribute__((ext_vector_type(16)));
typedef float    f32x4  __attribute__((ext_vector_type(4)));
typedef float    f32x2  __attribute__((ext_vector_type(2)));
typedef __bf16   bf16x2 __attribute__((ext_vector_type(2)));
typedef unsigned u32x4  __attribute__((ext_vector_type(4)));
typedef __attribute__((address_space(3))) char* lds_ptr;
typedef __attribute__((address_space(3))) const char* lds_cptr;

#define DI __device__ __forceinline__
#define SBAR() __builtin_amdgcn_sched_barrier(0)
#define PIN(x) asm volatile("" : "+v"(x))
#define MFMA(a, b, c) __builtin_amdgcn_mfma_f32_32x32x16_bf16(a, b, c, 0, 0, 0)
#define BARRIER() asm volatile("s_barrier" ::: "memory")

#define POLT "nt"
#define POLW ""
constexpr int PRIO_TOGGLE = 21;
constexpr int P3NA = 8;
constexpr int NB = 8, SQ = 224, DM = 512, NH = 32, SK = 1024, DH = 128;
constexpr float QSCALE = 0.088388347648318440f * 1.4426950408889634f;
constexpr float THR = 8.0f;

template <int... Is, class F> DI void static_for_(std::integer_sequence<int, Is...>, F&& f) { (f(std::integral_constant<int, Is>{}), ...); }
template <int N, class F> DI void static_for(F&& f) { static_for_(std::make_integer_sequence<int, N>{}, f); }
DI unsigned cvtpk(float lo, float hi) { f32x2 v = {lo, hi}; bf16x2 b = __builtin_convertvector(v, bf16x2); return __builtin_bit_cast(unsigned, b); }
DI bf16x8 ldsrd128(lds_cptr p) { return *(const __attribute__((address_space(3))) bf16x8*)p; }
DI s16x4 ldstr(lds_cptr p) { return __builtin_bit_cast(s16x4, __builtin_amdgcn_ds_read_tr16_b64_v4i16((__attribute__((address_space(3))) s16x4*)p)); }
DI bf16x8 cat(s16x4 a, s16x4 b) { return __builtin_shufflevector(a, b, 0, 1, 2, 3, 4, 5, 6, 7); }
#define MX3(a, b, c) __builtin_fmaxf(__builtin_fmaxf((a), (b)), (c))

#define DMA4(POL, m0v, v0, v1, v2, v3, srd, soff)                                                     \
  asm volatile("s_mov_b32 m0, %0\n\ts_nop 0\n\t"                                                      \
               "buffer_load_dwordx4 %1, %5, %6 offen " POL " lds\n\t"                                 \
               "buffer_load_dwordx4 %2, %5, %6 offen offset:1024 " POL " lds\n\t"                     \
               "buffer_load_dwordx4 %3, %5, %6 offen offset:2048 " POL " lds\n\t"                     \
               "buffer_load_dwordx4 %4, %5, %6 offen offset:3072 " POL " lds"                         \
               :: "s"(m0v), "v"(v0), "v"(v1), "v"(v2), "v"(v3), "s"(srd), "s"(soff) : "memory")
#define DMA2(POL, m0v, v0, v1, srd, soff)                                                             \
  asm volatile("s_mov_b32 m0, %0\n\ts_nop 0\n\t"                                                      \
               "buffer_load_dwordx4 %1, %3, %4 offen " POL " lds\n\t"                                 \
               "buffer_load_dwordx4 %2, %3, %4 offen offset:1024 " POL " lds"                         \
               :: "s"(m0v), "v"(v0), "v"(v1), "s"(srd), "s"(soff) : "memory")
#define BLOAD(POL, dst, voff, srd, soff, imm)                                                         \
  asm volatile("buffer_load_dwordx4 %0, %1, %2, %3 offen offset:%4 " POL                              \
               : "=v"(dst) : "v"(voff), "s"(srd), "s"(soff), "i"(imm) : "memory")
#define WAITV(n) asm volatile("s_waitcnt vmcnt(%0)" :: "i"(n) : "memory")
#define WAITV4(n, a, b, c, d) asm volatile("s_waitcnt vmcnt(%4)" : "+v"(a), "+v"(b), "+v"(c), "+v"(d) : "i"(n) : "memory")

constexpr int K1_LDS = 163840;
constexpr size_t WS_FLAGS = (size_t)32 << 20, WS_EPOCH = WS_FLAGS + 4096;
DI constexpr int SLOT(int t) { return (t % 5) * 32768; }
DI constexpr int RB(int i) { return ((i + 2) % 5) * 32768; }
constexpr int PUBW = 32768;
DI constexpr int WQO(int c) { return (c & 3) == 0 ? 0 : (c & 3) == 1 ? 16384 : (c & 3) == 2 ? 131072 : 147456; }
DI constexpr int XO(int c) { return (c % 3) == 0 ? 65536 : (c % 3) == 1 ? 98304 : 32768; }

#define MFMA16(a, b, c) __builtin_amdgcn_mfma_f32_16x16x32_bf16(a, b, c, 0, 0, 0)
struct AttnState {
  f32x4 o[2][8];
  bf16x8 q[2][4];
  f32x4 negm[2];
  float mhat[2];
  f32x4 lacc[2];
  bf16x8 ones;
  bf16x8 kf[16];
  s16x4 vlo[16], vhi[16];
  u32x4 pw[2][2];
};
struct STile { f32x4 s[2][4]; };
DI float xmax4(float v) {
  auto a = __builtin_amdgcn_permlane16_swap(__float_as_uint(v), __float_as_uint(v), false, false);
  v = __builtin_fmaxf(__uint_as_float(a[0]), __uint_as_float(a[1]));
  auto c = __builtin_amdgcn_permlane32_swap(__float_as_uint(v), __float_as_uint(v), false, false);
  return __builtin_fmaxf(__uint_as_float(c[0]), __uint_as_float(c[1]));
}

constexpr int PD = 5;
#define EL(T, n) (T).s[((n) >> 3) & 1][2 * ((n) >> 4) + (((n) >> 2) & 1)][(n) & 3]
#define ELV(T, n) (T).s[((n) >> 3) & 1][2 * ((n) >> 4) + (((n) >> 2) & 1)]
#define PWV(n) st.pw[((n) >> 3) & 1][(n) >> 4]
#define EX(v) __builtin_amdgcn_exp2f(v)
template <bool FIRST>
DI void attn_step(AttnState& st, STile& C, STile& P, lds_cptr kp, lds_cptr vp0, lds_cptr vp1, lds_cptr kn, const bool young) {
#define KRD(m, base) st.kf[m] = ldsrd128((base) + 4096 * ((m) >> 2) + 256 * ((m) & 3))
#define VRD(m) do { st.vlo[m] = ldstr((((m) & 1) ? vp1 : vp0) + 8192 * ((m) >> 3) + 256 * (((m) & 7) >> 1)); \
                    st.vhi[m] = ldstr((((m) & 1) ? vp1 : vp0) + 8192 * ((m) >> 3) + 4096 + 256 * (((m) & 7) >> 1)); } while (0)
  float ma = -3.0e38f, mb = -3.0e38f, fct0 = 1.f, fct1 = 1.f;
  bool resc = false;
  SBAR();
  static_for<32>([&](auto I) __attribute__((always_inline)) {
    constexpr int g = I.value;
    if constexpr (g == 0) { if (young) __builtin_amdgcn_s_setprio(1); }
    if constexpr (g == PRIO_TOGGLE) { __builtin_amdgcn_s_setprio(0); }
    if constexpr (g < 16) {
      constexpr int kb = g >> 2, u = g & 3;
      if constexpr (u == 0) C.s[0][kb] = MFMA16(st.kf[g], st.q[0][0], st.negm[0]); else C.s[0][kb] = MFMA16(st.kf[g], st.q[0][u], C.s[0][kb]);
      PIN(C.s[0][kb]);
    } else if constexpr (!FIRST) {
      constexpr int m = g - 16;
      st.o[0][m & 7] = MFMA16(cat(st.vlo[m], st.vhi[m]), __builtin_bit_cast(bf16x8, st.pw[0][m >> 3]), st.o[0][m & 7]);
      PIN(st.o[0][m & 7]);
    }
    SBAR();
    {
      constexpr int f = g + PD;
      if constexpr (f < 16) { KRD(f, kp); }
      else if constexpr (f < 32) { if constexpr (!FIRST) { VRD(f - 16); } }
      else { KRD(f - 32, kn); }
    }
    if constexpr ((g & 1) && g >= 5 && g <= 19) {
      constexpr int kb = (g - 5) >> 2, hf = ((g - 5) >> 1) & 1;
      ma = MX3(ma, C.s[0][kb][2 * hf], C.s[0][kb][2 * hf + 1]); PIN(ma);
    }
    SBAR();
    if constexpr (g < 16) {
      constexpr int kb = g >> 2, u = g & 3;
      if constexpr (u == 0) C.s[1][kb] = MFMA16(st.kf[g], st.q[1][0], st.negm[1]); else C.s[1][kb] = MFMA16(st.kf[g], st.q[1][u], C.s[1][kb]);
      PIN(C.s[1][kb]);
    } else if constexpr (!FIRST) {
      constexpr int m = g - 16;
      st.o[1][m & 7] = MFMA16(cat(st.vlo[m], st.vhi[m]), __builtin_bit_cast(bf16x8, st.pw[1][m >> 3]), st.o[1][m & 7]);
      PIN(st.o[1][m & 7]);
    }
    SBAR();
    if constexpr ((g & 1) == 0) {
      if constexpr (g <= 22) {
        if constexpr (!FIRST) { constexpr int n = 8 + g; PWV(n)[(n >> 1) & 3] = cvtpk(EL(P, n), EL(P, n + 1)); PIN(PWV(n)); }
      } else { constexpr int n = g - 24; PWV(n)[(n >> 1) & 3] = cvtpk(EL(C, n), EL(C, n + 1)); PIN(PWV(n)); }
    }
    if constexpr ((g & 1) && g >= 5 && g <= 19) {
      constexpr int kb = (g - 5) >> 2, hf = ((g - 5) >> 1) & 1;
      mb = MX3(mb, C.s[1][kb][2 * hf], C.s[1][kb][2 * hf + 1]); PIN(mb);
    }
    if constexpr (g == 22) {
      const bool ok = (ma <= THR) & (mb <= THR) & (!FIRST | ((ma >= -THR) & (mb >= -THR)));
      if (__builtin_expect(!__all(ok), 0)) {
#pragma unroll
        for (int qb = 0; qb < 2; ++qb) {
          float rm = qb ? mb : ma;
          rm = xmax4(rm);
          const float dl = FIRST ? rm : __builtin_fmaxf(rm, 0.f);
          st.mhat[qb] += dl;
#pragma unroll
          for (int kb = 0; kb < 4; ++kb)
#pragma unroll
            for (int r = 0; r < 4; ++r) C.s[qb][kb][r] -= dl;
#pragma unroll
          for (int r = 0; r < 4; ++r) st.negm[qb][r] = -st.mhat[qb];
          PIN(st.negm[qb]);
          if constexpr (!FIRST) { const float f_ = EX(-dl); if (qb == 0) fct0 = f_; else fct1 = f_; }
        }
        resc = true;
      }
    }
    if constexpr (g <= 21) {
      if constexpr (!FIRST) { constexpr int n = 10 + g; EL(P, n) = EX(EL(P, n)); PIN(ELV(P, n)); }
    } else { constexpr int n = g - 22; EL(C, n) = EX(EL(C, n)); PIN(ELV(C, n)); }
    SBAR();
    if constexpr (!FIRST && (g == 21 || g == 23 || g == 25 || g == 27)) {
      constexpr int qb = (g >> 1) & 1, w = g >= 24;
      st.lacc[qb] = MFMA16(st.ones, __builtin_bit_cast(bf16x8, st.pw[qb][w]), st.lacc[qb]); PIN(st.lacc[qb]);
      SBAR();
    }
  });
  if constexpr (!FIRST) {
    if (__builtin_expect(resc, 0)) {
#pragma unroll
      for (int d = 0; d < 8; ++d)
#pragma unroll
        for (int r = 0; r < 4; ++r) { st.o[0][d][r] *= fct0; st.o[1][d][r] *= fct1; }
#pragma unroll
      for (int r = 0; r < 4; ++r) { st.lacc[0][r] *= fct0; st.lacc[1][r] *= fct1; }
    }
  }
#undef KRD
#undef VRD
}

__global__ __launch_bounds__(512, 2)
void mha_k1(const char* __restrict__ x, const char* __restrict__ kc, const char* __restrict__ vc,
            const char* __restrict__ wq, const char* __restrict__ wo, char* ows, char* __restrict__ out) {
  extern __shared__ __attribute__((aligned(16))) char lds[];
  const int tid = threadIdx.x, lane = tid & 63;
  const int wid = __builtin_amdgcn_readfirstlane(tid >> 6);
  const int b = 4 * (blockIdx.x & 1) + ((blockIdx.x >> 3) & 3), h = 8 * ((blockIdx.x >> 1) & 3) + (blockIdx.x >> 5);
  const unsigned lds0 = (unsigned)(uintptr_t)lds;
  const lds_ptr L = (lds_ptr)lds;

  const char *pfx, *pfw;
  {
    const int j = blockIdx.x >> 3, half = lane >> 5, l5 = lane & 31;
    const int xrow = (b & 4) * SQ + 28 * j + (l5 < 28 ? l5 : 27);
    const int idx = 32 * j + l5;
    pfw = wq + (size_t)(64 * half + (idx >> 4)) * 8192 + (h & 24) * 256 + (idx & 15) * 128;
    pfx = x + (size_t)xrow * (DM * 2) + half * 128;
  }
  unsigned pfd0;
  const char* pfe = wid >= 4 ? x : (wid & 1) ? pfw + (size_t)(wid >> 1) * (128 * 8192) : pfx + (wid >> 1) * 256;
#define TOUCH(d, p) asm volatile("global_load_dword %0, %1, off " POLT : "=v"(d) : "v"(p) : "memory")
#define KEEP(d) asm volatile("" :: "v"(d) : "memory")
  const __amdgpu_buffer_rsrc_t srdW = __builtin_amdgcn_make_buffer_rsrc((void*)(wq + h * 256), (short)0, DM * 8192 - h * 256, 0x00020000);
  const unsigned pl = ((lane >> 2) & 3), dbl = lane >> 4, c4 = lane & 3;
  const unsigned wv0 = pl * 8192 + dbl * 64 + c4 * 16, wv1 = wv0 + 31744;
#define DMAW2(c) DMA2(POLW, lds0 + WQO(c) + 2048 * wid, wv0, wv1, srdW, (unsigned)(((c) * 64 + 8 * wid) * 8192))
  TOUCH(pfd0, pfe);
  const int ob = blockIdx.x & 7, oqt = blockIdx.x >> 6, ont = (blockIdx.x >> 3) & 7;
  const __amdgpu_buffer_rsrc_t srdWo = __builtin_amdgcn_make_buffer_rsrc((void*)(wo + ont * 128), (short)0, NH * DH * DM * 2 - ont * 128, 0x00020000);
  const unsigned ow0 = (4 * (lane >> 5) + ((lane >> 2) & 3)) * 1024 + ((lane >> 4) & 1) * 64 + (lane & 3) * 16;
  const unsigned ow1 = ow0 + 7168, ow2 = ow0 + 2 * 7168, ow3 = ow0 + 3 * 7168;
  unsigned long long* const epochp = (unsigned long long*)(ows + WS_EPOCH);
  unsigned long long* const flags = (unsigned long long*)(ows + WS_FLAGS);
  unsigned long long tag = 0;
  unsigned td = 0;
#define RR(i) (i)
  constexpr int NA = P3NA;
  const int r32 = lane & 31, hi = lane >> 5, tq = (lane >> 2) & 3, tp = lane & 3, tblk = (lane >> 4) & 1;
  bf16x8 wf[NA][4];
#define RBP(i) ((lds_cptr)L + RB(i) + rbo)
#define WDMA(i) DMA4("", lds0 + (unsigned)(RB(i)) + 4096u * w3, ow0, ow1, ow2, ow3, srdWo, 32768u * w3 + (unsigned)(RR(i) * 256 * 1024))
#define W3() unsigned w3 = (unsigned)__builtin_amdgcn_readfirstlane(tid >> 6); asm volatile("s_nop 4" : "+s"(w3))
#define WREAD_NW(i) do { lds_cptr p_ = RBP(i); PIN(p_); _Pragma("unroll") for (int f_ = 0; f_ < 4; ++f_) \
      wf[(i) % NA][f_] = cat(ldstr(p_ + 1024 * (f_ >> 1) + 256 * (f_ & 1)), ldstr(p_ + 1024 * (f_ >> 1) + 2048 + 256 * (f_ & 1))); } while (0)
#define WRWAIT(i) asm volatile("s_waitcnt lgkmcnt(0)" : "+v"(wf[(i) % NA][0]), "+v"(wf[(i) % NA][1]), "+v"(wf[(i) % NA][2]), "+v"(wf[(i) % NA][3]) :: "memory")
#define WREAD(i) do { WREAD_NW(i); WRWAIT(i); } while (0)
#define RBO() (wid * 4096 + 512 * hi + 64 * tq + 32 * tblk + 8 * tp)
  if (wid == 7) {
    const __amdgpu_buffer_rsrc_t srdK = __builtin_amdgcn_make_buffer_rsrc((void*)(kc + (size_t)(b * NH + h) * (SK * DH * 2)), (short)0, SK * DH * 2, 0x00020000);
    const __amdgpu_buffer_rsrc_t srdV = __builtin_amdgcn_make_buffer_rsrc((void*)(vc + (size_t)(b * NH + h) * (SK * DH * 2)), (short)0, SK * DH * 2, 0x00020000);
    const unsigned wv2 = wv0 + 2 * 31744, wv3 = wv0 + 3 * 31744;
    const unsigned kvA = pl * 256 + dbl * 64 + c4 * 16, kvB = pl * 256 + dbl * 64 + ((c4 ^ 2) * 16);
#define DMAW(c) do { _Pragma("unroll") for (int i_ = 0; i_ < 4; ++i_) DMA4(POLW, lds0 + (unsigned)(WQO(c) + 4096 * i_), wv0, wv1, wv2, wv3, srdW, (unsigned)(((c) * 64 + 16 * i_) * 8192)); } while (0)
#define DMAK_(t, so) do { _Pragma("unroll") for (int i_ = 0; i_ < 4; ++i_) { WAITV(59); DMA4("sc1 nt", lds0 + (unsigned)((so) + 4096 * i_), kvA, kvA, kvB, kvB, srdK, (unsigned)((t) * 16384 + 4096 * i_)); } } while (0)
#define DMAV_(t, so) do { _Pragma("unroll") for (int i_ = 0; i_ < 4; ++i_) { WAITV(59); DMA4("sc1 nt", lds0 + (unsigned)((so) + 16384 + 4096 * i_), kvA, kvB, kvA, kvB, srdV, (unsigned)((t) * 16384 + 4096 * i_)); } } while (0)
#define DMAK(t) DMAK_(t, SLOT(t))
#define DMAV(t) DMAV_(t, SLOT((t) + 1))
    const char* tw = wo + (size_t)(blockIdx.x >> 3) * 8192 + lane * 128;
#define TW(r) do { const char* p_ = tw + (size_t)(r) * 262144; asm volatile("s_waitcnt vmcnt(62)\n\tglobal_load_dword %0, %1, off" : "+v"(td) : "v"(p_) : "memory"); } while (0)
    unsigned t4, t5, t6, t7;
    asm volatile("global_load_dwordx2 %0, %1, off sc1" : "=v"(tag) : "v"(epochp) : "memory");
#define RW(i) do { _Pragma("unroll") for (int w_ = 0; w_ < 8; ++w_) { WAITV(59); DMA4("", lds0 + (unsigned)(RB(i) + 4096 * w_), ow0, ow1, ow2, ow3, srdWo, (unsigned)((RR(i) * 256 + 32 * w_) * 1024)); } } while (0)
    DMAW2(0);
    BARRIER();
    DMAW(1);
    WAITV(16); BARRIER();
    DMAW(2);
    WAITV(16); BARRIER();
    DMAW(3);
    { const char* p4 = pfx + 512; const char* p5 = pfw + (size_t)2 * 128 * 8192; const char* p6 = pfx + 768; const char* p7 = pfw + (size_t)3 * 128 * 8192;
      TOUCH(t4, p4); TOUCH(t5, p5); TOUCH(t6, p6); TOUCH(t7, p7); }
    WAITV(20); BARRIER();
    WAITV(4); BARRIER();
    DMAW(4); DMAW(5);
    WAITV(16); BARRIER();
    KEEP(pfd0); KEEP(t4); KEEP(t5); KEEP(t6); KEEP(t7);
    DMAW(6);
    WAITV(16); BARRIER();
    DMAW(7);
    WAITV(16); BARRIER();
    DMAK(0); DMAK(1); DMAV(0);
    WAITV(48); BARRIER();
    BARRIER();
    DMAK(2);
    WAITV(32); BARRIER();
    DMAV(1); DMAK(3); DMAV(2);
    WAITV(48); BARRIER();
    DMAK(4); DMAV(3); DMAK(5); DMAV(4);
    WAITV(63); BARRIER();
    {
      int so = SLOT(1);
      for (int t = 1; t < 11; ++t) {
        DMAK_(t + 5, so); DMAV_(t + 4, so);
        so = (so == 131072) ? 0 : so + 32768;
        WAITV(63);
        BARRIER();
      }
    }
    DMAV(15); TW(0); TW(1); TW(2); TW(3);
    asm volatile("" : "+v"(tag));
    tag = (((unsigned long long)(unsigned)__builtin_amdgcn_readfirstlane((unsigned)(tag >> 32)) << 32) | (unsigned)__builtin_amdgcn_readfirstlane((unsigned)tag)) + 1;
    if (lane == 0) { *(__attribute__((address_space(3))) unsigned*)(L + PUBW) = 0u; *(__attribute__((address_space(3))) unsigned long long*)(L + PUBW + 8) = tag; }
    asm volatile("s_waitcnt lgkmcnt(0)" ::: "memory");
    WAITV(63);
    BARRIER();
    TW(5); TW(6); TW(7); TW(4);
    WAITV(40);
    RW(0);
    BARRIER();
    RW(1); TW(8); TW(10); TW(11); TW(12);
    WAITV(63);
    BARRIER();
    RW(2); TW(9); TW(13); TW(14); TW(15);
    WAITV(4);
    BARRIER();
    {
      const int rbo = RBO();
      WREAD(0); WREAD(1); WREAD(2);
    }
    BARRIER();
    RW(3); RW(5); RW(6); RW(7);
    WAITV(0);
#undef TW
#undef RW
#undef DMAW
#undef DMAK
#undef DMAV
  } else {
  const __amdgpu_buffer_rsrc_t srdX = __builtin_amdgcn_make_buffer_rsrc((void*)(x + (size_t)(b * SQ + wid * 32) * (DM * 2)), (short)0, 32 * DM * 2, 0x00020000);
  const unsigned xs = (lane >> 3) * 1024 + (((lane & 7) ^ (lane >> 4)) * 16);
  const unsigned xv0 = xs, xv1 = (xs ^ 64) + 7168, xv2 = xs + 2 * 7168, xv3 = (xs ^ 64) + 3 * 7168;
#define XDMA(c) DMA4(POLW, lds0 + (unsigned)XO(c) + 4096u * wid, xv0, xv1, xv2, xv3, srdX, (unsigned)(128 * (c)))
  const unsigned xrb = (unsigned)(wid * 4096 + r32 * 128 + ((hi ^ ((r32 >> 1) & 7)) * 16));
  bf16x8 xf[2][4];
#define XRD(c, k_) xf[(c) & 1][k_] = ldsrd128((lds_cptr)L + ((xrb ^ (unsigned)((k_) << 5)) + XO(c)))
  DMAW2(0); XDMA(0);
  BARRIER();
  XDMA(1);
  const lds_cptr wrb = (lds_cptr)L + 2048 * hi + 64 * tq + 32 * (tp & 1) + 16 * tblk + 8 * (tp >> 1);
  lds_cptr wrb2 = wrb + 131072; PIN(wrb2);
#define WRB(c) ((WQO(c) < 65536 ? wrb : wrb2) + (WQO(c) & 65535))
#define WRD(c, m) do { wlo[m] = ldstr(WRB(c) + 4096 * ((m) >> 2) + 256 * ((m) & 3)); whi[m] = ldstr(WRB(c) + 4096 * ((m) >> 2) + 1024 + 256 * ((m) & 3)); } while (0)
  AttnState st;
  f32x16 acc[4];
#pragma unroll
  for (int i = 0; i < 4; ++i) acc[i] = f32x16{};
  s16x4 wlo[16], whi[16];
  WAITV(4);
  XRD(0, 0); XRD(0, 1); XRD(0, 2); XRD(0, 3);
  BARRIER();
#pragma unroll
  for (int m = 0; m < PD; ++m) WRD(0, m);
  u32x4 qx[4], qy[4];
#define QCV1(nb, e_) do { \
      const unsigned lo_ = cvtpk(acc[nb][2 * (e_)] * QSCALE, acc[nb][2 * (e_) + 1] * QSCALE), hi_ = cvtpk(acc[nb][8 + 2 * (e_)] * QSCALE, acc[nb][9 + 2 * (e_)] * QSCALE); \
      auto r_ = __builtin_amdgcn_permlane16_swap(lo_, hi_, false, false); qx[nb][e_] = r_[0]; qy[nb][e_] = r_[1]; PIN(qx[nb]); PIN(qy[nb]); } while (0)
  static_for<128>([&](auto I) __attribute__((always_inline)) {
    constexpr int c = I.value >> 4, mm = I.value & 15;
    constexpr int m = (c == 7) ? (4 * (mm & 3) + (mm >> 2)) : mm;
    acc[m & 3] = MFMA(cat(wlo[m], whi[m]), xf[c & 1][m >> 2], acc[m & 3]); PIN(acc[m & 3]);
    if constexpr (c == 0 && mm + PD == 16) { BARRIER(); }
    if constexpr (mm + PD < 16) { constexpr int f = mm + PD, ff = (c == 7) ? (4 * (f & 3) + (f >> 2)) : f; WRD(c, ff); }
    else if constexpr (c < 7) { constexpr int f = mm + PD - 16, ff = (c + 1 == 7) ? (4 * (f & 3) + (f >> 2)) : f; WRD(c + 1, ff); }
    if constexpr (c == 0 && mm == 0) { XDMA(2); }
    if constexpr (c == 0 && mm == 2) { XDMA(3); }
    if constexpr (c == 2 && mm == 8) { XDMA(4); }
    if constexpr (c == 3 && mm == 0) { XDMA(5); }
    if constexpr (c == 3 && mm == 8) { XDMA(6); }
    if constexpr (c == 4 && mm == 8) { XDMA(7); }
    if constexpr (c < 7) {
      if constexpr (mm == 4) { WAITV(c == 0 ? 8 : (c == 2 || c == 6) ? 0 : 4); }
      if constexpr (mm == 5 && c == 1) { KEEP(pfd0); }
      if constexpr (mm >= 4 && mm < 8) { XRD(c + 1, mm - 4); }
    }
    if constexpr (c == 5) { st.o[mm >> 3][mm & 7] = f32x4{}; PIN(st.o[mm >> 3][mm & 7]); }
    if constexpr (c == 7 && mm >= 5) { QCV1((mm - 5) >> 2, (mm - 5) & 3); }
    SBAR();
    if constexpr (mm == 15) { BARRIER(); SBAR(); }
  });
#undef XDMA
#undef XRD
#undef WRD
  const int l16 = lane & 15, g4 = lane >> 4;
  const lds_cptr kb = (lds_cptr)L + 1024 * (l16 >> 2) + 64 * (l16 & 3) + 16 * (g4 ^ ((l16 >> 2) & 2));
  const lds_cptr vb0 = (lds_cptr)L + 16384 + 1024 * g4 + 64 * tq + 32 * (g4 & 1) + 8 * tp;
  const lds_cptr vb1 = (lds_cptr)L + 16384 + 1024 * g4 + 64 * tq + 32 * (1 - (g4 & 1)) + 8 * tp;
#pragma unroll
  for (int m = 0; m < PD; ++m) st.kf[m] = ldsrd128(kb + SLOT(0) + 4096 * (m >> 2) + 256 * (m & 3));
  QCV1(2, 3); QCV1(3, 0); QCV1(3, 1); QCV1(3, 2); QCV1(3, 3);
#undef QCV1
#pragma unroll
  for (int nb = 0; nb < 4; ++nb) { st.q[0][nb] = __builtin_bit_cast(bf16x8, qx[nb]); st.q[1][nb] = __builtin_bit_cast(bf16x8, qy[nb]); }
  st.negm[0] = f32x4{}; st.negm[1] = f32x4{}; PIN(st.negm[0]); PIN(st.negm[1]);
  st.mhat[0] = st.mhat[1] = 0.f; st.lacc[0] = f32x4{}; st.lacc[1] = f32x4{};
  st.ones = bf16x8{0x3F80, 0x3F80, 0x3F80, 0x3F80, 0x3F80, 0x3F80, 0x3F80, 0x3F80}; PIN(st.ones);

  STile A, B;
  attn_step<true>(st, A, B, kb + SLOT(0), vb0 + SLOT(0), vb1 + SLOT(0), kb + SLOT(1), wid >= 4);
  BARRIER();
  int s0 = SLOT(1);
  for (int t = 1; t < 15; t += 2) {
    const int s1 = (s0 == 131072) ? 0 : s0 + 32768, s2 = (s1 == 131072) ? 0 : s1 + 32768;
    attn_step<false>(st, B, A, kb + s0, vb0 + s0, vb1 + s0, kb + s1, wid >= 4);
    BARRIER();
    attn_step<false>(st, A, B, kb + s1, vb0 + s1, vb1 + s1, kb + s2, wid >= 4);
    BARRIER();
    s0 = s2;
  }
  attn_step<false>(st, B, A, kb + SLOT(15), vb0 + SLOT(15), vb1 + SLOT(15), kb + SLOT(15), wid >= 4);
  BARRIER();
  {
    lds_cptr vd0 = vb0 + SLOT(16), vd1 = vb1 + SLOT(16); PIN(vd0); PIN(vd1);
#define VRD(m) do { st.vlo[m] = ldstr((((m) & 2) ? vd1 : vd0) + 8192 * ((m) & 1) + 256 * ((m) >> 2)); st.vhi[m] = ldstr((((m) & 2) ? vd1 : vd0) + 8192 * ((m) & 1) + 4096 + 256 * ((m) >> 2)); } while (0)
    const int rbo = RBO();
    WREAD(0); WREAD(1); WREAD(2);
    BARRIER();
#pragma unroll
    for (int m = 0; m < 8; ++m) VRD(m);
    static_for<22>([&](auto I) __attribute__((always_inline)) { constexpr int n = 10 + I.value; EL(B, n) = EX(EL(B, n)); });
    static_for<12>([&](auto I) __attribute__((always_inline)) { constexpr int n = 8 + 2 * I.value; PWV(n)[(n >> 1) & 3] = cvtpk(EL(B, n), EL(B, n + 1)); });
#pragma unroll
    for (int w = 0; w < 2; ++w) {
      st.lacc[0] = MFMA16(st.ones, __builtin_bit_cast(bf16x8, st.pw[0][w]), st.lacc[0]);
      st.lacc[1] = MFMA16(st.ones, __builtin_bit_cast(bf16x8, st.pw[1][w]), st.lacc[1]);
    }
    const float inv0 = 1.0f / st.lacc[0][0], inv1 = 1.0f / st.lacc[1][0];
    const __amdgpu_buffer_rsrc_t srdOs = __builtin_amdgcn_make_buffer_rsrc((void*)(ows + (size_t)(b * NH + h) * 57344), (short)0, 57344, 0x00020000);
    const int qr0 = 32 * wid + l16, vk0 = qr0 / 28, qr1 = qr0 + 16, vk1 = qr1 / 28;
    const unsigned osv0 = vk0 * 7168 + (g4 >> 1) * 896 + ((g4 & 1) * 28 + qr0 - 28 * vk0) * 16, osv1 = vk1 * 7168 + (g4 >> 1) * 896 + ((g4 & 1) * 28 + qr1 - 28 * vk1) * 16;
#define OSTORE(u) do { \
      u32x4 w0_ = {cvtpk(st.o[0][2 * (u)][0] * inv0, st.o[0][2 * (u)][1] * inv0), cvtpk(st.o[0][2 * (u)][2] * inv0, st.o[0][2 * (u)][3] * inv0), \
                   cvtpk(st.o[0][2 * (u) + 1][0] * inv0, st.o[0][2 * (u) + 1][1] * inv0), cvtpk(st.o[0][2 * (u) + 1][2] * inv0, st.o[0][2 * (u) + 1][3] * inv0)}; \
      __builtin_amdgcn_raw_buffer_store_b128(w0_, srdOs, osv0 + (u) * 1792, 0, 16); \
      u32x4 w1_ = {cvtpk(st.o[1][2 * (u)][0] * inv1, st.o[1][2 * (u)][1] * inv1), cvtpk(st.o[1][2 * (u)][2] * inv1, st.o[1][2 * (u)][3] * inv1), \
                   cvtpk(st.o[1][2 * (u) + 1][0] * inv1, st.o[1][2 * (u) + 1][1] * inv1), cvtpk(st.o[1][2 * (u) + 1][2] * inv1, st.o[1][2 * (u) + 1][3] * inv1)}; \
      __builtin_amdgcn_raw_buffer_store_b128(w1_, srdOs, osv1 + (u) * 1792, 0, 16); } while (0)
    SBAR();
#pragma unroll
    for (int u = 0; u < 4; ++u) {
#pragma unroll
      for (int k = 0; k < 4; ++k) {
        const int m = 4 * u + k, db = m >> 1, w = m & 1;
        st.o[0][db] = MFMA16(cat(st.vlo[m], st.vhi[m]), __builtin_bit_cast(bf16x8, st.pw[0][w]), st.o[0][db]);
        st.o[1][db] = MFMA16(cat(st.vlo[m], st.vhi[m]), __builtin_bit_cast(bf16x8, st.pw[1][w]), st.o[1][db]);
        if (u < 2) { VRD(m + 8); }
      }
      SBAR();
      if (u >= 1) { OSTORE(u - 1); }
      SBAR();
    }
    OSTORE(3);
#undef VRD
#undef OSTORE
    WAITV(0);
    if (lane == 0) {
      const unsigned old = __hip_atomic_fetch_add((__attribute__((address_space(3))) unsigned*)(L + PUBW), 1u, __ATOMIC_RELAXED, __HIP_MEMORY_SCOPE_WORKGROUP);
      if (old == 6) __hip_atomic_store(flags + b * NH + h, *(__attribute__((address_space(3))) unsigned long long*)(L + PUBW + 8), __ATOMIC_RELAXED, __HIP_MEMORY_SCOPE_AGENT);
    }
    asm volatile("s_waitcnt lgkmcnt(0)" ::: "memory");
  }
  }

  {
    static_assert(NA == 8, "prefetch sequences above are written for NA = 8");
    const int rbo = RBO(); W3();
    const __amdgpu_buffer_rsrc_t srdO = __builtin_amdgcn_make_buffer_rsrc((void*)(ows + (size_t)ob * (NH * 57344)), (short)0, NH * 57344, 0x00020000);
    const unsigned ov0 = (hi * 28 + (r32 < 28 ? r32 : 27)) * 16;
    const unsigned osb = (w3 >> 2) * 57344 + (w3 & 3) * 1792 + oqt * 2 * 7168;
    u32x4 of[3][4];
    f32x16 acc[2][2];
#pragma unroll
    for (int i = 0; i < 2; ++i) { acc[i][0] = f32x16{}; acc[i][1] = f32x16{}; }
#define OLOAD(i) do { const unsigned so_ = osb + (unsigned)(RR(i) * 2 * 57344), so1_ = so_ + 7168u; \
      BLOAD("sc1", of[(i) % 3][0], ov0, srdO, so_, 0); BLOAD("sc1", of[(i) % 3][1], ov0, srdO, so1_, 0); \
      BLOAD("sc1", of[(i) % 3][2], ov0, srdO, so_, 896); BLOAD("sc1", of[(i) % 3][3], ov0, srdO, so1_, 896); } while (0)
    BARRIER();
    WDMA(4);
    WREAD(3); WDMA(8); WREAD(5); WDMA(10); WREAD(6); WDMA(11); WREAD(7); WDMA(12);
    WAITV(16); WREAD(4); WDMA(9);
    if (w3 == 7) {
      if (blockIdx.x != 0) {
        const unsigned long long* fp = flags + ob * NH + r32;
        for (unsigned spins = 0; spins < (1u << 18); ++spins) {
          const unsigned long long v = __hip_atomic_load(fp, __ATOMIC_RELAXED, __HIP_MEMORY_SCOPE_AGENT);
          if (__all(v == tag)) break;
          __builtin_amdgcn_s_sleep(1);
        }
      } else {
        for (unsigned spins = 0; spins < (1u << 18); ++spins) {
          bool ok = true;
#pragma unroll
          for (int k = 0; k < 4; ++k) ok &= __hip_atomic_load(flags + 64 * k + lane, __ATOMIC_RELAXED, __HIP_MEMORY_SCOPE_AGENT) == tag;
          if (__all(ok)) break;
          __builtin_amdgcn_s_sleep(1);
        }
        if (lane == 0) __hip_atomic_store(epochp, tag, __ATOMIC_RELAXED, __HIP_MEMORY_SCOPE_AGENT);
      }
      KEEP(td);
    }
    BARRIER();
    OLOAD(0); OLOAD(1);
#pragma unroll
    for (int i = 0; i < 16; ++i) {
      const int cnt = (i < 15 ? 4 : 0) + ((i - 1 >= 1 && i - 1 <= 3) ? 4 : 0);
      WAITV4(cnt, of[i % 3][0], of[i % 3][1], of[i % 3][2], of[i % 3][3]);
      if (i >= 1 && i <= 3) { WRWAIT(i - 1 + NA); WDMA(i + 12); }
      if (i + 2 < 16) { OLOAD(i + 2); }
      SBAR();
#pragma unroll
      for (int ks = 0; ks < 2; ++ks)
#pragma unroll
        for (int nb = 0; nb < 2; ++nb)
#pragma unroll
          for (int vb = 0; vb < 2; ++vb)
            acc[nb][vb] = MFMA(wf[i % NA][2 * ks + nb], __builtin_bit_cast(bf16x8, of[i % 3][2 * ks + vb]), acc[nb][vb]);
      SBAR();
      if (i + NA < 16) { WREAD_NW(i + NA); }
      SBAR();
    }
#undef WDMA
#undef WREAD
#undef OLOAD
#undef RBP
#pragma unroll
    for (int vb = 0; vb < 2; ++vb)
#pragma unroll
      for (int nb = 0; nb < 2; ++nb)
#pragma unroll
        for (int rg = 0; rg < 4; ++rg) {
          const int q = 32 * vb + r32, slot = 8 * nb + 2 * rg + hi;
          f32x4 v = {acc[nb][vb][4 * rg], acc[nb][vb][4 * rg + 1], acc[nb][vb][4 * rg + 2], acc[nb][vb][4 * rg + 3]};
          *(__attribute__((address_space(3))) f32x4*)(L + (q >> 4) * 32768 + wid * 4096 + (q & 15) * 256 + ((slot ^ (q & 15)) * 16)) = v;
        }
    asm volatile("s_waitcnt lgkmcnt(0)" ::: "memory");
    BARRIER();
    const int q = tid >> 3, pc = tid & 7;
    f32x4 s0 = {0, 0, 0, 0}, s1 = {0, 0, 0, 0};
#pragma unroll
    for (int w = 0; w < 8; ++w) {
      s0 += *(const __attribute__((address_space(3))) f32x4*)(L + (q >> 4) * 32768 + w * 4096 + (q & 15) * 256 + (((2 * pc) ^ (q & 15)) * 16));
      s1 += *(const __attribute__((address_space(3))) f32x4*)(L + (q >> 4) * 32768 + w * 4096 + (q & 15) * 256 + (((2 * pc + 1) ^ (q & 15)) * 16));
    }
    u32x4 w = {cvtpk(s0[0], s0[1]), cvtpk(s0[2], s0[3]), cvtpk(s1[0], s1[1]), cvtpk(s1[2], s1[3])};
    if ((q & 31) < 28)
      *(u32x4*)(out + (size_t)(ob * SQ + 56 * oqt + 28 * (q >> 5) + (q & 31)) * (DM * 2) + ont * 128 + pc * 16) = w;
  }
}

extern "C" void kernel_launch(void* const* d_in, const int* in_sizes, int n_in,
                              void* d_out, int out_size, void* d_ws, size_t ws_size,
                              hipStream_t stream) {
  (void)in_sizes; (void)n_in; (void)out_size; (void)ws_size;
  (void)hipFuncSetAttribute((const void*)mha_k1, hipFuncAttributeMaxDynamicSharedMemorySize, K1_LDS);
  mha_k1<<<256, 512, K1_LDS, stream>>>((const char*)d_in[0], (const char*)d_in[1], (const char*)d_in[2], (const char*)d_in[3], (const char*)d_in[4], (char*)d_ws, (char*)d_out);
}
```

```cpp
#include <hip/hip_runtime.h>
#include <hip/hip_bf16.h>
#include <stdint.h>
#include <utility>
#include <type_traits>

typedef short    bf16x8 __attribute__((ext_vector_type(8)));
typedef short    s16x4  __attribute__((ext_vector_type(4)));
typedef float    f32x16 __attribute__((ext_vector_type(16)));
typedef float    f32x4  __attribute__((ext_vector_type(4)));
typedef float    f32x2  __attribute__((ext_vector_type(2)));
typedef __bf16   bf16x2 __attribute__((ext_vector_type(2)));
typedef unsigned u32x4  __attribute__((ext_vector_type(4)));
typedef __attribute__((address_space(3))) char* lds_ptr;
typedef __attribute__((address_space(3))) const char* lds_cptr;

#define DI __device__ __forceinline__
#define SBAR() __builtin_amdgcn_sched_barrier(0)
#define PIN(x) asm volatile("" : "+v"(x))
#define MFMA(a, b, c) __builtin_amdgcn_mfma_f32_32x32x16_bf16(a, b, c, 0, 0, 0)
#define BARRIER() asm volatile("s_barrier" ::: "memory")

#define POLT "nt"
#define POLW ""
constexpr int PRIO_TOGGLE = 21;
constexpr int P3NA = 8;
constexpr int NB = 8, SQ = 224, DM = 512, NH = 32, SK = 1024, DH = 128;
constexpr float QSCALE = 0.088388347648318440f * 1.4426950408889634f;
constexpr float THR = 8.0f;

template <int... Is, class F> DI void static_for_(std::integer_sequence<int, Is...>, F&& f) { (f(std::integral_constant<int, Is>{}), ...); }
template <int N, class F> DI void static_for(F&& f) { static_for_(std::make_integer_sequence<int, N>{}, f); }
DI unsigned cvtpk(float lo, float hi) { f32x2 v = {lo, hi}; bf16x2 b = __builtin_convertvector(v, bf16x2); return __builtin_bit_cast(unsigned, b); }
DI bf16x8 ldsrd128(lds_cptr p) { return *(const __attribute__((address_space(3))) bf16x8*)p; }
DI s16x4 ldstr(lds_cptr p) { return __builtin_bit_cast(s16x4, __builtin_amdgcn_ds_read_tr16_b64_v4i16((__attribute__((address_space(3))) s16x4*)p)); }
DI bf16x8 cat(s16x4 a, s16x4 b) { return __builtin_shufflevector(a, b, 0, 1, 2, 3, 4, 5, 6, 7); }
#define MX3(a, b, c) __builtin_fmaxf(__builtin_fmaxf((a), (b)), (c))

#define DMA4(POL, m0v, v0, v1, v2, v3, srd, soff)                                                     \
  asm volatile("s_mov_b32 m0, %0\n\ts_nop 0\n\t"                                                      \
               "buffer_load_dwordx4 %1, %5, %6 offen " POL " lds\n\t"                                 \
               "buffer_load_dwordx4 %2, %5, %6 offen offset:1024 " POL " lds\n\t"                     \
               "buffer_load_dwordx4 %3, %5, %6 offen offset:2048 " POL " lds\n\t"                     \
               "buffer_load_dwordx4 %4, %5, %6 offen offset:3072 " POL " lds"                         \
               :: "s"(m0v), "v"(v0), "v"(v1), "v"(v2), "v"(v3), "s"(srd), "s"(soff) : "memory")
#define DMA2(POL, m0v, v0, v1, srd, soff)                                                             \
  asm volatile("s_mov_b32 m0, %0\n\ts_nop 0\n\t"                                                      \
               "buffer_load_dwordx4 %1, %3, %4 offen " POL " lds\n\t"                                 \
               "buffer_load_dwordx4 %2, %3, %4 offen offset:1024 " POL " lds"                         \
               :: "s"(m0v), "v"(v0), "v"(v1), "s"(srd), "s"(soff) : "memory")
#define BLOAD(POL, dst, voff, srd, soff, imm)                                                         \
  asm volatile("buffer_load_dwordx4 %0, %1, %2, %3 offen offset:%4 " POL                              \
               : "=v"(dst) : "v"(voff), "s"(srd), "s"(soff), "i"(imm) : "memory")
#define WAITV(n) asm volatile("s_waitcnt vmcnt(%0)" :: "i"(n) : "memory")
#define WAITV4(n, a, b, c, d) asm volatile("s_waitcnt vmcnt(%4)" : "+v"(a), "+v"(b), "+v"(c), "+v"(d) : "i"(n) : "memory")

constexpr int K1_LDS = 163840;
constexpr size_t WS_FLAGS = (size_t)32 << 20, WS_EPOCH = WS_FLAGS + 4096;
DI constexpr int SLOT(int t) { return (t % 5) * 32768; }
DI constexpr int RB(int i) { return ((i + 2) % 5) * 32768; }
constexpr int PUBW = 32768;
DI constexpr int WQO(int c) { return (c & 3) == 0 ? 0 : (c & 3) == 1 ? 16384 : (c & 3) == 2 ? 131072 : 147456; }
DI constexpr int XO(int c) { return (c % 3) == 0 ? 65536 : (c % 3) == 1 ? 98304 : 32768; }

#define MFMA16(a, b, c) __builtin_amdgcn_mfma_f32_16x16x32_bf16(a, b, c, 0, 0, 0)
struct AttnState {
  f32x4 o[2][8];
  bf16x8 q[2][4];
  f32x4 negm[2];
  float mhat[2];
  f32x4 lacc[2];
  bf16x8 ones;
  bf16x8 kf[16];
  s16x4 vlo[16], vhi[16];
  u32x4 pw[2][2];
};
struct STile { f32x4 s[2][4]; };
DI float xmax4(float v) {
  auto a = __builtin_amdgcn_permlane16_swap(__float_as_uint(v), __float_as_uint(v), false, false);
  v = __builtin_fmaxf(__uint_as_float(a[0]), __uint_as_float(a[1]));
  auto c = __builtin_amdgcn_permlane32_swap(__float_as_uint(v), __float_as_uint(v), false, false);
  return __builtin_fmaxf(__uint_as_float(c[0]), __uint_as_float(c[1]));
}

constexpr int PD = 5;
#define EL(T, n) (T).s[((n) >> 3) & 1][2 * ((n) >> 4) + (((n) >> 2) & 1)][(n) & 3]
#define ELV(T, n) (T).s[((n) >> 3) & 1][2 * ((n) >> 4) + (((n) >> 2) & 1)]
#define PWV(n) st.pw[((n) >> 3) & 1][(n) >> 4]
#define EX(v) __builtin_amdgcn_exp2f(v)
template <bool FIRST>
DI void attn_step(AttnState& st, STile& C, STile& P, lds_cptr kp, lds_cptr vp0, lds_cptr vp1, lds_cptr kn, const bool young) {
#define KRD(m, base) st.kf[m] = ldsrd128((base) + 4096 * ((m) >> 2) + 256 * ((m) & 3))
#define VRD(m) do { st.vlo[m] = ldstr((((m) & 1) ? vp1 : vp0) + 8192 * ((m) >> 3) + 256 * (((m) & 7) >> 1)); \
                    st.vhi[m] = ldstr((((m) & 1) ? vp1 : vp0) + 8192 * ((m) >> 3) + 4096 + 256 * (((m) & 7) >> 1)); } while (0)
  float ma = -3.0e38f, mb = -3.0e38f, fct0 = 1.f, fct1 = 1.f;
  bool resc = false;
  SBAR();
  static_for<32>([&](auto I) __attribute__((always_inline)) {
    constexpr int g = I.value;
    if constexpr (g == 0) { if (young) __builtin_amdgcn_s_setprio(1); }
    if constexpr (g == PRIO_TOGGLE) { __builtin_amdgcn_s_setprio(0); }
    if constexpr (g < 16) {
      constexpr int kb = g >> 2, u = g & 3;
      if constexpr (u == 0) C.s[0][kb] = MFMA16(st.kf[g], st.q[0][0], st.negm[0]); else C.s[0][kb] = MFMA16(st.kf[g], st.q[0][u], C.s[0][kb]);
      PIN(C.s[0][kb]);
    } else if constexpr (!FIRST) {
      constexpr int m = g - 16;
      st.o[0][m & 7] = MFMA16(cat(st.vlo[m], st.vhi[m]), __builtin_bit_cast(bf16x8, st.pw[0][m >> 3]), st.o[0][m & 7]);
      PIN(st.o[0][m & 7]);
    }
    SBAR();
    {
      constexpr int f = g + PD;
      if constexpr (f < 16) { KRD(f, kp); }
      else if constexpr (f < 32) { if constexpr (!FIRST) { VRD(f - 16); } }
      else { KRD(f - 32, kn); }
    }
    if constexpr ((g & 1) && g >= 5 && g <= 19) {
      constexpr int kb = (g - 5) >> 2, hf = ((g - 5) >> 1) & 1;
      ma = MX3(ma, C.s[0][kb][2 * hf], C.s[0][kb][2 * hf + 1]); PIN(ma);
    }
    SBAR();
    if constexpr (g < 16) {
      constexpr int kb = g >> 2, u = g & 3;
      if constexpr (u == 0) C.s[1][kb] = MFMA16(st.kf[g], st.q[1][0], st.negm[1]); else C.s[1][kb] = MFMA16(st.kf[g], st.q[1][u], C.s[1][kb]);
      PIN(C.s[1][kb]);
    } else if constexpr (!FIRST) {
      constexpr int m = g - 16;
      st.o[1][m & 7] = MFMA16(cat(st.vlo[m], st.vhi[m]), __builtin_bit_cast(bf16x8, st.pw[1][m >> 3]), st.o[1][m & 7]);
      PIN(st.o[1][m & 7]);
    }
    SBAR();
    if constexpr ((g & 1) == 0) {
      if constexpr (g <= 22) {
        if constexpr (!FIRST) { constexpr int n = 8 + g; PWV(n)[(n >> 1) & 3] = cvtpk(EL(P, n), EL(P, n + 1)); PIN(PWV(n)); }
      } else { constexpr int n = g - 24; PWV(n)[(n >> 1) & 3] = cvtpk(EL(C, n), EL(C, n + 1)); PIN(PWV(n)); }
    }
    if constexpr ((g & 1) && g >= 5 && g <= 19) {
      constexpr int kb = (g - 5) >> 2, hf = ((g - 5) >> 1) & 1;
      mb = MX3(mb, C.s[1][kb][2 * hf], C.s[1][kb][2 * hf + 1]); PIN(mb);
    }
    if constexpr (g == 22) {
      const bool ok = (ma <= THR) & (mb <= THR);
      if (FIRST || __builtin_expect(!__all(ok), 0)) {
#pragma unroll
        for (int qb = 0; qb < 2; ++qb) {
          float rm = qb ? mb : ma;
          rm = xmax4(rm);
          const float dl = FIRST ? rm : __builtin_fmaxf(rm, 0.f);
          st.mhat[qb] += dl;
#pragma unroll
          for (int kb = 0; kb < 4; ++kb)
#pragma unroll
            for (int r = 0; r < 4; ++r) C.s[qb][kb][r] -= dl;
#pragma unroll
          for (int r = 0; r < 4; ++r) st.negm[qb][r] = -st.mhat[qb];
          PIN(st.negm[qb]);
          if constexpr (!FIRST) { const float f_ = EX(-dl); if (qb == 0) fct0 = f_; else fct1 = f_; }
        }
        resc = true;
      }
    }
    if constexpr (g <= 21) {
      if constexpr (!FIRST) { constexpr int n = 10 + g; EL(P, n) = EX(EL(P, n)); PIN(ELV(P, n)); }
    } else { constexpr int n = g - 22; EL(C, n) = EX(EL(C, n)); PIN(ELV(C, n)); }
    SBAR();
    if constexpr (!FIRST && (g == 21 || g == 23 || g == 25 || g == 27)) {
      constexpr int qb = (g >> 1) & 1, w = g >= 24;
      st.lacc[qb] = MFMA16(st.ones, __builtin_bit_cast(bf16x8, st.pw[qb][w]), st.lacc[qb]); PIN(st.lacc[qb]);
      SBAR();
    }
  });
  if constexpr (!FIRST) {
    if (__builtin_expect(resc, 0)) {
#pragma unroll
      for (int d = 0; d < 8; ++d)
#pragma unroll
        for (int r = 0; r < 4; ++r) { st.o[0][d][r] *= fct0; st.o[1][d][r] *= fct1; }
#pragma unroll
      for (int r = 0; r < 4; ++r) { st.lacc[0][r] *= fct0; st.lacc[1][r] *= fct1; }
    }
  }
#undef KRD
#undef VRD
}

__global__ __launch_bounds__(512, 2)
void mha_k1(const char* __restrict__ x, const char* __restrict__ kc, const char* __restrict__ vc,
            const char* __restrict__ wq, const char* __restrict__ wo, char* ows, char* __restrict__ out) {
  extern __shared__ __attribute__((aligned(16))) char lds[];
  const int tid = threadIdx.x, lane = tid & 63;
  const int wid = __builtin_amdgcn_readfirstlane(tid >> 6);
  const int b = 4 * (blockIdx.x & 1) + ((blockIdx.x >> 3) & 3), h = 8 * ((blockIdx.x >> 1) & 3) + (blockIdx.x >> 5);
  const unsigned lds0 = (unsigned)(uintptr_t)lds;
  const lds_ptr L = (lds_ptr)lds;

  const char *pfx, *pfw;
  {
    const int j = blockIdx.x >> 3, half = lane >> 5, l5 = lane & 31;
    const int xrow = (b & 4) * SQ + 28 * j + (l5 < 28 ? l5 : 27);
    const int idx = 32 * j + l5;
    pfw = wq + (size_t)(64 * half + (idx >> 4)) * 8192 + (h & 24) * 256 + (idx & 15) * 128;
    pfx = x + (size_t)xrow * (DM * 2) + half * 128;
  }
  unsigned pfd0;
  const char* pfe = wid >= 4 ? x : (wid & 1) ? pfw + (size_t)(wid >> 1) * (128 * 8192) : pfx + (wid >> 1) * 256;
#define TOUCH(d, p) asm volatile("global_load_dword %0, %1, off " POLT : "=v"(d) : "v"(p) : "memory")
#define KEEP(d) asm volatile("" :: "v"(d) : "memory")
  const __amdgpu_buffer_rsrc_t srdW = __builtin_amdgcn_make_buffer_rsrc((void*)(wq + h * 256), (short)0, DM * 8192 - h * 256, 0x00020000);
  const unsigned pl = ((lane >> 2) & 3), dbl = lane >> 4, c4 = lane & 3;
  const unsigned wv0 = pl * 8192 + dbl * 64 + c4 * 16, wv1 = wv0 + 31744;
#define DMAW2(c) DMA2(POLW, lds0 + WQO(c) + 2048 * wid, wv0, wv1, srdW, (unsigned)(((c) * 64 + 8 * wid) * 8192))
  TOUCH(pfd0, pfe);
  const int ob = blockIdx.x & 7, oqt = blockIdx.x >> 6, ont = (blockIdx.x >> 3) & 7;
  const __amdgpu_buffer_rsrc_t srdWo = __builtin_amdgcn_make_buffer_rsrc((void*)(wo + ont * 128), (short)0, NH * DH * DM * 2 - ont * 128, 0x00020000);
  const unsigned ow0 = (4 * (lane >> 5) + ((lane >> 2) & 3)) * 1024 + ((lane >> 4) & 1) * 64 + (lane & 3) * 16;
  const unsigned ow1 = ow0 + 7168, ow2 = ow0 + 2 * 7168, ow3 = ow0 + 3 * 7168;
  unsigned long long* const epochp = (unsigned long long*)(ows + WS_EPOCH);
  unsigned long long* const flags = (unsigned long long*)(ows + WS_FLAGS);
  unsigned long long tag = 0;
  unsigned td = 0;
#define RR(i) (i)
  constexpr int NA = P3NA;
  const int r32 = lane & 31, hi = lane >> 5, tq = (lane >> 2) & 3, tp = lane & 3, tblk = (lane >> 4) & 1;
  bf16x8 wf[NA][4];
#define RBP(i) ((lds_cptr)L + RB(i) + rbo)
#define WDMA(i) DMA4("", lds0 + (unsigned)(RB(i)) + 4096u * w3, ow0, ow1, ow2, ow3, srdWo, 32768u * w3 + (unsigned)(RR(i) * 256 * 1024))
#define W3() unsigned w3 = (unsigned)__builtin_amdgcn_readfirstlane(tid >> 6); asm volatile("s_nop 4" : "+s"(w3))
#define WREAD_NW(i) do { lds_cptr p_ = RBP(i); PIN(p_); _Pragma("unroll") for (int f_ = 0; f_ < 4; ++f_) \
      wf[(i) % NA][f_] = cat(ldstr(p_ + 1024 * (f_ >> 1) + 256 * (f_ & 1)), ldstr(p_ + 1024 * (f_ >> 1) + 2048 + 256 * (f_ & 1))); } while (0)
#define WRWAIT(i) asm volatile("s_waitcnt lgkmcnt(0)" : "+v"(wf[(i) % NA][0]), "+v"(wf[(i) % NA][1]), "+v"(wf[(i) % NA][2]), "+v"(wf[(i) % NA][3]) :: "memory")
#define WREAD(i) do { WREAD_NW(i); WRWAIT(i); } while (0)
#define RBO() (wid * 4096 + 512 * hi + 64 * tq + 32 * tblk + 8 * tp)
  if (wid == 7) {
    const __amdgpu_buffer_rsrc_t srdK = __builtin_amdgcn_make_buffer_rsrc((void*)(kc + (size_t)(b * NH + h) * (SK * DH * 2)), (short)0, SK * DH * 2, 0x00020000);
    const __amdgpu_buffer_rsrc_t srdV = __builtin_amdgcn_make_buffer_rsrc((void*)(vc + (size_t)(b * NH + h) * (SK * DH * 2)), (short)0, SK * DH * 2, 0x00020000);
    const unsigned wv2 = wv0 + 2 * 31744, wv3 = wv0 + 3 * 31744;
    const unsigned kvA = pl * 256 + dbl * 64 + c4 * 16, kvB = pl * 256 + dbl * 64 + ((c4 ^ 2) * 16);
#define DMAW(c) do { _Pragma("unroll") for (int i_ = 0; i_ < 4; ++i_) DMA4(POLW, lds0 + (unsigned)(WQO(c) + 4096 * i_), wv0, wv1, wv2, wv3, srdW, (unsigned)(((c) * 64 + 16 * i_) * 8192)); } while (0)
#define DMAK_(t, so) do { _Pragma("unroll") for (int i_ = 0; i_ < 4; ++i_) { WAITV(59); DMA4("nt", lds0 + (unsigned)((so) + 4096 * i_), kvA, kvA, kvB, kvB, srdK, (unsigned)((t) * 16384 + 4096 * i_)); } } while (0)
#define DMAV_(t, so) do { _Pragma("unroll") for (int i_ = 0; i_ < 4; ++i_) { WAITV(59); DMA4("nt", lds0 + (unsigned)((so) + 16384 + 4096 * i_), kvA, kvB, kvA, kvB, srdV, (unsigned)((t) * 16384 + 4096 * i_)); } } while (0)
#define DMAK(t) DMAK_(t, SLOT(t))
#define DMAV(t) DMAV_(t, SLOT((t) + 1))
    const char* tw = wo + (size_t)(blockIdx.x >> 3) * 8192 + lane * 128;
#define TW(r) do { const char* p_ = tw + (size_t)(r) * 262144; asm volatile("s_waitcnt vmcnt(62)\n\tglobal_load_dword %0, %1, off" : "+v"(td) : "v"(p_) : "memory"); } while (0)
    unsigned t4, t5, t6, t7;
    asm volatile("global_load_dwordx2 %0, %1, off sc1" : "=v"(tag) : "v"(epochp) : "memory");
#define RW(i) do { _Pragma("unroll") for (int w_ = 0; w_ < 8; ++w_) { WAITV(59); DMA4("", lds0 + (unsigned)(RB(i) + 4096 * w_), ow0, ow1, ow2, ow3, srdWo, (unsigned)((RR(i) * 256 + 32 * w_) * 1024)); } } while (0)
    DMAW2(0);
    BARRIER();
    DMAW(1);
    WAITV(16); BARRIER();
    DMAW(2);
    WAITV(16); BARRIER();
    DMAW(3);
    { const char* p4 = pfx + 512; const char* p5 = pfw + (size_t)2 * 128 * 8192; const char* p6 = pfx + 768; const char* p7 = pfw + (size_t)3 * 128 * 8192;
      TOUCH(t4, p4); TOUCH(t5, p5); TOUCH(t6, p6); TOUCH(t7, p7); }
    WAITV(20); BARRIER();
    WAITV(4); BARRIER();
    DMAW(4); DMAW(5);
    WAITV(16); BARRIER();
    KEEP(pfd0); KEEP(t4); KEEP(t5); KEEP(t6); KEEP(t7);
    DMAW(6);
    WAITV(16); BARRIER();
    DMAW(7);
    WAITV(16); BARRIER();
    DMAK(0); DMAK(1); DMAV(0);
    WAITV(48); BARRIER();
    BARRIER();
    DMAK(2);
    WAITV(32); BARRIER();
    DMAV(1); DMAK(3); DMAV(2);
    WAITV(48); BARRIER();
    DMAK(4); DMAV(3); DMAK(5); DMAV(4);
    WAITV(63); BARRIER();
    {
      int so = SLOT(1);
      for (int t = 1; t < 11; ++t) {
        DMAK_(t + 5, so); DMAV_(t + 4, so);
        so = (so == 131072) ? 0 : so + 32768;
        WAITV(63);
        BARRIER();
      }
    }
    DMAV(15); TW(0); TW(1); TW(2); TW(3);
    asm volatile("" : "+v"(tag));
    tag = (((unsigned long long)(unsigned)__builtin_amdgcn_readfirstlane((unsigned)(tag >> 32)) << 32) | (unsigned)__builtin_amdgcn_readfirstlane((unsigned)tag)) + 1;
    if (lane == 0) { *(__attribute__((address_space(3))) unsigned*)(L + PUBW) = 0u; *(__attribute__((address_space(3))) unsigned long long*)(L + PUBW + 8) = tag; }
    asm volatile("s_waitcnt lgkmcnt(0)" ::: "memory");
    WAITV(63);
    BARRIER();
    TW(5); TW(6); TW(7); TW(4);
    WAITV(40);
    RW(0);
    BARRIER();
    RW(1); TW(8); TW(10); TW(11); TW(12);
    WAITV(63);
    BARRIER();
    RW(2); TW(9); TW(13); TW(14); TW(15);
    WAITV(4);
    BARRIER();
    {
      const int rbo = RBO();
      WREAD(0); WREAD(1); WREAD(2);
    }
    BARRIER();
    RW(3); RW(5); RW(6); RW(7);
    WAITV(0);
#undef TW
#undef RW
#undef DMAW
#undef DMAK
#undef DMAV
  } else {
  const __amdgpu_buffer_rsrc_t srdX = __builtin_amdgcn_make_buffer_rsrc((void*)(x + (size_t)(b * SQ + wid * 32) * (DM * 2)), (short)0, 32 * DM * 2, 0x00020000);
  const unsigned xs = (lane >> 3) * 1024 + (((lane & 7) ^ (lane >> 4)) * 16);
  const unsigned xv0 = xs, xv1 = (xs ^ 64) + 7168, xv2 = xs + 2 * 7168, xv3 = (xs ^ 64) + 3 * 7168;
#define XDMA(c) DMA4(POLW, lds0 + (unsigned)XO(c) + 4096u * wid, xv0, xv1, xv2, xv3, srdX, (unsigned)(128 * (c)))
  const unsigned xrb = (unsigned)(wid * 4096 + r32 * 128 + ((hi ^ ((r32 >> 1) & 7)) * 16));
  bf16x8 xf[2][4];
#define XRD(c, k_) xf[(c) & 1][k_] = ldsrd128((lds_cptr)L + ((xrb ^ (unsigned)((k_) << 5)) + XO(c)))
  DMAW2(0); XDMA(0);
  BARRIER();
  XDMA(1);
  const lds_cptr wrb = (lds_cptr)L + 2048 * hi + 64 * tq + 32 * (tp & 1) + 16 * tblk + 8 * (tp >> 1);
  lds_cptr wrb2 = wrb + 131072; PIN(wrb2);
#define WRB(c) ((WQO(c) < 65536 ? wrb : wrb2) + (WQO(c) & 65535))
#define WRD(c, m) do { wlo[m] = ldstr(WRB(c) + 4096 * ((m) >> 2) + 256 * ((m) & 3)); whi[m] = ldstr(WRB(c) + 4096 * ((m) >> 2) + 1024 + 256 * ((m) & 3)); } while (0)
  AttnState st;
  f32x16 acc[4];
#pragma unroll
  for (int i = 0; i < 4; ++i) acc[i] = f32x16{};
  s16x4 wlo[16], whi[16];
  WAITV(4);
  XRD(0, 0); XRD(0, 1); XRD(0, 2); XRD(0, 3);
  BARRIER();
#pragma unroll
  for (int m = 0; m < PD; ++m) WRD(0, m);
  u32x4 qx[4], qy[4];
#define QCV1(nb, e_) do { \
      const unsigned lo_ = cvtpk(acc[nb][2 * (e_)] * QSCALE, acc[nb][2 * (e_) + 1] * QSCALE), hi_ = cvtpk(acc[nb][8 + 2 * (e_)] * QSCALE, acc[nb][9 + 2 * (e_)] * QSCALE); \
      auto r_ = __builtin_amdgcn_permlane16_swap(lo_, hi_, false, false); qx[nb][e_] = r_[0]; qy[nb][e_] = r_[1]; PIN(qx[nb]); PIN(qy[nb]); } while (0)
  static_for<128>([&](auto I) __attribute__((always_inline)) {
    constexpr int c = I.value >> 4, mm = I.value & 15;
    constexpr int m = (c == 7) ? (4 * (mm & 3) + (mm >> 2)) : mm;
    acc[m & 3] = MFMA(cat(wlo[m], whi[m]), xf[c & 1][m >> 2], acc[m & 3]); PIN(acc[m & 3]);
    if constexpr (c == 0 && mm + PD == 16) { BARRIER(); }
    if constexpr (mm + PD < 16) { constexpr int f = mm + PD, ff = (c == 7) ? (4 * (f & 3) + (f >> 2)) : f; WRD(c, ff); }
    else if constexpr (c < 7) { constexpr int f = mm + PD - 16, ff = (c + 1 == 7) ? (4 * (f & 3) + (f >> 2)) : f; WRD(c + 1, ff); }
    if constexpr (c == 0 && mm == 0) { XDMA(2); }
    if constexpr (c == 0 && mm == 2) { XDMA(3); }
    if constexpr (c == 2 && mm == 8) { XDMA(4); }
    if constexpr (c == 3 && mm == 0) { XDMA(5); }
    if constexpr (c == 3 && mm == 8) { XDMA(6); }
    if constexpr (c == 4 && mm == 8) { XDMA(7); }
    if constexpr (c < 7) {
      if constexpr (mm == 4) { WAITV(c == 0 ? 8 : (c == 2 || c == 6) ? 0 : 4); }
      if constexpr (mm == 5 && c == 1) { KEEP(pfd0); }
      if constexpr (mm >= 4 && mm < 8) { XRD(c + 1, mm - 4); }
    }
    if constexpr (c == 5) { st.o[mm >> 3][mm & 7] = f32x4{}; PIN(st.o[mm >> 3][mm & 7]); }
    if constexpr (c == 7 && mm >= 5) { QCV1((mm - 5) >> 2, (mm - 5) & 3); }
    SBAR();
    if constexpr (mm == 15) { BARRIER(); SBAR(); }
  });
#undef XDMA
#undef XRD
#undef WRD
  const int l16 = lane & 15, g4 = lane >> 4;
  const lds_cptr kb = (lds_cptr)L + 1024 * (l16 >> 2) + 64 * (l16 & 3) + 16 * (g4 ^ ((l16 >> 2) & 2));
  const lds_cptr vb0 = (lds_cptr)L + 16384 + 1024 * g4 + 64 * tq + 32 * (g4 & 1) + 8 * tp;
  const lds_cptr vb1 = (lds_cptr)L + 16384 + 1024 * g4 + 64 * tq + 32 * (1 - (g4 & 1)) + 8 * tp;
#pragma unroll
  for (int m = 0; m < PD; ++m) st.kf[m] = ldsrd128(kb + SLOT(0) + 4096 * (m >> 2) + 256 * (m & 3));
  QCV1(2, 3); QCV1(3, 0); QCV1(3, 1); QCV1(3, 2); QCV1(3, 3);
#undef QCV1
#pragma unroll
  for (int nb = 0; nb < 4; ++nb) { st.q[0][nb] = __builtin_bit_cast(bf16x8, qx[nb]); st.q[1][nb] = __builtin_bit_cast(bf16x8, qy[nb]); }
  st.negm[0] = f32x4{}; st.negm[1] = f32x4{}; PIN(st.negm[0]); PIN(st.negm[1]);
  st.mhat[0] = st.mhat[1] = 0.f; st.lacc[0] = f32x4{}; st.lacc[1] = f32x4{};
  st.ones = bf16x8{0x3F80, 0x3F80, 0x3F80, 0x3F80, 0x3F80, 0x3F80, 0x3F80, 0x3F80}; PIN(st.ones);

  STile A, B;
  attn_step<true>(st, A, B, kb + SLOT(0), vb0 + SLOT(0), vb1 + SLOT(0), kb + SLOT(1), wid >= 4);
  BARRIER();
  int s0 = SLOT(1);
  for (int t = 1; t < 15; t += 2) {
    const int s1 = (s0 == 131072) ? 0 : s0 + 32768, s2 = (s1 == 131072) ? 0 : s1 + 32768;
    attn_step<false>(st, B, A, kb + s0, vb0 + s0, vb1 + s0, kb + s1, wid >= 4);
    BARRIER();
    attn_step<false>(st, A, B, kb + s1, vb0 + s1, vb1 + s1, kb + s2, wid >= 4);
    BARRIER();
    s0 = s2;
  }
  attn_step<false>(st, B, A, kb + SLOT(15), vb0 + SLOT(15), vb1 + SLOT(15), kb + SLOT(15), wid >= 4);
  BARRIER();
  {
    lds_cptr vd0 = vb0 + SLOT(16), vd1 = vb1 + SLOT(16); PIN(vd0); PIN(vd1);
#define VRD(m) do { st.vlo[m] = ldstr((((m) & 2) ? vd1 : vd0) + 8192 * ((m) & 1) + 256 * ((m) >> 2)); st.vhi[m] = ldstr((((m) & 2) ? vd1 : vd0) + 8192 * ((m) & 1) + 4096 + 256 * ((m) >> 2)); } while (0)
    const int rbo = RBO();
    WREAD(0); WREAD(1); WREAD(2);
    BARRIER();
#pragma unroll
    for (int m = 0; m < 8; ++m) VRD(m);
    static_for<22>([&](auto I) __attribute__((always_inline)) { constexpr int n = 10 + I.value; EL(B, n) = EX(EL(B, n)); });
    static_for<12>([&](auto I) __attribute__((always_inline)) { constexpr int n = 8 + 2 * I.value; PWV(n)[(n >> 1) & 3] = cvtpk(EL(B, n), EL(B, n + 1)); });
#pragma unroll
    for (int w = 0; w < 2; ++w) {
      st.lacc[0] = MFMA16(st.ones, __builtin_bit_cast(bf16x8, st.pw[0][w]), st.lacc[0]);
      st.lacc[1] = MFMA16(st.ones, __builtin_bit_cast(bf16x8, st.pw[1][w]), st.lacc[1]);
    }
    const float inv0 = 1.0f / st.lacc[0][0], inv1 = 1.0f / st.lacc[1][0];
    const __amdgpu_buffer_rsrc_t srdOs = __builtin_amdgcn_make_buffer_rsrc((void*)(ows + (size_t)(b * NH + h) * 57344), (short)0, 57344, 0x00020000);
    const int qr0 = 32 * wid + l16, vk0 = qr0 / 28, qr1 = qr0 + 16, vk1 = qr1 / 28;
    const unsigned osv0 = vk0 * 7168 + (g4 >> 1) * 896 + ((g4 & 1) * 28 + qr0 - 28 * vk0) * 16, osv1 = vk1 * 7168 + (g4 >> 1) * 896 + ((g4 & 1) * 28 + qr1 - 28 * vk1) * 16;
#define OSTORE(u) do { \
      u32x4 w0_ = {cvtpk(st.o[0][2 * (u)][0] * inv0, st.o[0][2 * (u)][1] * inv0), cvtpk(st.o[0][2 * (u)][2] * inv0, st.o[0][2 * (u)][3] * inv0), \
                   cvtpk(st.o[0][2 * (u) + 1][0] * inv0, st.o[0][2 * (u) + 1][1] * inv0), cvtpk(st.o[0][2 * (u) + 1][2] * inv0, st.o[0][2 * (u) + 1][3] * inv0)}; \
      __builtin_amdgcn_raw_buffer_store_b128(w0_, srdOs, osv0 + (u) * 1792, 0, 16); \
      u32x4 w1_ = {cvtpk(st.o[1][2 * (u)][0] * inv1, st.o[1][2 * (u)][1] * inv1), cvtpk(st.o[1][2 * (u)][2] * inv1, st.o[1][2 * (u)][3] * inv1), \
                   cvtpk(st.o[1][2 * (u) + 1][0] * inv1, st.o[1][2 * (u) + 1][1] * inv1), cvtpk(st.o[1][2 * (u) + 1][2] * inv1, st.o[1][2 * (u) + 1][3] * inv1)}; \
      __builtin_amdgcn_raw_buffer_store_b128(w1_, srdOs, osv1 + (u) * 1792, 0, 16); } while (0)
    SBAR();
#pragma unroll
    for (int u = 0; u < 4; ++u) {
#pragma unroll
      for (int k = 0; k < 4; ++k) {
        const int m = 4 * u + k, db = m >> 1, w = m & 1;
        st.o[0][db] = MFMA16(cat(st.vlo[m], st.vhi[m]), __builtin_bit_cast(bf16x8, st.pw[0][w]), st.o[0][db]);
        st.o[1][db] = MFMA16(cat(st.vlo[m], st.vhi[m]), __builtin_bit_cast(bf16x8, st.pw[1][w]), st.o[1][db]);
        if (u < 2) { VRD(m + 8); }
      }
      SBAR();
      if (u >= 1) { OSTORE(u - 1); }
      SBAR();
    }
    OSTORE(3);
#undef VRD
#undef OSTORE
    WAITV(0);
    if (lane == 0) {
      const unsigned old = __hip_atomic_fetch_add((__attribute__((address_space(3))) unsigned*)(L + PUBW), 1u, __ATOMIC_RELAXED, __HIP_MEMORY_SCOPE_WORKGROUP);
      if (old == 6) __hip_atomic_store(flags + b * NH + h, *(__attribute__((address_space(3))) unsigned long long*)(L + PUBW + 8), __ATOMIC_RELAXED, __HIP_MEMORY_SCOPE_AGENT);
    }
    asm volatile("s_waitcnt lgkmcnt(0)" ::: "memory");
  }
  }

  {
    static_assert(NA == 8, "prefetch sequences above are written for NA = 8");
    const int rbo = RBO(); W3();
    const __amdgpu_buffer_rsrc_t srdO = __builtin_amdgcn_make_buffer_rsrc((void*)(ows + (size_t)ob * (NH * 57344)), (short)0, NH * 57344, 0x00020000);
    const unsigned ov0 = (hi * 28 + (r32 < 28 ? r32 : 27)) * 16;
    const unsigned osb = (w3 >> 2) * 57344 + (w3 & 3) * 1792 + oqt * 2 * 7168;
    u32x4 of[3][4];
    f32x16 acc[2][2];
#pragma unroll
    for (int i = 0; i < 2; ++i) { acc[i][0] = f32x16{}; acc[i][1] = f32x16{}; }
#define OLOAD(i) do { const unsigned so_ = osb + (unsigned)(RR(i) * 2 * 57344), so1_ = so_ + 7168u; \
      BLOAD("sc1", of[(i) % 3][0], ov0, srdO, so_, 0); BLOAD("sc1", of[(i) % 3][1], ov0, srdO, so1_, 0); \
      BLOAD("sc1", of[(i) % 3][2], ov0, srdO, so_, 896); BLOAD("sc1", of[(i) % 3][3], ov0, srdO, so1_, 896); } while (0)
    BARRIER();
    WDMA(4);
    WREAD(3); WDMA(8); WREAD(5); WDMA(10); WREAD(6); WDMA(11); WREAD(7); WDMA(12);
    WAITV(16); WREAD(4); WDMA(9);
    if (w3 == 7) {
      if (blockIdx.x != 0) {
        const unsigned long long* fp = flags + ob * NH + r32;
        for (unsigned spins = 0; spins < (1u << 18); ++spins) {
          const unsigned long long v = __hip_atomic_load(fp, __ATOMIC_RELAXED, __HIP_MEMORY_SCOPE_AGENT);
          if (__all(v == tag)) break;
          __builtin_amdgcn_s_sleep(1);
        }
      } else {
        for (unsigned spins = 0; spins < (1u << 18); ++spins) {
          bool ok = true;
#pragma unroll
          for (int k = 0; k < 4; ++k) ok &= __hip_atomic_load(flags + 64 * k + lane, __ATOMIC_RELAXED, __HIP_MEMORY_SCOPE_AGENT) == tag;
          if (__all(ok)) break;
          __builtin_amdgcn_s_sleep(1);
        }
        if (lane == 0) __hip_atomic_store(epochp, tag, __ATOMIC_RELAXED, __HIP_MEMORY_SCOPE_AGENT);
      }
      KEEP(td);
    }
    BARRIER();
    OLOAD(0); OLOAD(1);
#pragma unroll
    for (int i = 0; i < 16; ++i) {
      const int cnt = (i < 15 ? 4 : 0) + ((i - 1 >= 1 && i - 1 <= 3) ? 4 : 0);
      WAITV4(cnt, of[i % 3][0], of[i % 3][1], of[i % 3][2], of[i % 3][3]);
      if (i >= 1 && i <= 3) { WRWAIT(i - 1 + NA); WDMA(i + 12); }
      if (i + 2 < 16) { OLOAD(i + 2); }
      SBAR();
#pragma unroll
      for (int ks = 0; ks < 2; ++ks)
#pragma unroll
        for (int nb = 0; nb < 2; ++nb)
#pragma unroll
          for (int vb = 0; vb < 2; ++vb)
            acc[nb][vb] = MFMA(wf[i % NA][2 * ks + nb], __builtin_bit_cast(bf16x8, of[i % 3][2 * ks + vb]), acc[nb][vb]);
      SBAR();
      if (i + NA < 16) { WREAD_NW(i + NA); }
      SBAR();
    }
#undef WDMA
#undef WREAD
#undef OLOAD
#undef RBP
#pragma unroll
    for (int vb = 0; vb < 2; ++vb)
#pragma unroll
      for (int nb = 0; nb < 2; ++nb)
#pragma unroll
        for (int rg = 0; rg < 4; ++rg) {
          const int q = 32 * vb + r32, slot = 8 * nb + 2 * rg + hi;
          f32x4 v = {acc[nb][vb][4 * rg], acc[nb][vb][4 * rg + 1], acc[nb][vb][4 * rg + 2], acc[nb][vb][4 * rg + 3]};
          *(__attribute__((address_space(3))) f32x4*)(L + (q >> 4) * 32768 + wid * 4096 + (q & 15) * 256 + ((slot ^ (q & 15)) * 16)) = v;
        }
    asm volatile("s_waitcnt lgkmcnt(0)" ::: "memory");
    BARRIER();
    const int q = tid >> 3, pc = tid & 7;
    f32x4 s0 = {0, 0, 0, 0}, s1 = {0, 0, 0, 0};
#pragma unroll
    for (int w = 0; w < 8; ++w) {
      s0 += *(const __attribute__((address_space(3))) f32x4*)(L + (q >> 4) * 32768 + w * 4096 + (q & 15) * 256 + (((2 * pc) ^ (q & 15)) * 16));
      s1 += *(const __attribute__((address_space(3))) f32x4*)(L + (q >> 4) * 32768 + w * 4096 + (q & 15) * 256 + (((2 * pc + 1) ^ (q & 15)) * 16));
    }
    u32x4 w = {cvtpk(s0[0], s0[1]), cvtpk(s0[2], s0[3]), cvtpk(s1[0], s1[1]), cvtpk(s1[2], s1[3])};
    if ((q & 31) < 28)
      *(u32x4*)(out + (size_t)(ob * SQ + 56 * oqt + 28 * (q >> 5) + (q & 31)) * (DM * 2) + ont * 128 + pc * 16) = w;
  }
}

extern "C" void kernel_launch(void* const* d_in, const int* in_sizes, int n_in,
                              void* d_out, int out_size, void* d_ws, size_t ws_size,
                              hipStream_t stream) {
  (void)in_sizes; (void)n_in; (void)out_size; (void)ws_size;
  (void)hipFuncSetAttribute((const void*)mha_k1, hipFuncAttributeMaxDynamicSharedMemorySize, K1_LDS);
  mha_k1<<<256, 512, K1_LDS, stream>>>((const char*)d_in[0], (const char*)d_in[1], (const char*)d_in[2], (const char*)d_in[3], (const char*)d_in[4], (char*)d_ws, (char*)d_out);
}
```

```cpp
#include <hip/hip_runtime.h>
#include <hip/hip_bf16.h>
#include <stdint.h>
#include <utility>
#include <type_traits>

typedef short    bf16x8 __attribute__((ext_vector_type(8)));
typedef short    s16x4  __attribute__((ext_vector_type(4)));
typedef float    f32x16 __attribute__((ext_vector_type(16)));
typedef float    f32x4  __attribute__((ext_vector_type(4)));
typedef float    f32x2  __attribute__((ext_vector_type(2)));
typedef __bf16   bf16x2 __attribute__((ext_vector_type(2)));
typedef unsigned u32x4  __attribute__((ext_vector_type(4)));
typedef __attribute__((address_space(3))) char* lds_ptr;
typedef __attribute__((address_space(3))) const char* lds_cptr;

#define DI __device__ __forceinline__
#define SBAR() __builtin_amdgcn_sched_barrier(0)
#define PIN(x) asm volatile("" : "+v"(x))
#define MFMA(a, b, c) __builtin_amdgcn_mfma_f32_32x32x16_bf16(a, b, c, 0, 0, 0)
#define BARRIER() asm volatile("s_barrier" ::: "memory")

#define STAMP(i)
#define RSTAMP(i)
#define LSTAMP(i)
#define STFLUSH(dbg)
#define POLT "nt"
#define POLW ""
#define POLWO ""
#define POLT2 ""
#define POLO "sc1"
#define P3NA 8
#define TSCHED 3
#define PRIO_TOGGLE 21
#define EARLYKV 0
#define KVTN 0
#define MAPK1 1
constexpr int NB = 8, SQ = 224, DM = 512, NH = 32, SK = 1024, DH = 128;
constexpr float QSCALE = 0.088388347648318440f * 1.4426950408889634f;
constexpr float THR = 8.0f;

template <int... Is, class F> DI void static_for_(std::integer_sequence<int, Is...>, F&& f) { (f(std::integral_constant<int, Is>{}), ...); }
template <int N, class F> DI void static_for(F&& f) { static_for_(std::make_integer_sequence<int, N>{}, f); }
DI unsigned cvtpk(float lo, float hi) { f32x2 v = {lo, hi}; bf16x2 b = __builtin_convertvector(v, bf16x2); return __builtin_bit_cast(unsigned, b); }
DI bf16x8 ldsrd128(lds_cptr p) { return *(const __attribute__((address_space(3))) bf16x8*)p; }
DI s16x4 ldstr(lds_cptr p) { return __builtin_bit_cast(s16x4, __builtin_amdgcn_ds_read_tr16_b64_v4i16((__attribute__((address_space(3))) s16x4*)p)); }
DI bf16x8 cat(s16x4 a, s16x4 b) { return __builtin_shufflevector(a, b, 0, 1, 2, 3, 4, 5, 6, 7); }
DI void ldswr128(lds_ptr p, u32x4 v) { *(__attribute__((address_space(3))) u32x4*)p = v; }
#define MX3(a, b, c) __builtin_fmaxf(__builtin_fmaxf((a), (b)), (c))

#define DMA4(POL, m0v, v0, v1, v2, v3, srd, soff)                                                     \
  asm volatile("s_mov_b32 m0, %0\n\ts_nop 0\n\t"                                                      \
               "buffer_load_dwordx4 %1, %5, %6 offen " POL " lds\n\t"                                 \
               "buffer_load_dwordx4 %2, %5, %6 offen offset:1024 " POL " lds\n\t"                     \
               "buffer_load_dwordx4 %3, %5, %6 offen offset:2048 " POL " lds\n\t"                     \
               "buffer_load_dwordx4 %4, %5, %6 offen offset:3072 " POL " lds"                         \
               :: "s"(m0v), "v"(v0), "v"(v1), "v"(v2), "v"(v3), "s"(srd), "s"(soff) : "memory")
#define DMA2(POL, m0v, v0, v1, srd, soff)                                                             \
  asm volatile("s_mov_b32 m0, %0\n\ts_nop 0\n\t"                                                      \
               "buffer_load_dwordx4 %1, %3, %4 offen " POL " lds\n\t"                                 \
               "buffer_load_dwordx4 %2, %3, %4 offen offset:1024 " POL " lds"                         \
               :: "s"(m0v), "v"(v0), "v"(v1), "s"(srd), "s"(soff) : "memory")
#define BLOAD(POL, dst, voff, srd, soff, imm)                                                         \
  asm volatile("buffer_load_dwordx4 %0, %1, %2, %3 offen offset:%4 " POL                              \
               : "=v"(dst) : "v"(voff), "s"(srd), "s"(soff), "i"(imm) : "memory")
#define WAITV(n) asm volatile("s_waitcnt vmcnt(%0)" :: "i"(n) : "memory")
#define WAITV4(n, a, b, c, d) asm volatile("s_waitcnt vmcnt(%4)" : "+v"(a), "+v"(b), "+v"(c), "+v"(d) : "i"(n) : "memory")

constexpr int K1_LDS = 163840;
constexpr size_t WS_FLAGS = (size_t)32 << 20, WS_EPOCH = WS_FLAGS + 4096;
DI constexpr int SLOT(int t) { return (t % 5) * 32768; }
DI constexpr int RB(int i) { return ((i + 2) % 5) * 32768; }
constexpr int PUBW = 32768;
DI constexpr int WQO(int c) { return (c & 3) == 0 ? 0 : (c & 3) == 1 ? 16384 : (c & 3) == 2 ? 131072 : 147456; }
DI constexpr int XO(int c) { return (c % 3) == 0 ? 65536 : (c % 3) == 1 ? 98304 : 32768; }

#define MFMA16(a, b, c) __builtin_amdgcn_mfma_f32_16x16x32_bf16(a, b, c, 0, 0, 0)
struct AttnState {
  f32x4 o[2][8];
  bf16x8 q[2][4];
  f32x4 negm[2];
  float mhat[2];
  f32x4 lacc[2];
  bf16x8 ones;
  bf16x8 kf[16];
  s16x4 vlo[16], vhi[16];
  u32x4 pw[2][2];
};
struct STile { f32x4 s[2][4]; };
DI float xmax4(float v) {
  auto a = __builtin_amdgcn_permlane16_swap(__float_as_uint(v), __float_as_uint(v), false, false);
  v = __builtin_fmaxf(__uint_as_float(a[0]), __uint_as_float(a[1]));
  auto c = __builtin_amdgcn_permlane32_swap(__float_as_uint(v), __float_as_uint(v), false, false);
  return __builtin_fmaxf(__uint_as_float(c[0]), __uint_as_float(c[1]));
}
DI float xsum4(float v) {
  auto a = __builtin_amdgcn_permlane16_swap(__float_as_uint(v), __float_as_uint(v), false, false);
  v = __uint_as_float(a[0]) + __uint_as_float(a[1]);
  auto c = __builtin_amdgcn_permlane32_swap(__float_as_uint(v), __float_as_uint(v), false, false);
  return __uint_as_float(c[0]) + __uint_as_float(c[1]);
}

#define PDV 5
constexpr int PD = PDV;
#define EL(T, n) (T).s[((n) >> 3) & 1][2 * ((n) >> 4) + (((n) >> 2) & 1)][(n) & 3]
#define ELV(T, n) (T).s[((n) >> 3) & 1][2 * ((n) >> 4) + (((n) >> 2) & 1)]
#define PWV(n) st.pw[((n) >> 3) & 1][(n) >> 4]
#define EX(v) __builtin_amdgcn_exp2f(v)
template <bool FIRST>
DI void attn_step(AttnState& st, STile& C, STile& P, lds_cptr kp, lds_cptr vp0, lds_cptr vp1, lds_cptr kn, const bool young) {
#define KRD(m, base) st.kf[m] = ldsrd128((base) + 4096 * ((m) >> 2) + 256 * ((m) & 3))
#define VRD(m) do { st.vlo[m] = ldstr((((m) & 1) ? vp1 : vp0) + 8192 * ((m) >> 3) + 256 * (((m) & 7) >> 1)); \
                    st.vhi[m] = ldstr((((m) & 1) ? vp1 : vp0) + 8192 * ((m) >> 3) + 4096 + 256 * (((m) & 7) >> 1)); } while (0)
  float ma = -3.0e38f, mb = -3.0e38f, fct0 = 1.f, fct1 = 1.f;
  bool resc = false;
  SBAR();
  static_for<32>([&](auto I) __attribute__((always_inline)) {
    constexpr int g = I.value;
    if constexpr (g == 0) { if (young) __builtin_amdgcn_s_setprio(1); }
    if constexpr (g == PRIO_TOGGLE) { __builtin_amdgcn_s_setprio(0); }
    if constexpr (g < 16) {
      constexpr int kb = g >> 2, u = g & 3;
      if constexpr (u == 0) C.s[0][kb] = MFMA16(st.kf[g], st.q[0][0], st.negm[0]); else C.s[0][kb] = MFMA16(st.kf[g], st.q[0][u], C.s[0][kb]);
      PIN(C.s[0][kb]);
    } else if constexpr (!FIRST) {
      constexpr int m = g - 16;
      st.o[0][m & 7] = MFMA16(cat(st.vlo[m], st.vhi[m]), __builtin_bit_cast(bf16x8, st.pw[0][m >> 3]), st.o[0][m & 7]);
      PIN(st.o[0][m & 7]);
    }
    SBAR();
    {
      constexpr int f = g + PD;
      if constexpr (f < 16) { KRD(f, kp); }
      else if constexpr (f < 32) { if constexpr (!FIRST) { VRD(f - 16); } }
      else { KRD(f - 32, kn); }
    }
    if constexpr ((g & 1) && g >= 5 && g <= 19) {
      constexpr int kb = (g - 5) >> 2, hf = ((g - 5) >> 1) & 1;
      ma = MX3(ma, C.s[0][kb][2 * hf], C.s[0][kb][2 * hf + 1]); PIN(ma);
    }
    SBAR();
    if constexpr (g < 16) {
      constexpr int kb = g >> 2, u = g & 3;
      if constexpr (u == 0) C.s[1][kb] = MFMA16(st.kf[g], st.q[1][0], st.negm[1]); else C.s[1][kb] = MFMA16(st.kf[g], st.q[1][u], C.s[1][kb]);
      PIN(C.s[1][kb]);
    } else if constexpr (!FIRST) {
      constexpr int m = g - 16;
      st.o[1][m & 7] = MFMA16(cat(st.vlo[m], st.vhi[m]), __builtin_bit_cast(bf16x8, st.pw[1][m >> 3]), st.o[1][m & 7]);
      PIN(st.o[1][m & 7]);
    }
    SBAR();
    if constexpr ((g & 1) == 0) {
      if constexpr (g <= 22) {
        if constexpr (!FIRST) { constexpr int n = 8 + g; PWV(n)[(n >> 1) & 3] = cvtpk(EL(P, n), EL(P, n + 1)); PIN(PWV(n)); }
      } else { constexpr int n = g - 24; PWV(n)[(n >> 1) & 3] = cvtpk(EL(C, n), EL(C, n + 1)); PIN(PWV(n)); }
    }
    if constexpr ((g & 1) && g >= 5 && g <= 19) {
      constexpr int kb = (g - 5) >> 2, hf = ((g - 5) >> 1) & 1;
      mb = MX3(mb, C.s[1][kb][2 * hf], C.s[1][kb][2 * hf + 1]); PIN(mb);
    }
    if constexpr (g == 22) {
      const bool ok = (ma <= THR) & (mb <= THR);
      if (FIRST || __builtin_expect(!__all(ok), 0)) {
#pragma unroll
        for (int qb = 0; qb < 2; ++qb) {
          float rm = qb ? mb : ma;
          rm = xmax4(rm);
          const float dl = FIRST ? rm : __builtin_fmaxf(rm, 0.f);
          st.mhat[qb] += dl;
#pragma unroll
          for (int kb = 0; kb < 4; ++kb)
#pragma unroll
            for (int r = 0; r < 4; ++r) C.s[qb][kb][r] -= dl;
#pragma unroll
          for (int r = 0; r < 4; ++r) st.negm[qb][r] = -st.mhat[qb];
          PIN(st.negm[qb]);
          if constexpr (!FIRST) { const float f_ = EX(-dl); if (qb == 0) fct0 = f_; else fct1 = f_; }
        }
        resc = true;
      }
    }
    if constexpr (g <= 21) {
      if constexpr (!FIRST) { constexpr int n = 10 + g; EL(P, n) = EX(EL(P, n)); PIN(ELV(P, n)); }
    } else { constexpr int n = g - 22; EL(C, n) = EX(EL(C, n)); PIN(ELV(C, n)); }
    SBAR();
    if constexpr (!FIRST && (g == 21 || g == 23 || g == 25 || g == 27)) {
      constexpr int qb = (g >> 1) & 1, w = g >= 24;
      st.lacc[qb] = MFMA16(st.ones, __builtin_bit_cast(bf16x8, st.pw[qb][w]), st.lacc[qb]); PIN(st.lacc[qb]);
      SBAR();
    }
  });
  if constexpr (!FIRST) {
    if (__builtin_expect(resc, 0)) {
#pragma unroll
      for (int d = 0; d < 8; ++d)
#pragma unroll
        for (int r = 0; r < 4; ++r) { st.o[0][d][r] *= fct0; st.o[1][d][r] *= fct1; }
#pragma unroll
      for (int r = 0; r < 4; ++r) { st.lacc[0][r] *= fct0; st.lacc[1][r] *= fct1; }
    }
  }
#undef KRD
#undef VRD
}

__global__ __launch_bounds__(512, 2)
void mha_k1(const char* __restrict__ x, const char* __restrict__ kc, const char* __restrict__ vc,
            const char* __restrict__ wq, const char* __restrict__ wo, char* ows, char* __restrict__ out) {
  extern __shared__ __attribute__((aligned(16))) char lds[];
  const int tid = threadIdx.x, lane = tid & 63;
  const int wid = __builtin_amdgcn_readfirstlane(tid >> 6);
  const int b = 4 * (blockIdx.x & 1) + ((blockIdx.x >> 3) & 3), h = 8 * ((blockIdx.x >> 1) & 3) + (blockIdx.x >> 5);
  const unsigned lds0 = (unsigned)(uintptr_t)lds;
  const lds_ptr L = (lds_ptr)lds;
  RSTAMP(62); STAMP(0);

  const char *pfx, *pfw;
  {
    const int j = blockIdx.x >> 3, half = lane >> 5, l5 = lane & 31;
    const int xrow = (b & 4) * SQ + 28 * j + (l5 < 28 ? l5 : 27);
    const int idx = 32 * j + l5;
    pfw = wq + (size_t)(64 * half + (idx >> 4)) * 8192 + (h & 24) * 256 + (idx & 15) * 128;
    pfx = x + (size_t)xrow * (DM * 2) + half * 128;
  }
  unsigned pfd0;
  const char* pfe = wid >= 0 ? x : (wid & 1) ? pfw + (size_t)(wid >> 1) * (128 * 8192) : pfx + (wid >> 1) * 256;
#define TOUCH(d, p) asm volatile("global_load_dword %0, %1, off " POLT : "=v"(d) : "v"(p) : "memory")
#define KEEP(d) asm volatile("" :: "v"(d) : "memory")
  const __amdgpu_buffer_rsrc_t srdW = __builtin_amdgcn_make_buffer_rsrc((void*)(wq + h * 256), (short)0, DM * 8192 - h * 256, 0x00020000);
  const unsigned pl = ((lane >> 2) & 3), dbl = lane >> 4, c4 = lane & 3;
  const unsigned wv0 = pl * 8192 + dbl * 64 + c4 * 16, wv1 = wv0 + 31744;
#define DMAW2(c) DMA2(POLW, lds0 + WQO(c) + 2048 * wid, wv0, wv1, srdW, (unsigned)(((c) * 64 + 8 * wid) * 8192))
  TOUCH(pfd0, pfe);
  const int ob = blockIdx.x & 7, oqt = blockIdx.x >> 6, ont = (blockIdx.x >> 3) & 7;
  const __amdgpu_buffer_rsrc_t srdWo = __builtin_amdgcn_make_buffer_rsrc((void*)(wo + ont * 128), (short)0, NH * DH * DM * 2 - ont * 128, 0x00020000);
  const unsigned ow0 = (4 * (lane >> 5) + ((lane >> 2) & 3)) * 1024 + ((lane >> 4) & 1) * 64 + (lane & 3) * 16;
  const unsigned ow1 = ow0 + 7168, ow2 = ow0 + 2 * 7168, ow3 = ow0 + 3 * 7168;
  unsigned long long* const epochp = (unsigned long long*)(ows + WS_EPOCH);
  unsigned long long* const flags = (unsigned long long*)(ows + WS_FLAGS);
  unsigned long long tag = 0;
  unsigned td = 0;
#define ROTM 0
#define ROTA 0
#define RR(i) (((i) + ROTM * ont + ROTA) & 15)
  constexpr int NA = P3NA;
  const int r32 = lane & 31, hi = lane >> 5, tq = (lane >> 2) & 3, tp = lane & 3, tblk = (lane >> 4) & 1;
  bf16x8 wf[NA][4];
#define RBP(i) ((lds_cptr)L + RB(i) + rbo)
#define WDMA(i) DMA4("", lds0 + (unsigned)(RB(i)) + 4096u * w3, ow0, ow1, ow2, ow3, srdWo, 32768u * w3 + (unsigned)(RR(i) * 256 * 1024))
#define W3() unsigned w3 = (unsigned)__builtin_amdgcn_readfirstlane(tid >> 6); asm volatile("s_nop 4" : "+s"(w3))
#define WREAD_NW(i) do { lds_cptr p_ = RBP(i); PIN(p_); _Pragma("unroll") for (int f_ = 0; f_ < 4; ++f_) \
      wf[(i) % NA][f_] = cat(ldstr(p_ + 1024 * (f_ >> 1) + 256 * (f_ & 1)), ldstr(p_ + 1024 * (f_ >> 1) + 2048 + 256 * (f_ & 1))); } while (0)
#define WRWAIT(i) asm volatile("s_waitcnt lgkmcnt(0)" : "+v"(wf[(i) % NA][0]), "+v"(wf[(i) % NA][1]), "+v"(wf[(i) % NA][2]), "+v"(wf[(i) % NA][3]) :: "memory")
#define WREAD(i) do { WREAD_NW(i); WRWAIT(i); } while (0)
#define RBO() (wid * 4096 + 512 * hi + 64 * tq + 32 * tblk + 8 * tp)
  if (wid == 7) {
    const __amdgpu_buffer_rsrc_t srdK = __builtin_amdgcn_make_buffer_rsrc((void*)(kc + (size_t)(b * NH + h) * (SK * DH * 2)), (short)0, SK * DH * 2, 0x00020000);
    const __amdgpu_buffer_rsrc_t srdV = __builtin_amdgcn_make_buffer_rsrc((void*)(vc + (size_t)(b * NH + h) * (SK * DH * 2)), (short)0, SK * DH * 2, 0x00020000);
    const unsigned wv2 = wv0 + 2 * 31744, wv3 = wv0 + 3 * 31744;
    const unsigned kvA = pl * 256 + dbl * 64 + c4 * 16, kvB = pl * 256 + dbl * 64 + ((c4 ^ 2) * 16);
#define DMAW(c) do { _Pragma("unroll") for (int i_ = 0; i_ < 4; ++i_) DMA4(POLW, lds0 + (unsigned)(WQO(c) + 4096 * i_), wv0, wv1, wv2, wv3, srdW, (unsigned)(((c) * 64 + 16 * i_) * 8192)); } while (0)
#define DMAK_(t, so) do { _Pragma("unroll") for (int i_ = 0; i_ < 4; ++i_) { WAITV(59); DMA4("nt", lds0 + (unsigned)((so) + 4096 * i_), kvA, kvA, kvB, kvB, srdK, (unsigned)((t) * 16384 + 4096 * i_)); } } while (0)
#define DMAV_(t, so) do { _Pragma("unroll") for (int i_ = 0; i_ < 4; ++i_) { WAITV(59); DMA4("nt", lds0 + (unsigned)((so) + 16384 + 4096 * i_), kvA, kvB, kvA, kvB, srdV, (unsigned)((t) * 16384 + 4096 * i_)); } } while (0)
#define DMAK(t) DMAK_(t, SLOT(t))
#define DMAV(t) DMAV_(t, SLOT((t) + 1))
    const char* tw = wo + (size_t)(blockIdx.x >> 3) * 8192 + lane * 128;
#define TW(r) do { const char* p_ = tw + (size_t)((((r) + ROTM * (lane & 7) + ROTA) & 15) * 262144); asm volatile("s_waitcnt vmcnt(62)\n\tglobal_load_dword %0, %1, off" : "+v"(td) : "v"(p_) : "memory"); } while (0)
    asm volatile("global_load_dwordx2 %0, %1, off sc1" : "=v"(tag) : "v"(epochp) : "memory");
#define RW(i) do { _Pragma("unroll") for (int w_ = 0; w_ < 8; ++w_) { WAITV(59); DMA4("", lds0 + (unsigned)(RB(i) + 4096 * w_), ow0, ow1, ow2, ow3, srdWo, (unsigned)((RR(i) * 256 + 32 * w_) * 1024)); } } while (0)
    DMAW2(0);
    BARRIER();
    const unsigned ktv = lane * 128;
#define KVT(i) asm volatile("buffer_load_dword %0, %1, %2, %3 offen nt" : "+v"(td) : "v"(ktv), \
      "s"((((i) >> 1) >= 2 && !(((i) >> 1) & 1)) ? srdV : srdK), \
      "s"((unsigned)(((((i) >> 1) >= 2 && !(((i) >> 1) & 1)) ? (((i) >> 1) - 2) / 2 : (((i) >> 1) + 1) / 2) * 16384 + ((i) & 1) * 8192)) : "memory")
#define KVTS(k)
    DMAW(1);
    WAITV(16); BARRIER();
    DMAW(2);
    WAITV(16); BARRIER();
    DMAW(3); KVTS(0);
    WAITV(16 + KVTN); BARRIER();
    DMAW(4); KVTS(1);
    WAITV(16 + 2 * KVTN); BARRIER();
    DMAW(5); KVTS(2);
    WAITV(16 + 2 * KVTN); BARRIER();
    KEEP(pfd0);
    DMAW(6); KVTS(3);
    WAITV(16 + 2 * KVTN); BARRIER();
    DMAW(7); KVTS(4);
    WAITV(16 + 2 * KVTN); BARRIER();
    DMAK(0); DMAK(1); DMAV(0);
    WAITV(48 + KVTN); BARRIER();
    BARRIER();
    DMAK(2);
    WAITV(32); BARRIER();
    DMAV(1); DMAK(3); DMAV(2);
    WAITV(48); BARRIER();
    DMAK(4); DMAV(3); DMAK(5); DMAV(4);
    WAITV(63); BARRIER();
    {
      int so = SLOT(1);
      for (int t = 1; t < 11; ++t) {
        DMAK_(t + 5, so); DMAV_(t + 4, so);
        so = (so == 131072) ? 0 : so + 32768;
        WAITV(63);
        BARRIER();
      }
    }
    DMAV(15); TW(0); TW(1); TW(2); TW(3);
    asm volatile("" : "+v"(tag));
    tag = (((unsigned long long)(unsigned)__builtin_amdgcn_readfirstlane((unsigned)(tag >> 32)) << 32) | (unsigned)__builtin_amdgcn_readfirstlane((unsigned)tag)) + 1;
    if (lane == 0) { *(__attribute__((address_space(3))) unsigned*)(L + PUBW) = 0u; *(__attribute__((address_space(3))) unsigned long long*)(L + PUBW + 8) = tag; }
    asm volatile("s_waitcnt lgkmcnt(0)" ::: "memory");
    WAITV(63);
    BARRIER();
    TW(5); TW(6); TW(7); TW(4);
    WAITV(40);
    RW(0);
    BARRIER();
    RW(1); TW(8); TW(10); TW(11); TW(12);
    WAITV(63);
    BARRIER();
    RW(2); TW(9); TW(13); TW(14); TW(15);
    WAITV(4);
    BARRIER();
    {
      const int rbo = RBO();
      WREAD(0); WREAD(1); WREAD(2);
    }
    BARRIER();
    RW(3); RW(5); RW(6); RW(7);
    WAITV(0);
#undef TW
#undef RW
#undef DMAW
#undef DMAK
#undef DMAV
  } else {
  const __amdgpu_buffer_rsrc_t srdX = __builtin_amdgcn_make_buffer_rsrc((void*)(x + (size_t)(b * SQ + wid * 32) * (DM * 2)), (short)0, 32 * DM * 2, 0x00020000);
  const unsigned xs = (lane >> 3) * 1024 + (((lane & 7) ^ (lane >> 4)) * 16);
  const unsigned xv0 = xs, xv1 = (xs ^ 64) + 7168, xv2 = xs + 2 * 7168, xv3 = (xs ^ 64) + 3 * 7168;
#define XDMA(c) DMA4(POLW, lds0 + (unsigned)XO(c) + 4096u * wid, xv0, xv1, xv2, xv3, srdX, (unsigned)(128 * (c)))
  const unsigned xrb = (unsigned)(wid * 4096 + r32 * 128 + ((hi ^ ((r32 >> 1) & 7)) * 16));
  bf16x8 xf[2][4];
#define XRD(c, k_) xf[(c) & 1][k_] = ldsrd128((lds_cptr)L + ((xrb ^ (unsigned)((k_) << 5)) + XO(c)))
  DMAW2(0); XDMA(0);
  BARRIER();
  XDMA(1);
  const lds_cptr wrb = (lds_cptr)L + 2048 * hi + 64 * tq + 32 * (tp & 1) + 16 * tblk + 8 * (tp >> 1);
  lds_cptr wrb2 = wrb + 131072; PIN(wrb2);
#define WRB(c) ((WQO(c) < 65536 ? wrb : wrb2) + (WQO(c) & 65535))
#define WRD(c, m) do { wlo[m] = ldstr(WRB(c) + 4096 * ((m) >> 2) + 256 * ((m) & 3)); whi[m] = ldstr(WRB(c) + 4096 * ((m) >> 2) + 1024 + 256 * ((m) & 3)); } while (0)
  AttnState st;
  f32x16 acc[4];
#pragma unroll
  for (int i = 0; i < 4; ++i) acc[i] = f32x16{};
  s16x4 wlo[16], whi[16];
  WAITV(4);
  XRD(0, 0); XRD(0, 1); XRD(0, 2); XRD(0, 3);
  STAMP(1);
  BARRIER();
  STAMP(2);
#pragma unroll
  for (int m = 0; m < PD; ++m) WRD(0, m);
  u32x4 qx[4], qy[4];
#define QCV1(nb, e_) do { \
      const unsigned lo_ = cvtpk(acc[nb][2 * (e_)] * QSCALE, acc[nb][2 * (e_) + 1] * QSCALE), hi_ = cvtpk(acc[nb][8 + 2 * (e_)] * QSCALE, acc[nb][9 + 2 * (e_)] * QSCALE); \
      auto r_ = __builtin_amdgcn_permlane16_swap(lo_, hi_, false, false); qx[nb][e_] = r_[0]; qy[nb][e_] = r_[1]; PIN(qx[nb]); PIN(qy[nb]); } while (0)
  static_for<128>([&](auto I) __attribute__((always_inline)) {
    constexpr int c = I.value >> 4, mm = I.value & 15;
    constexpr int m = (c == 7) ? (4 * (mm & 3) + (mm >> 2)) : mm;
    acc[m & 3] = MFMA(cat(wlo[m], whi[m]), xf[c & 1][m >> 2], acc[m & 3]); PIN(acc[m & 3]);
    if constexpr (c == 0 && mm + PD == 16) { BARRIER(); }
    if constexpr (mm + PD < 16) { constexpr int f = mm + PD, ff = (c == 7) ? (4 * (f & 3) + (f >> 2)) : f; WRD(c, ff); }
    else if constexpr (c < 7) { constexpr int f = mm + PD - 16, ff = (c + 1 == 7) ? (4 * (f & 3) + (f >> 2)) : f; WRD(c + 1, ff); }
    if constexpr (c == 0 && mm == 0) { XDMA(2); }
    if constexpr (c == 0 && mm == 2) { XDMA(3); }
    if constexpr (c >= 1 && c <= 4 && mm == 0) { XDMA(c + 3); }
    if constexpr (c < 7) {
      if constexpr (mm == 4) { WAITV(c <= 4 ? 8 : 4 * (6 - c)); }
      if constexpr (mm == 5 && c == 1) { KEEP(pfd0); }
      if constexpr (mm >= 4 && mm < 8) { XRD(c + 1, mm - 4); }
    }
    if constexpr (c == 5) { st.o[mm >> 3][mm & 7] = f32x4{}; PIN(st.o[mm >> 3][mm & 7]); }
    if constexpr (c == 7 && mm >= 5) { QCV1((mm - 5) >> 2, (mm - 5) & 3); }
    SBAR();
    if constexpr (mm == 15) { BARRIER(); STAMP(3 + c); SBAR(); }
  });
  LSTAMP(58);
#undef XDMA
#undef XRD
#undef WRD
  const int l16 = lane & 15, g4 = lane >> 4;
  const lds_cptr kb = (lds_cptr)L + 1024 * (l16 >> 2) + 64 * (l16 & 3) + 16 * (g4 ^ ((l16 >> 2) & 2));
  const lds_cptr vb0 = (lds_cptr)L + 16384 + 1024 * g4 + 64 * tq + 32 * (g4 & 1) + 8 * tp;
  const lds_cptr vb1 = (lds_cptr)L + 16384 + 1024 * g4 + 64 * tq + 32 * (1 - (g4 & 1)) + 8 * tp;
#pragma unroll
  for (int m = 0; m < PD; ++m) st.kf[m] = ldsrd128(kb + SLOT(0) + 4096 * (m >> 2) + 256 * (m & 3));
  QCV1(2, 3); QCV1(3, 0); QCV1(3, 1); QCV1(3, 2); QCV1(3, 3);
#undef QCV1
#pragma unroll
  for (int nb = 0; nb < 4; ++nb) { st.q[0][nb] = __builtin_bit_cast(bf16x8, qx[nb]); st.q[1][nb] = __builtin_bit_cast(bf16x8, qy[nb]); }
  st.negm[0] = f32x4{}; st.negm[1] = f32x4{}; PIN(st.negm[0]); PIN(st.negm[1]);
  st.mhat[0] = st.mhat[1] = 0.f; st.lacc[0] = f32x4{}; st.lacc[1] = f32x4{};
  st.ones = bf16x8{0x3F80, 0x3F80, 0x3F80, 0x3F80, 0x3F80, 0x3F80, 0x3F80, 0x3F80}; PIN(st.ones);

  STile A, B;
  attn_step<true>(st, A, B, kb + SLOT(0), vb0 + SLOT(0), vb1 + SLOT(0), kb + SLOT(1), wid >= 4);
  STAMP(11);
  BARRIER();
  STAMP(12);
  int s0 = SLOT(1);
  for (int t = 1; t < 15; t += 2) {
    const int s1 = (s0 == 131072) ? 0 : s0 + 32768, s2 = (s1 == 131072) ? 0 : s1 + 32768;
    attn_step<false>(st, B, A, kb + s0, vb0 + s0, vb1 + s0, kb + s1, wid >= 4);
    STAMP(11 + 2 * t);
    BARRIER();
    STAMP(12 + 2 * t);
    attn_step<false>(st, A, B, kb + s1, vb0 + s1, vb1 + s1, kb + s2, wid >= 4);
    STAMP(13 + 2 * t);
    BARRIER();
    STAMP(14 + 2 * t);
    s0 = s2;
  }
  attn_step<false>(st, B, A, kb + SLOT(15), vb0 + SLOT(15), vb1 + SLOT(15), kb + SLOT(15), wid >= 4);
  STAMP(41);
  BARRIER();
  STAMP(42); LSTAMP(59);
  {
    lds_cptr vd0 = vb0 + SLOT(16), vd1 = vb1 + SLOT(16); PIN(vd0); PIN(vd1);
#define VRD(m) do { st.vlo[m] = ldstr((((m) & 2) ? vd1 : vd0) + 8192 * ((m) & 1) + 256 * ((m) >> 2)); st.vhi[m] = ldstr((((m) & 2) ? vd1 : vd0) + 8192 * ((m) & 1) + 4096 + 256 * ((m) >> 2)); } while (0)
    const int rbo = RBO();
    WREAD(0); WREAD(1); WREAD(2);
    BARRIER();
#pragma unroll
    for (int m = 0; m < 8; ++m) VRD(m);
    static_for<22>([&](auto I) __attribute__((always_inline)) { constexpr int n = 10 + I.value; EL(B, n) = EX(EL(B, n)); });
    static_for<12>([&](auto I) __attribute__((always_inline)) { constexpr int n = 8 + 2 * I.value; PWV(n)[(n >> 1) & 3] = cvtpk(EL(B, n), EL(B, n + 1)); });
#pragma unroll
    for (int w = 0; w < 2; ++w) {
      st.lacc[0] = MFMA16(st.ones, __builtin_bit_cast(bf16x8, st.pw[0][w]), st.lacc[0]);
      st.lacc[1] = MFMA16(st.ones, __builtin_bit_cast(bf16x8, st.pw[1][w]), st.lacc[1]);
    }
    const float inv0 = 1.0f / st.lacc[0][0], inv1 = 1.0f / st.lacc[1][0];
    const __amdgpu_buffer_rsrc_t srdOs = __builtin_amdgcn_make_buffer_rsrc((void*)(ows + (size_t)(b * NH + h) * 57344), (short)0, 57344, 0x00020000);
    const int qr0 = 32 * wid + l16, vk0 = qr0 / 28, qr1 = qr0 + 16, vk1 = qr1 / 28;
    const unsigned osv0 = vk0 * 7168 + (g4 >> 1) * 896 + ((g4 & 1) * 28 + qr0 - 28 * vk0) * 16, osv1 = vk1 * 7168 + (g4 >> 1) * 896 + ((g4 & 1) * 28 + qr1 - 28 * vk1) * 16;
#define OSTORE(u) do { \
      u32x4 w0_ = {cvtpk(st.o[0][2 * (u)][0] * inv0, st.o[0][2 * (u)][1] * inv0), cvtpk(st.o[0][2 * (u)][2] * inv0, st.o[0][2 * (u)][3] * inv0), \
                   cvtpk(st.o[0][2 * (u) + 1][0] * inv0, st.o[0][2 * (u) + 1][1] * inv0), cvtpk(st.o[0][2 * (u) + 1][2] * inv0, st.o[0][2 * (u) + 1][3] * inv0)}; \
      __builtin_amdgcn_raw_buffer_store_b128(w0_, srdOs, osv0 + (u) * 1792, 0, 16); \
      u32x4 w1_ = {cvtpk(st.o[1][2 * (u)][0] * inv1, st.o[1][2 * (u)][1] * inv1), cvtpk(st.o[1][2 * (u)][2] * inv1, st.o[1][2 * (u)][3] * inv1), \
                   cvtpk(st.o[1][2 * (u) + 1][0] * inv1, st.o[1][2 * (u) + 1][1] * inv1), cvtpk(st.o[1][2 * (u) + 1][2] * inv1, st.o[1][2 * (u) + 1][3] * inv1)}; \
      __builtin_amdgcn_raw_buffer_store_b128(w1_, srdOs, osv1 + (u) * 1792, 0, 16); } while (0)
    SBAR();
#pragma unroll
    for (int u = 0; u < 4; ++u) {
#pragma unroll
      for (int k = 0; k < 4; ++k) {
        const int m = 4 * u + k, db = m >> 1, w = m & 1;
        st.o[0][db] = MFMA16(cat(st.vlo[m], st.vhi[m]), __builtin_bit_cast(bf16x8, st.pw[0][w]), st.o[0][db]);
        st.o[1][db] = MFMA16(cat(st.vlo[m], st.vhi[m]), __builtin_bit_cast(bf16x8, st.pw[1][w]), st.o[1][db]);
        if (u < 2) { VRD(m + 8); }
      }
      SBAR();
      if (u >= 1) { OSTORE(u - 1); }
      SBAR();
    }
    STAMP(43);
    OSTORE(3);
    STAMP(44);
#undef VRD
#undef OSTORE
    WAITV(0);
    if (lane == 0) {
      const unsigned old = __hip_atomic_fetch_add((__attribute__((address_space(3))) unsigned*)(L + PUBW), 1u, __ATOMIC_RELAXED, __HIP_MEMORY_SCOPE_WORKGROUP);
      if (old == 6) __hip_atomic_store(flags + b * NH + h, *(__attribute__((address_space(3))) unsigned long long*)(L + PUBW + 8), __ATOMIC_RELAXED, __HIP_MEMORY_SCOPE_AGENT);
    }
    asm volatile("s_waitcnt lgkmcnt(0)" ::: "memory");
    STAMP(50);
  }
  }

  {
    static_assert(NA == 8, "prefetch sequences above are written for NA = 8");
    const int rbo = RBO(); W3();
    const __amdgpu_buffer_rsrc_t srdO = __builtin_amdgcn_make_buffer_rsrc((void*)(ows + (size_t)ob * (NH * 57344)), (short)0, NH * 57344, 0x00020000);
    const unsigned ov0 = (hi * 28 + (r32 < 28 ? r32 : 27)) * 16;
    const unsigned osb = (w3 >> 2) * 57344 + (w3 & 3) * 1792 + oqt * 2 * 7168;
    u32x4 of[3][4];
    f32x16 acc[2][2];
#pragma unroll
    for (int i = 0; i < 2; ++i) { acc[i][0] = f32x16{}; acc[i][1] = f32x16{}; }
#define OLOAD(i) do { const unsigned so_ = osb + (unsigned)(RR(i) * 2 * 57344), so1_ = so_ + 7168u; \
      BLOAD("sc1", of[(i) % 3][0], ov0, srdO, so_, 0); BLOAD("sc1", of[(i) % 3][1], ov0, srdO, so1_, 0); \
      BLOAD("sc1", of[(i) % 3][2], ov0, srdO, so_, 896); BLOAD("sc1", of[(i) % 3][3], ov0, srdO, so1_, 896); } while (0)
    BARRIER();
    LSTAMP(57);
    STAMP(51);
    WDMA(4);
    WREAD(3); WDMA(8); WREAD(5); WDMA(10); WREAD(6); WDMA(11); WREAD(7); WDMA(12);
    WAITV(16); WREAD(4); WDMA(9);
    STAMP(52);
    if (w3 == 7) {
      if (blockIdx.x != 0) {
        const unsigned long long* fp = flags + ob * NH + r32;
        for (unsigned spins = 0; spins < (1u << 18); ++spins) {
          const unsigned long long v = __hip_atomic_load(fp, __ATOMIC_RELAXED, __HIP_MEMORY_SCOPE_AGENT);
          if (__all(v == tag)) break;
          __builtin_amdgcn_s_sleep(1);
        }
      } else {
        for (unsigned spins = 0; spins < (1u << 18); ++spins) {
          bool ok = true;
#pragma unroll
          for (int k = 0; k < 4; ++k) ok &= __hip_atomic_load(flags + 64 * k + lane, __ATOMIC_RELAXED, __HIP_MEMORY_SCOPE_AGENT) == tag;
          if (__all(ok)) break;
          __builtin_amdgcn_s_sleep(1);
        }
        if (lane == 0) __hip_atomic_store(epochp, tag, __ATOMIC_RELAXED, __HIP_MEMORY_SCOPE_AGENT);
      }
      KEEP(td);
    }
    STAMP(53);
    BARRIER();
    LSTAMP(60);
    STAMP(54);
    OLOAD(0); OLOAD(1);
#pragma unroll
    for (int i = 0; i < 16; ++i) {
      const int cnt = (i < 15 ? 4 : 0) + ((i - 1 >= 1 && i - 1 <= 3) ? 4 : 0);
      WAITV4(cnt, of[i % 3][0], of[i % 3][1], of[i % 3][2], of[i % 3][3]);
      if (i >= 1 && i <= 3) { WRWAIT(i - 1 + NA); WDMA(i + 12); }
      if (i + 2 < 16) { OLOAD(i + 2); }
      SBAR();
#pragma unroll
      for (int ks = 0; ks < 2; ++ks)
#pragma unroll
        for (int nb = 0; nb < 2; ++nb)
#pragma unroll
          for (int vb = 0; vb < 2; ++vb)
            acc[nb][vb] = MFMA(wf[i % NA][2 * ks + nb], __builtin_bit_cast(bf16x8, of[i % 3][2 * ks + vb]), acc[nb][vb]);
      SBAR();
      if (i + NA < 16) { WREAD_NW(i + NA); }
      SBAR();
    }
    STAMP(55);
#undef WDMA
#undef WREAD
#undef OLOAD
#undef RBP
#pragma unroll
    for (int vb = 0; vb < 2; ++vb)
#pragma unroll
      for (int nb = 0; nb < 2; ++nb)
#pragma unroll
        for (int rg = 0; rg < 4; ++rg) {
          const int q = 32 * vb + r32, slot = 8 * nb + 2 * rg + hi;
          f32x4 v = {acc[nb][vb][4 * rg], acc[nb][vb][4 * rg + 1], acc[nb][vb][4 * rg + 2], acc[nb][vb][4 * rg + 3]};
          *(__attribute__((address_space(3))) f32x4*)(L + (q >> 4) * 32768 + wid * 4096 + (q & 15) * 256 + ((slot ^ (q & 15)) * 16)) = v;
        }
    asm volatile("s_waitcnt lgkmcnt(0)" ::: "memory");
    BARRIER();
    STAMP(56); LSTAMP(61);
    const int q = tid >> 3, pc = tid & 7;
    f32x4 s0 = {0, 0, 0, 0}, s1 = {0, 0, 0, 0};
#pragma unroll
    for (int w = 0; w < 8; ++w) {
      s0 += *(const __attribute__((address_space(3))) f32x4*)(L + (q >> 4) * 32768 + w * 4096 + (q & 15) * 256 + (((2 * pc) ^ (q & 15)) * 16));
      s1 += *(const __attribute__((address_space(3))) f32x4*)(L + (q >> 4) * 32768 + w * 4096 + (q & 15) * 256 + (((2 * pc + 1) ^ (q & 15)) * 16));
    }
    u32x4 w = {cvtpk(s0[0], s0[1]), cvtpk(s0[2], s0[3]), cvtpk(s1[0], s1[1]), cvtpk(s1[2], s1[3])};
    if ((q & 31) < 28)
      *(u32x4*)(out + (size_t)(ob * SQ + 56 * oqt + 28 * (q >> 5) + (q & 31)) * (DM * 2) + ont * 128 + pc * 16) = w;
  }
  STAMP(40); RSTAMP(63);
  STFLUSH(ows + (64 << 20));
}

extern "C" void kernel_launch(void* const* d_in, const int* in_sizes, int n_in,
                              void* d_out, int out_size, void* d_ws, size_t ws_size,
                              hipStream_t stream) {
  (void)in_sizes; (void)n_in; (void)out_size; (void)ws_size;
  (void)hipFuncSetAttribute((const void*)mha_k1, hipFuncAttributeMaxDynamicSharedMemorySize, K1_LDS);
  mha_k1<<<256, 512, K1_LDS, stream>>>((const char*)d_in[0], (const char*)d_in[1], (const char*)d_in[2], (const char*)d_in[3], (const char*)d_in[4], (char*)d_ws, (char*)d_out);
}
```

```cpp
#include <hip/hip_runtime.h>
#include <hip/hip_bf16.h>
#include <stdint.h>
#include <utility>
#include <type_traits>

typedef short    bf16x8 __attribute__((ext_vector_type(8)));
typedef short    s16x4  __attribute__((ext_vector_type(4)));
typedef float    f32x16 __attribute__((ext_vector_type(16)));
typedef float    f32x4  __attribute__((ext_vector_type(4)));
typedef float    f32x2  __attribute__((ext_vector_type(2)));
typedef __bf16   bf16x2 __attribute__((ext_vector_type(2)));
typedef unsigned u32x4  __attribute__((ext_vector_type(4)));
typedef __attribute__((address_space(3))) char* lds_ptr;
typedef __attribute__((address_space(3))) const char* lds_cptr;

#define DI __device__ __forceinline__
#define SBAR() __builtin_amdgcn_sched_barrier(0)
#define PIN(x) asm volatile("" : "+v"(x))
#define MFMA(a, b, c) __builtin_amdgcn_mfma_f32_32x32x16_bf16(a, b, c, 0, 0, 0)
#define BARRIER() asm volatile("s_barrier" ::: "memory")

#define STAMP(i)
#define RSTAMP(i)
#define LSTAMP(i)
#define STFLUSH(dbg)
#define POLT "nt"
#define POLW ""
#define POLWO ""
#define POLT2 ""
#define POLO "sc1"
#define P3NA 8
#define TSCHED 3
#define PRIO_TOGGLE 21
#define EARLYKV 0
#define MAPK1 1
constexpr int NB = 8, SQ = 224, DM = 512, NH = 32, SK = 1024, DH = 128;
constexpr float QSCALE = 0.088388347648318440f * 1.4426950408889634f;
constexpr float THR = 8.0f;

template <int... Is, class F> DI void static_for_(std::integer_sequence<int, Is...>, F&& f) { (f(std::integral_constant<int, Is>{}), ...); }
template <int N, class F> DI void static_for(F&& f) { static_for_(std::make_integer_sequence<int, N>{}, f); }
DI unsigned cvtpk(float lo, float hi) { f32x2 v = {lo, hi}; bf16x2 b = __builtin_convertvector(v, bf16x2); return __builtin_bit_cast(unsigned, b); }
DI bf16x8 ldsrd128(lds_cptr p) { return *(const __attribute__((address_space(3))) bf16x8*)p; }
DI s16x4 ldstr(lds_cptr p) { return __builtin_bit_cast(s16x4, __builtin_amdgcn_ds_read_tr16_b64_v4i16((__attribute__((address_space(3))) s16x4*)p)); }
DI bf16x8 cat(s16x4 a, s16x4 b) { return __builtin_shufflevector(a, b, 0, 1, 2, 3, 4, 5, 6, 7); }
DI void ldswr128(lds_ptr p, u32x4 v) { *(__attribute__((address_space(3))) u32x4*)p = v; }
#define MX3(a, b, c) __builtin_fmaxf(__builtin_fmaxf((a), (b)), (c))

#define DMA4(POL, m0v, v0, v1, v2, v3, srd, soff)                                                     \
  asm volatile("s_mov_b32 m0, %0\n\ts_nop 0\n\t"                                                      \
               "buffer_load_dwordx4 %1, %5, %6 offen " POL " lds\n\t"                                 \
               "buffer_load_dwordx4 %2, %5, %6 offen offset:1024 " POL " lds\n\t"                     \
               "buffer_load_dwordx4 %3, %5, %6 offen offset:2048 " POL " lds\n\t"                     \
               "buffer_load_dwordx4 %4, %5, %6 offen offset:3072 " POL " lds"                         \
               :: "s"(m0v), "v"(v0), "v"(v1), "v"(v2), "v"(v3), "s"(srd), "s"(soff) : "memory")
#define DMA2(POL, m0v, v0, v1, srd, soff)                                                             \
  asm volatile("s_mov_b32 m0, %0\n\ts_nop 0\n\t"                                                      \
               "buffer_load_dwordx4 %1, %3, %4 offen " POL " lds\n\t"                                 \
               "buffer_load_dwordx4 %2, %3, %4 offen offset:1024 " POL " lds"                         \
               :: "s"(m0v), "v"(v0), "v"(v1), "s"(srd), "s"(soff) : "memory")
#define BLOAD(POL, dst, voff, srd, soff, imm)                                                         \
  asm volatile("buffer_load_dwordx4 %0, %1, %2, %3 offen offset:%4 " POL                              \
               : "=v"(dst) : "v"(voff), "s"(srd), "s"(soff), "i"(imm) : "memory")
#define WAITV(n) asm volatile("s_waitcnt vmcnt(%0)" :: "i"(n) : "memory")
#define WAITV4(n, a, b, c, d) asm volatile("s_waitcnt vmcnt(%4)" : "+v"(a), "+v"(b), "+v"(c), "+v"(d) : "i"(n) : "memory")

constexpr int K1_LDS = 163840;
constexpr size_t WS_FLAGS = (size_t)32 << 20, WS_EPOCH = WS_FLAGS + 4096;
DI constexpr int SLOT(int t) { return (t % 5) * 32768; }
DI constexpr int RB(int i) { return ((i + 2) % 5) * 32768; }
constexpr int PUBW = 32768;
DI constexpr int WQO(int c) { return (c & 3) == 0 ? 0 : (c & 3) == 1 ? 16384 : (c & 3) == 2 ? 131072 : 147456; }
DI constexpr int XO(int c) { return (c % 3) == 0 ? 65536 : (c % 3) == 1 ? 98304 : 32768; }

#define MFMA16(a, b, c) __builtin_amdgcn_mfma_f32_16x16x32_bf16(a, b, c, 0, 0, 0)
struct AttnState {
  f32x4 o[2][8];
  bf16x8 q[2][4];
  f32x4 negm[2];
  float mhat[2];
  f32x4 lacc[2];
  bf16x8 ones;
  bf16x8 kf[16];
  s16x4 vlo[16], vhi[16];
  u32x4 pw[2][2];
};
struct STile { f32x4 s[2][4]; };
DI float xmax4(float v) {
  auto a = __builtin_amdgcn_permlane16_swap(__float_as_uint(v), __float_as_uint(v), false, false);
  v = __builtin_fmaxf(__uint_as_float(a[0]), __uint_as_float(a[1]));
  auto c = __builtin_amdgcn_permlane32_swap(__float_as_uint(v), __float_as_uint(v), false, false);
  return __builtin_fmaxf(__uint_as_float(c[0]), __uint_as_float(c[1]));
}
DI float xsum4(float v) {
  auto a = __builtin_amdgcn_permlane16_swap(__float_as_uint(v), __float_as_uint(v), false, false);
  v = __uint_as_float(a[0]) + __uint_as_float(a[1]);
  auto c = __builtin_amdgcn_permlane32_swap(__float_as_uint(v), __float_as_uint(v), false, false);
  return __uint_as_float(c[0]) + __uint_as_float(c[1]);
}

#define PDV 5
constexpr int PD = PDV;
#define EL(T, n) (T).s[((n) >> 3) & 1][2 * ((n) >> 4) + (((n) >> 2) & 1)][(n) & 3]
#define ELV(T, n) (T).s[((n) >> 3) & 1][2 * ((n) >> 4) + (((n) >> 2) & 1)]
#define PWV(n) st.pw[((n) >> 3) & 1][(n) >> 4]
#define EX(v) __builtin_amdgcn_exp2f(v)
template <bool FIRST>
DI void attn_step(AttnState& st, STile& C, STile& P, lds_cptr kp, lds_cptr vp0, lds_cptr vp1, lds_cptr kn, const bool young) {
#define KRD(m, base) st.kf[m] = ldsrd128((base) + 4096 * ((m) >> 2) + 256 * ((m) & 3))
#define VRD(m) do { st.vlo[m] = ldstr((((m) & 1) ? vp1 : vp0) + 8192 * ((m) >> 3) + 256 * (((m) & 7) >> 1)); \
                    st.vhi[m] = ldstr((((m) & 1) ? vp1 : vp0) + 8192 * ((m) >> 3) + 4096 + 256 * (((m) & 7) >> 1)); } while (0)
  float ma = -3.0e38f, mb = -3.0e38f, fct0 = 1.f, fct1 = 1.f;
  bool resc = false;
  SBAR();
  static_for<32>([&](auto I) __attribute__((always_inline)) {
    constexpr int g = I.value;
    if constexpr (g == 0) { if (young) __builtin_amdgcn_s_setprio(1); }
    if constexpr (g == PRIO_TOGGLE) { __builtin_amdgcn_s_setprio(0); }
    if constexpr (g < 16) {
      constexpr int kb = g >> 2, u = g & 3;
      if constexpr (u == 0) C.s[0][kb] = MFMA16(st.kf[g], st.q[0][0], st.negm[0]); else C.s[0][kb] = MFMA16(st.kf[g], st.q[0][u], C.s[0][kb]);
      PIN(C.s[0][kb]);
    } else if constexpr (!FIRST) {
      constexpr int m = g - 16;
      st.o[0][m & 7] = MFMA16(cat(st.vlo[m], st.vhi[m]), __builtin_bit_cast(bf16x8, st.pw[0][m >> 3]), st.o[0][m & 7]);
      PIN(st.o[0][m & 7]);
    }
    SBAR();
    {
      constexpr int f = g + PD;
      if constexpr (f < 16) { KRD(f, kp); }
      else if constexpr (f < 32) { if constexpr (!FIRST) { VRD(f - 16); } }
      else { KRD(f - 32, kn); }
    }
    if constexpr ((g & 1) && g >= 5 && g <= 19) {
      constexpr int kb = (g - 5) >> 2, hf = ((g - 5) >> 1) & 1;
      ma = MX3(ma, C.s[0][kb][2 * hf], C.s[0][kb][2 * hf + 1]); PIN(ma);
    }
    SBAR();
    if constexpr (g < 16) {
      constexpr int kb = g >> 2, u = g & 3;
      if constexpr (u == 0) C.s[1][kb] = MFMA16(st.kf[g], st.q[1][0], st.negm[1]); else C.s[1][kb] = MFMA16(st.kf[g], st.q[1][u], C.s[1][kb]);
      PIN(C.s[1][kb]);
    } else if constexpr (!FIRST) {
      constexpr int m = g - 16;
      st.o[1][m & 7] = MFMA16(cat(st.vlo[m], st.vhi[m]), __builtin_bit_cast(bf16x8, st.pw[1][m >> 3]), st.o[1][m & 7]);
      PIN(st.o[1][m & 7]);
    }
    SBAR();
    if constexpr ((g & 1) == 0) {
      if constexpr (g <= 22) {
        if constexpr (!FIRST) { constexpr int n = 8 + g; PWV(n)[(n >> 1) & 3] = cvtpk(EL(P, n), EL(P, n + 1)); PIN(PWV(n)); }
      } else { constexpr int n = g - 24; PWV(n)[(n >> 1) & 3] = cvtpk(EL(C, n), EL(C, n + 1)); PIN(PWV(n)); }
    }
    if constexpr ((g & 1) && g >= 5 && g <= 19) {
      constexpr int kb = (g - 5) >> 2, hf = ((g - 5) >> 1) & 1;
      mb = MX3(mb, C.s[1][kb][2 * hf], C.s[1][kb][2 * hf + 1]); PIN(mb);
    }
    if constexpr (g == 22) {
      const bool ok = (ma <= THR) & (mb <= THR);
      if (FIRST || __builtin_expect(!__all(ok), 0)) {
#pragma unroll
        for (int qb = 0; qb < 2; ++qb) {
          float rm = qb ? mb : ma;
          rm = xmax4(rm);
          const float dl = FIRST ? rm : __builtin_fmaxf(rm, 0.f);
          st.mhat[qb] += dl;
#pragma unroll
          for (int kb = 0; kb < 4; ++kb)
#pragma unroll
            for (int r = 0; r < 4; ++r) C.s[qb][kb][r] -= dl;
#pragma unroll
          for (int r = 0; r < 4; ++r) st.negm[qb][r] = -st.mhat[qb];
          PIN(st.negm[qb]);
          if constexpr (!FIRST) { const float f_ = EX(-dl); if (qb == 0) fct0 = f_; else fct1 = f_; }
        }
        resc = true;
      }
    }
    if constexpr (g <= 21) {
      if constexpr (!FIRST) { constexpr int n = 10 + g; EL(P, n) = EX(EL(P, n)); PIN(ELV(P, n)); }
    } else { constexpr int n = g - 22; EL(C, n) = EX(EL(C, n)); PIN(ELV(C, n)); }
    SBAR();
    if constexpr (!FIRST && (g == 21 || g == 23 || g == 25 || g == 27)) {
      constexpr int qb = (g >> 1) & 1, w = g >= 24;
      st.lacc[qb] = MFMA16(st.ones, __builtin_bit_cast(bf16x8, st.pw[qb][w]), st.lacc[qb]); PIN(st.lacc[qb]);
      SBAR();
    }
  });
  if constexpr (!FIRST) {
    if (__builtin_expect(resc, 0)) {
#pragma unroll
      for (int d = 0; d < 8; ++d)
#pragma unroll
        for (int r = 0; r < 4; ++r) { st.o[0][d][r] *= fct0; st.o[1][d][r] *= fct1; }
#pragma unroll
      for (int r = 0; r < 4; ++r) { st.lacc[0][r] *= fct0; st.lacc[1][r] *= fct1; }
    }
  }
#undef KRD
#undef VRD
}

__global__ __launch_bounds__(512, 2)
void mha_k1(const char* __restrict__ x, const char* __restrict__ kc, const char* __restrict__ vc,
            const char* __restrict__ wq, const char* __restrict__ wo, char* ows, char* __restrict__ out) {
  extern __shared__ __attribute__((aligned(16))) char lds[];
  const int tid = threadIdx.x, lane = tid & 63;
  const int wid = __builtin_amdgcn_readfirstlane(tid >> 6);
  const int b = 4 * (blockIdx.x & 1) + ((blockIdx.x >> 3) & 3), h = 8 * ((blockIdx.x >> 1) & 3) + (blockIdx.x >> 5);
  const unsigned lds0 = (unsigned)(uintptr_t)lds;
  const lds_ptr L = (lds_ptr)lds;
  RSTAMP(62); STAMP(0);

#define KEEP(d) asm volatile("" :: "v"(d) : "memory")
  const __amdgpu_buffer_rsrc_t srdW = __builtin_amdgcn_make_buffer_rsrc((void*)(wq + h * 256), (short)0, DM * 8192 - h * 256, 0x00020000);
  const unsigned pl = ((lane >> 2) & 3), dbl = lane >> 4, c4 = lane & 3;
  const unsigned wv0 = pl * 8192 + dbl * 64 + c4 * 16, wv1 = wv0 + 31744;
#define DMAW2(c) DMA2(POLW, lds0 + WQO(c) + 2048 * wid, wv0, wv1, srdW, (unsigned)(((c) * 64 + 8 * wid) * 8192))
  const int ob = blockIdx.x & 7, oqt = blockIdx.x >> 6, ont = (blockIdx.x >> 3) & 7;
  const __amdgpu_buffer_rsrc_t srdWo = __builtin_amdgcn_make_buffer_rsrc((void*)(wo + ont * 128), (short)0, NH * DH * DM * 2 - ont * 128, 0x00020000);
  const unsigned ow0 = (4 * (lane >> 5) + ((lane >> 2) & 3)) * 1024 + ((lane >> 4) & 1) * 64 + (lane & 3) * 16;
  const unsigned ow1 = ow0 + 7168, ow2 = ow0 + 2 * 7168, ow3 = ow0 + 3 * 7168;
  unsigned long long* const epochp = (unsigned long long*)(ows + WS_EPOCH);
  unsigned long long* const flags = (unsigned long long*)(ows + WS_FLAGS);
  unsigned long long tag = 0;
  unsigned td = 0;
#define ROTM 0
#define ROTA 0
#define RR(i) (((i) + ROTM * ont + ROTA) & 15)
  constexpr int NA = P3NA;
  const int r32 = lane & 31, hi = lane >> 5, tq = (lane >> 2) & 3, tp = lane & 3, tblk = (lane >> 4) & 1;
  bf16x8 wf[NA][4];
#define RBP(i) ((lds_cptr)L + RB(i) + rbo)
#define WDMA(i) DMA4("", lds0 + (unsigned)(RB(i)) + 4096u * w3, ow0, ow1, ow2, ow3, srdWo, 32768u * w3 + (unsigned)(RR(i) * 256 * 1024))
#define W3() unsigned w3 = (unsigned)__builtin_amdgcn_readfirstlane(tid >> 6); asm volatile("s_nop 4" : "+s"(w3))
#define WREAD_NW(i) do { lds_cptr p_ = RBP(i); PIN(p_); _Pragma("unroll") for (int f_ = 0; f_ < 4; ++f_) \
      wf[(i) % NA][f_] = cat(ldstr(p_ + 1024 * (f_ >> 1) + 256 * (f_ & 1)), ldstr(p_ + 1024 * (f_ >> 1) + 2048 + 256 * (f_ & 1))); } while (0)
#define WRWAIT(i) asm volatile("s_waitcnt lgkmcnt(0)" : "+v"(wf[(i) % NA][0]), "+v"(wf[(i) % NA][1]), "+v"(wf[(i) % NA][2]), "+v"(wf[(i) % NA][3]) :: "memory")
#define WREAD(i) do { WREAD_NW(i); WRWAIT(i); } while (0)
#define RBO() (wid * 4096 + 512 * hi + 64 * tq + 32 * tblk + 8 * tp)
  if (wid == 7) {
    const __amdgpu_buffer_rsrc_t srdK = __builtin_amdgcn_make_buffer_rsrc((void*)(kc + (size_t)(b * NH + h) * (SK * DH * 2)), (short)0, SK * DH * 2, 0x00020000);
    const __amdgpu_buffer_rsrc_t srdV = __builtin_amdgcn_make_buffer_rsrc((void*)(vc + (size_t)(b * NH + h) * (SK * DH * 2)), (short)0, SK * DH * 2, 0x00020000);
    const unsigned wv2 = wv0 + 2 * 31744, wv3 = wv0 + 3 * 31744;
    const unsigned kvA = pl * 256 + dbl * 64 + c4 * 16, kvB = pl * 256 + dbl * 64 + ((c4 ^ 2) * 16);
#define DMAW(c) do { _Pragma("unroll") for (int i_ = 0; i_ < 4; ++i_) DMA4(POLW, lds0 + (unsigned)(WQO(c) + 4096 * i_), wv0, wv1, wv2, wv3, srdW, (unsigned)(((c) * 64 + 16 * i_) * 8192)); } while (0)
#define DMAK_(t, so) do { _Pragma("unroll") for (int i_ = 0; i_ < 4; ++i_) { WAITV(59); DMA4("nt", lds0 + (unsigned)((so) + 4096 * i_), kvA, kvA, kvB, kvB, srdK, (unsigned)((t) * 16384 + 4096 * i_)); } } while (0)
#define DMAV_(t, so) do { _Pragma("unroll") for (int i_ = 0; i_ < 4; ++i_) { WAITV(59); DMA4("nt", lds0 + (unsigned)((so) + 16384 + 4096 * i_), kvA, kvB, kvA, kvB, srdV, (unsigned)((t) * 16384 + 4096 * i_)); } } while (0)
#define DMAK(t) DMAK_(t, SLOT(t))
#define DMAV(t) DMAV_(t, SLOT((t) + 1))
    const char* tw = wo + (size_t)(blockIdx.x >> 3) * 8192 + lane * 128;
#define TW(r) do { const char* p_ = tw + (size_t)((((r) + ROTM * (lane & 7) + ROTA) & 15) * 262144); asm volatile("s_waitcnt vmcnt(62)\n\tglobal_load_dword %0, %1, off" : "+v"(td) : "v"(p_) : "memory"); } while (0)
    asm volatile("global_load_dwordx2 %0, %1, off sc1" : "=v"(tag) : "v"(epochp) : "memory");
#define RW(i) do { _Pragma("unroll") for (int w_ = 0; w_ < 8; ++w_) { WAITV(59); DMA4("", lds0 + (unsigned)(RB(i) + 4096 * w_), ow0, ow1, ow2, ow3, srdWo, (unsigned)((RR(i) * 256 + 32 * w_) * 1024)); } } while (0)
    DMAW2(0);
    BARRIER();
    DMAW(1);
    WAITV(16); BARRIER();
    DMAW(2);
    WAITV(16); BARRIER();
    DMAW(3);
    WAITV(16); BARRIER();
    DMAW(4);
    WAITV(16); BARRIER();
    DMAW(5);
    WAITV(16); BARRIER();
    DMAW(6);
    WAITV(16); BARRIER();
    DMAW(7);
    WAITV(16); BARRIER();
    DMAK(0); DMAK(1); DMAV(0);
    WAITV(48); BARRIER();
    BARRIER();
    DMAK(2);
    WAITV(32); BARRIER();
    DMAV(1); DMAK(3); DMAV(2);
    WAITV(48); BARRIER();
    DMAK(4); DMAV(3); DMAK(5); DMAV(4);
    WAITV(63); BARRIER();
    {
      int so = SLOT(1);
      for (int t = 1; t < 11; ++t) {
        DMAK_(t + 5, so); DMAV_(t + 4, so);
        so = (so == 131072) ? 0 : so + 32768;
        WAITV(63);
        BARRIER();
      }
    }
    DMAV(15); TW(0); TW(1); TW(2); TW(3);
    asm volatile("" : "+v"(tag));
    tag = (((unsigned long long)(unsigned)__builtin_amdgcn_readfirstlane((unsigned)(tag >> 32)) << 32) | (unsigned)__builtin_amdgcn_readfirstlane((unsigned)tag)) + 1;
    if (lane == 0) { *(__attribute__((address_space(3))) unsigned*)(L + PUBW) = 0u; *(__attribute__((address_space(3))) unsigned long long*)(L + PUBW + 8) = tag; }
    asm volatile("s_waitcnt lgkmcnt(0)" ::: "memory");
    WAITV(63);
    BARRIER();
    TW(5); TW(6); TW(7); TW(4);
    WAITV(40);
    RW(0);
    BARRIER();
    RW(1); TW(8); TW(10); TW(11); TW(12);
    WAITV(63);
    BARRIER();
    RW(2); TW(9); TW(13); TW(14); TW(15);
    WAITV(4);
    BARRIER();
    {
      const int rbo = RBO();
      WREAD(0); WREAD(1); WREAD(2);
    }
    BARRIER();
    RW(3); RW(5); RW(6); RW(7);
    WAITV(0);
#undef TW
#undef RW
#undef DMAW
#undef DMAK
#undef DMAV
  } else {
  const __amdgpu_buffer_rsrc_t srdX = __builtin_amdgcn_make_buffer_rsrc((void*)(x + (size_t)(b * SQ + wid * 32) * (DM * 2)), (short)0, 32 * DM * 2, 0x00020000);
  const unsigned xs = (lane >> 3) * 1024 + (((lane & 7) ^ (lane >> 4)) * 16);
  const unsigned xv0 = xs, xv1 = (xs ^ 64) + 7168, xv2 = xs + 2 * 7168, xv3 = (xs ^ 64) + 3 * 7168;
#define XDMA(c) DMA4(POLW, lds0 + (unsigned)XO(c) + 4096u * wid, xv0, xv1, xv2, xv3, srdX, (unsigned)(128 * (c)))
  const unsigned xrb = (unsigned)(wid * 4096 + r32 * 128 + ((hi ^ ((r32 >> 1) & 7)) * 16));
  bf16x8 xf[2][4];
#define XRD(c, k_) xf[(c) & 1][k_] = ldsrd128((lds_cptr)L + ((xrb ^ (unsigned)((k_) << 5)) + XO(c)))
  DMAW2(0); XDMA(0);
  BARRIER();
  XDMA(1);
  const lds_cptr wrb = (lds_cptr)L + 2048 * hi + 64 * tq + 32 * (tp & 1) + 16 * tblk + 8 * (tp >> 1);
  lds_cptr wrb2 = wrb + 131072; PIN(wrb2);
#define WRB(c) ((WQO(c) < 65536 ? wrb : wrb2) + (WQO(c) & 65535))
#define WRD(c, m) do { wlo[m] = ldstr(WRB(c) + 4096 * ((m) >> 2) + 256 * ((m) & 3)); whi[m] = ldstr(WRB(c) + 4096 * ((m) >> 2) + 1024 + 256 * ((m) & 3)); } while (0)
  AttnState st;
  f32x16 acc[4];
#pragma unroll
  for (int i = 0; i < 4; ++i) acc[i] = f32x16{};
  s16x4 wlo[16], whi[16];
  WAITV(4);
  XRD(0, 0); XRD(0, 1); XRD(0, 2); XRD(0, 3);
  STAMP(1);
  BARRIER();
  STAMP(2);
#pragma unroll
  for (int m = 0; m < PD; ++m) WRD(0, m);
  u32x4 qx[4], qy[4];
#define QCV1(nb, e_) do { \
      const unsigned lo_ = cvtpk(acc[nb][2 * (e_)] * QSCALE, acc[nb][2 * (e_) + 1] * QSCALE), hi_ = cvtpk(acc[nb][8 + 2 * (e_)] * QSCALE, acc[nb][9 + 2 * (e_)] * QSCALE); \
      auto r_ = __builtin_amdgcn_permlane16_swap(lo_, hi_, false, false); qx[nb][e_] = r_[0]; qy[nb][e_] = r_[1]; PIN(qx[nb]); PIN(qy[nb]); } while (0)
  static_for<128>([&](auto I) __attribute__((always_inline)) {
    constexpr int c = I.value >> 4, mm = I.value & 15;
    constexpr int m = (c == 7) ? (4 * (mm & 3) + (mm >> 2)) : mm;
    acc[m & 3] = MFMA(cat(wlo[m], whi[m]), xf[c & 1][m >> 2], acc[m & 3]); PIN(acc[m & 3]);
    if constexpr (c == 0 && mm + PD == 16) { BARRIER(); }
    if constexpr (mm + PD < 16) { constexpr int f = mm + PD, ff = (c == 7) ? (4 * (f & 3) + (f >> 2)) : f; WRD(c, ff); }
    else if constexpr (c < 7) { constexpr int f = mm + PD - 16, ff = (c + 1 == 7) ? (4 * (f & 3) + (f >> 2)) : f; WRD(c + 1, ff); }
    if constexpr (c == 0 && mm == 0) { XDMA(2); }
    if constexpr (c == 0 && mm == 2) { XDMA(3); }
    if constexpr (c >= 1 && c <= 4 && mm == 0) { XDMA(c + 3); }
    if constexpr (c < 7) {
      if constexpr (mm == 4) { WAITV(c <= 4 ? 8 : 4 * (6 - c)); }
      if constexpr (mm >= 4 && mm < 8) { XRD(c + 1, mm - 4); }
    }
    if constexpr (c == 5) { st.o[mm >> 3][mm & 7] = f32x4{}; PIN(st.o[mm >> 3][mm & 7]); }
    if constexpr (c == 7 && mm >= 5) { QCV1((mm - 5) >> 2, (mm - 5) & 3); }
    SBAR();
    if constexpr (mm == 15) { BARRIER(); STAMP(3 + c); SBAR(); }
  });
  LSTAMP(58);
#undef XDMA
#undef XRD
#undef WRD
  const int l16 = lane & 15, g4 = lane >> 4;
  const lds_cptr kb = (lds_cptr)L + 1024 * (l16 >> 2) + 64 * (l16 & 3) + 16 * (g4 ^ ((l16 >> 2) & 2));
  const lds_cptr vb0 = (lds_cptr)L + 16384 + 1024 * g4 + 64 * tq + 32 * (g4 & 1) + 8 * tp;
  const lds_cptr vb1 = (lds_cptr)L + 16384 + 1024 * g4 + 64 * tq + 32 * (1 - (g4 & 1)) + 8 * tp;
#pragma unroll
  for (int m = 0; m < PD; ++m) st.kf[m] = ldsrd128(kb + SLOT(0) + 4096 * (m >> 2) + 256 * (m & 3));
  QCV1(2, 3); QCV1(3, 0); QCV1(3, 1); QCV1(3, 2); QCV1(3, 3);
#undef QCV1
#pragma unroll
  for (int nb = 0; nb < 4; ++nb) { st.q[0][nb] = __builtin_bit_cast(bf16x8, qx[nb]); st.q[1][nb] = __builtin_bit_cast(bf16x8, qy[nb]); }
  st.negm[0] = f32x4{}; st.negm[1] = f32x4{}; PIN(st.negm[0]); PIN(st.negm[1]);
  st.mhat[0] = st.mhat[1] = 0.f; st.lacc[0] = f32x4{}; st.lacc[1] = f32x4{};
  st.ones = bf16x8{0x3F80, 0x3F80, 0x3F80, 0x3F80, 0x3F80, 0x3F80, 0x3F80, 0x3F80}; PIN(st.ones);

  STile A, B;
  attn_step<true>(st, A, B, kb + SLOT(0), vb0 + SLOT(0), vb1 + SLOT(0), kb + SLOT(1), wid >= 4);
  STAMP(11);
  BARRIER();
  STAMP(12);
  int s0 = SLOT(1);
  for (int t = 1; t < 15; t += 2) {
    const int s1 = (s0 == 131072) ? 0 : s0 + 32768, s2 = (s1 == 131072) ? 0 : s1 + 32768;
    attn_step<false>(st, B, A, kb + s0, vb0 + s0, vb1 + s0, kb + s1, wid >= 4);
    STAMP(11 + 2 * t);
    BARRIER();
    STAMP(12 + 2 * t);
    attn_step<false>(st, A, B, kb + s1, vb0 + s1, vb1 + s1, kb + s2, wid >= 4);
    STAMP(13 + 2 * t);
    BARRIER();
    STAMP(14 + 2 * t);
    s0 = s2;
  }
  attn_step<false>(st, B, A, kb + SLOT(15), vb0 + SLOT(15), vb1 + SLOT(15), kb + SLOT(15), wid >= 4);
  STAMP(41);
  BARRIER();
  STAMP(42); LSTAMP(59);
  {
    lds_cptr vd0 = vb0 + SLOT(16), vd1 = vb1 + SLOT(16); PIN(vd0); PIN(vd1);
#define VRD(m) do { st.vlo[m] = ldstr((((m) & 2) ? vd1 : vd0) + 8192 * ((m) & 1) + 256 * ((m) >> 2)); st.vhi[m] = ldstr((((m) & 2) ? vd1 : vd0) + 8192 * ((m) & 1) + 4096 + 256 * ((m) >> 2)); } while (0)
    const int rbo = RBO();
    WREAD(0); WREAD(1); WREAD(2);
    BARRIER();
#pragma unroll
    for (int m = 0; m < 8; ++m) VRD(m);
    static_for<22>([&](auto I) __attribute__((always_inline)) { constexpr int n = 10 + I.value; EL(B, n) = EX(EL(B, n)); });
    static_for<12>([&](auto I) __attribute__((always_inline)) { constexpr int n = 8 + 2 * I.value; PWV(n)[(n >> 1) & 3] = cvtpk(EL(B, n), EL(B, n + 1)); });
#pragma unroll
    for (int w = 0; w < 2; ++w) {
      st.lacc[0] = MFMA16(st.ones, __builtin_bit_cast(bf16x8, st.pw[0][w]), st.lacc[0]);
      st.lacc[1] = MFMA16(st.ones, __builtin_bit_cast(bf16x8, st.pw[1][w]), st.lacc[1]);
    }
    const float inv0 = 1.0f / st.lacc[0][0], inv1 = 1.0f / st.lacc[1][0];
    const __amdgpu_buffer_rsrc_t srdOs = __builtin_amdgcn_make_buffer_rsrc((void*)(ows + (size_t)(b * NH + h) * 57344), (short)0, 57344, 0x00020000);
    const int qr0 = 32 * wid + l16, vk0 = qr0 / 28, qr1 = qr0 + 16, vk1 = qr1 / 28;
    const unsigned osv0 = vk0 * 7168 + (g4 >> 1) * 896 + ((g4 & 1) * 28 + qr0 - 28 * vk0) * 16, osv1 = vk1 * 7168 + (g4 >> 1) * 896 + ((g4 & 1) * 28 + qr1 - 28 * vk1) * 16;
#define OSTORE(u) do { \
      u32x4 w0_ = {cvtpk(st.o[0][2 * (u)][0] * inv0, st.o[0][2 * (u)][1] * inv0), cvtpk(st.o[0][2 * (u)][2] * inv0, st.o[0][2 * (u)][3] * inv0), \
                   cvtpk(st.o[0][2 * (u) + 1][0] * inv0, st.o[0][2 * (u) + 1][1] * inv0), cvtpk(st.o[0][2 * (u) + 1][2] * inv0, st.o[0][2 * (u) + 1][3] * inv0)}; \
      __builtin_amdgcn_raw_buffer_store_b128(w0_, srdOs, osv0 + (u) * 1792, 0, 16); \
      u32x4 w1_ = {cvtpk(st.o[1][2 * (u)][0] * inv1, st.o[1][2 * (u)][1] * inv1), cvtpk(st.o[1][2 * (u)][2] * inv1, st.o[1][2 * (u)][3] * inv1), \
                   cvtpk(st.o[1][2 * (u) + 1][0] * inv1, st.o[1][2 * (u) + 1][1] * inv1), cvtpk(st.o[1][2 * (u) + 1][2] * inv1, st.o[1][2 * (u) + 1][3] * inv1)}; \
      __builtin_amdgcn_raw_buffer_store_b128(w1_, srdOs, osv1 + (u) * 1792, 0, 16); } while (0)
    SBAR();
#pragma unroll
    for (int u = 0; u < 4; ++u) {
#pragma unroll
      for (int k = 0; k < 4; ++k) {
        const int m = 4 * u + k, db = m >> 1, w = m & 1;
        st.o[0][db] = MFMA16(cat(st.vlo[m], st.vhi[m]), __builtin_bit_cast(bf16x8, st.pw[0][w]), st.o[0][db]);
        st.o[1][db] = MFMA16(cat(st.vlo[m], st.vhi[m]), __builtin_bit_cast(bf16x8, st.pw[1][w]), st.o[1][db]);
        if (u < 2) { VRD(m + 8); }
      }
      SBAR();
      if (u >= 1) { OSTORE(u - 1); }
      SBAR();
    }
    STAMP(43);
    OSTORE(3);
    STAMP(44);
#undef VRD
#undef OSTORE
    WAITV(0);
    if (lane == 0) {
      const unsigned old = __hip_atomic_fetch_add((__attribute__((address_space(3))) unsigned*)(L + PUBW), 1u, __ATOMIC_RELAXED, __HIP_MEMORY_SCOPE_WORKGROUP);
      if (old == 6) __hip_atomic_store(flags + b * NH + h, *(__attribute__((address_space(3))) unsigned long long*)(L + PUBW + 8), __ATOMIC_RELAXED, __HIP_MEMORY_SCOPE_AGENT);
    }
    asm volatile("s_waitcnt lgkmcnt(0)" ::: "memory");
    STAMP(50);
  }
  }

  {
    static_assert(NA == 8, "prefetch sequences above are written for NA = 8");
    const int rbo = RBO(); W3();
    const __amdgpu_buffer_rsrc_t srdO = __builtin_amdgcn_make_buffer_rsrc((void*)(ows + (size_t)ob * (NH * 57344)), (short)0, NH * 57344, 0x00020000);
    const unsigned ov0 = (hi * 28 + (r32 < 28 ? r32 : 27)) * 16;
    const unsigned osb = (w3 >> 2) * 57344 + (w3 & 3) * 1792 + oqt * 2 * 7168;
    u32x4 of[3][4];
    f32x16 acc[2][2];
#pragma unroll
    for (int i = 0; i < 2; ++i) { acc[i][0] = f32x16{}; acc[i][1] = f32x16{}; }
#define OLOAD(i) do { const unsigned so_ = osb + (unsigned)(RR(i) * 2 * 57344), so1_ = so_ + 7168u; \
      BLOAD("sc1", of[(i) % 3][0], ov0, srdO, so_, 0); BLOAD("sc1", of[(i) % 3][1], ov0, srdO, so1_, 0); \
      BLOAD("sc1", of[(i) % 3][2], ov0, srdO, so_, 896); BLOAD("sc1", of[(i) % 3][3], ov0, srdO, so1_, 896); } while (0)
    BARRIER();
    LSTAMP(57);
    STAMP(51);
    WDMA(4);
    WREAD(3); WDMA(8); WREAD(5); WDMA(10); WREAD(6); WDMA(11); WREAD(7); WDMA(12);
    WAITV(16); WREAD(4); WDMA(9);
    STAMP(52);
    if (w3 == 7) {
      if (blockIdx.x != 0) {
        const unsigned long long* fp = flags + ob * NH + r32;
        for (unsigned spins = 0; spins < (1u << 18); ++spins) {
          const unsigned long long v = __hip_atomic_load(fp, __ATOMIC_RELAXED, __HIP_MEMORY_SCOPE_AGENT);
          if (__all(v == tag)) break;
          __builtin_amdgcn_s_sleep(1);
        }
      } else {
        for (unsigned spins = 0; spins < (1u << 18); ++spins) {
          bool ok = true;
#pragma unroll
          for (int k = 0; k < 4; ++k) ok &= __hip_atomic_load(flags + 64 * k + lane, __ATOMIC_RELAXED, __HIP_MEMORY_SCOPE_AGENT) == tag;
          if (__all(ok)) break;
          __builtin_amdgcn_s_sleep(1);
        }
        if (lane == 0) __hip_atomic_store(epochp, tag, __ATOMIC_RELAXED, __HIP_MEMORY_SCOPE_AGENT);
      }
      KEEP(td);
    }
    STAMP(53);
    BARRIER();
    LSTAMP(60);
    STAMP(54);
    OLOAD(0); OLOAD(1);
#pragma unroll
    for (int i = 0; i < 16; ++i) {
      const int cnt = (i < 15 ? 4 : 0) + ((i - 1 >= 1 && i - 1 <= 3) ? 4 : 0);
      WAITV4(cnt, of[i % 3][0], of[i % 3][1], of[i % 3][2], of[i % 3][3]);
      if (i >= 1 && i <= 3) { WRWAIT(i - 1 + NA); WDMA(i + 12); }
      if (i + 2 < 16) { OLOAD(i + 2); }
      SBAR();
#pragma unroll
      for (int ks = 0; ks < 2; ++ks)
#pragma unroll
        for (int nb = 0; nb < 2; ++nb)
#pragma unroll
          for (int vb = 0; vb < 2; ++vb)
            acc[nb][vb] = MFMA(wf[i % NA][2 * ks + nb], __builtin_bit_cast(bf16x8, of[i % 3][2 * ks + vb]), acc[nb][vb]);
      SBAR();
      if (i + NA < 16) { WREAD_NW(i + NA); }
      SBAR();
    }
    STAMP(55);
#undef WDMA
#undef WREAD
#undef OLOAD
#undef RBP
#pragma unroll
    for (int vb = 0; vb < 2; ++vb)
#pragma unroll
      for (int nb = 0; nb < 2; ++nb)
#pragma unroll
        for (int rg = 0; rg < 4; ++rg) {
          const int q = 32 * vb + r32, slot = 8 * nb + 2 * rg + hi;
          f32x4 v = {acc[nb][vb][4 * rg], acc[nb][vb][4 * rg + 1], acc[nb][vb][4 * rg + 2], acc[nb][vb][4 * rg + 3]};
          *(__attribute__((address_space(3))) f32x4*)(L + (q >> 4) * 32768 + wid * 4096 + (q & 15) * 256 + ((slot ^ (q & 15)) * 16)) = v;
        }
    asm volatile("s_waitcnt lgkmcnt(0)" ::: "memory");
    BARRIER();
    STAMP(56); LSTAMP(61);
    const int q = tid >> 3, pc = tid & 7;
    f32x4 s0 = {0, 0, 0, 0}, s1 = {0, 0, 0, 0};
#pragma unroll
    for (int w = 0; w < 8; ++w) {
      s0 += *(const __attribute__((address_space(3))) f32x4*)(L + (q >> 4) * 32768 + w * 4096 + (q & 15) * 256 + (((2 * pc) ^ (q & 15)) * 16));
      s1 += *(const __attribute__((address_space(3))) f32x4*)(L + (q >> 4) * 32768 + w * 4096 + (q & 15) * 256 + (((2 * pc + 1) ^ (q & 15)) * 16));
    }
    u32x4 w = {cvtpk(s0[0], s0[1]), cvtpk(s0[2], s0[3]), cvtpk(s1[0], s1[1]), cvtpk(s1[2], s1[3])};
    if ((q & 31) < 28)
      *(u32x4*)(out + (size_t)(ob * SQ + 56 * oqt + 28 * (q >> 5) + (q & 31)) * (DM * 2) + ont * 128 + pc * 16) = w;
  }
  STAMP(40); RSTAMP(63);
  STFLUSH(ows + (64 << 20));
}

extern "C" void kernel_launch(void* const* d_in, const int* in_sizes, int n_in,
                              void* d_out, int out_size, void* d_ws, size_t ws_size,
                              hipStream_t stream) {
  (void)in_sizes; (void)n_in; (void)out_size; (void)ws_size;
  (void)hipFuncSetAttribute((const void*)mha_k1, hipFuncAttributeMaxDynamicSharedMemorySize, K1_LDS);
  mha_k1<<<256, 512, K1_LDS, stream>>>((const char*)d_in[0], (const char*)d_in[1], (const char*)d_in[2], (const char*)d_in[3], (const char*)d_in[4], (char*)d_ws, (char*)d_out);
}
```

```cpp
#include <hip/hip_runtime.h>
#include <hip/hip_bf16.h>
#include <stdint.h>
#include <utility>
#include <type_traits>

typedef short    bf16x8 __attribute__((ext_vector_type(8)));
typedef short    s16x4  __attribute__((ext_vector_type(4)));
typedef float    f32x16 __attribute__((ext_vector_type(16)));
typedef float    f32x4  __attribute__((ext_vector_type(4)));
typedef float    f32x2  __attribute__((ext_vector_type(2)));
typedef __bf16   bf16x2 __attribute__((ext_vector_type(2)));
typedef unsigned u32x4  __attribute__((ext_vector_type(4)));
typedef __attribute__((address_space(3))) char* lds_ptr;
typedef __attribute__((address_space(3))) const char* lds_cptr;

#define DI __device__ __forceinline__
#define SBAR() __builtin_amdgcn_sched_barrier(0)
#define PIN(x) asm volatile("" : "+v"(x))
#define MFMA(a, b, c) __builtin_amdgcn_mfma_f32_32x32x16_bf16(a, b, c, 0, 0, 0)
#define BARRIER() asm volatile("s_barrier" ::: "memory")

#define STAMP(i)
#define RSTAMP(i)
#define LSTAMP(i)
#define STFLUSH(dbg)
#define POLT "nt"
#define POLW ""
#define POLWO ""
#define POLT2 ""
#define POLO "sc1"
#define P3NA 8
#define TSCHED 3
#define PRIO_TOGGLE 21
#define EARLYKV 0
#define MAPK1 1
constexpr int NB = 8, SQ = 224, DM = 512, NH = 32, SK = 1024, DH = 128;
constexpr float QSCALE = 0.088388347648318440f * 1.4426950408889634f;
constexpr float THR = 8.0f;

template <int... Is, class F> DI void static_for_(std::integer_sequence<int, Is...>, F&& f) { (f(std::integral_constant<int, Is>{}), ...); }
template <int N, class F> DI void static_for(F&& f) { static_for_(std::make_integer_sequence<int, N>{}, f); }
DI unsigned cvtpk(float lo, float hi) { f32x2 v = {lo, hi}; bf16x2 b = __builtin_convertvector(v, bf16x2); return __builtin_bit_cast(unsigned, b); }
DI bf16x8 ldsrd128(lds_cptr p) { return *(const __attribute__((address_space(3))) bf16x8*)p; }
DI s16x4 ldstr(lds_cptr p) { return __builtin_bit_cast(s16x4, __builtin_amdgcn_ds_read_tr16_b64_v4i16((__attribute__((address_space(3))) s16x4*)p)); }
DI bf16x8 cat(s16x4 a, s16x4 b) { return __builtin_shufflevector(a, b, 0, 1, 2, 3, 4, 5, 6, 7); }
DI void ldswr128(lds_ptr p, u32x4 v) { *(__attribute__((address_space(3))) u32x4*)p = v; }
#define MX3(a, b, c) __builtin_fmaxf(__builtin_fmaxf((a), (b)), (c))

#define DMA4(POL, m0v, v0, v1, v2, v3, srd, soff)                                                     \
  asm volatile("s_mov_b32 m0, %0\n\ts_nop 0\n\t"                                                      \
               "buffer_load_dwordx4 %1, %5, %6 offen " POL " lds\n\t"                                 \
               "buffer_load_dwordx4 %2, %5, %6 offen offset:1024 " POL " lds\n\t"                     \
               "buffer_load_dwordx4 %3, %5, %6 offen offset:2048 " POL " lds\n\t"                     \
               "buffer_load_dwordx4 %4, %5, %6 offen offset:3072 " POL " lds"                         \
               :: "s"(m0v), "v"(v0), "v"(v1), "v"(v2), "v"(v3), "s"(srd), "s"(soff) : "memory")
#define DMA2(POL, m0v, v0, v1, srd, soff)                                                             \
  asm volatile("s_mov_b32 m0, %0\n\ts_nop 0\n\t"                                                      \
               "buffer_load_dwordx4 %1, %3, %4 offen " POL " lds\n\t"                                 \
               "buffer_load_dwordx4 %2, %3, %4 offen offset:1024 " POL " lds"                         \
               :: "s"(m0v), "v"(v0), "v"(v1), "s"(srd), "s"(soff) : "memory")
#define BLOAD(POL, dst, voff, srd, soff, imm)                                                         \
  asm volatile("buffer_load_dwordx4 %0, %1, %2, %3 offen offset:%4 " POL                              \
               : "=v"(dst) : "v"(voff), "s"(srd), "s"(soff), "i"(imm) : "memory")
#define WAITV(n) asm volatile("s_waitcnt vmcnt(%0)" :: "i"(n) : "memory")
#define WAITV4(n, a, b, c, d) asm volatile("s_waitcnt vmcnt(%4)" : "+v"(a), "+v"(b), "+v"(c), "+v"(d) : "i"(n) : "memory")

constexpr int K1_LDS = 163840;
constexpr size_t WS_FLAGS = (size_t)32 << 20, WS_EPOCH = WS_FLAGS + 4096;
DI constexpr int SLOT(int t) { return (t % 5) * 32768; }
DI constexpr int RB(int i) { return ((i + 2) % 5) * 32768; }
constexpr int PUBW = 32768;
DI constexpr int WQO(int c) { return (c & 3) == 0 ? 0 : (c & 3) == 1 ? 16384 : (c & 3) == 2 ? 131072 : 147456; }
DI constexpr int XO(int c) { return (c % 3) == 0 ? 65536 : (c % 3) == 1 ? 98304 : 32768; }

#define MFMA16(a, b, c) __builtin_amdgcn_mfma_f32_16x16x32_bf16(a, b, c, 0, 0, 0)
struct AttnState {
  f32x4 o[2][8];
  bf16x8 q[2][4];
  f32x4 negm[2];
  float mhat[2];
  f32x4 lacc[2];
  bf16x8 ones;
  bf16x8 kf[16];
  s16x4 vlo[16], vhi[16];
  u32x4 pw[2][2];
};
struct STile { f32x4 s[2][4]; };
DI float xmax4(float v) {
  auto a = __builtin_amdgcn_permlane16_swap(__float_as_uint(v), __float_as_uint(v), false, false);
  v = __builtin_fmaxf(__uint_as_float(a[0]), __uint_as_float(a[1]));
  auto c = __builtin_amdgcn_permlane32_swap(__float_as_uint(v), __float_as_uint(v), false, false);
  return __builtin_fmaxf(__uint_as_float(c[0]), __uint_as_float(c[1]));
}
DI float xsum4(float v) {
  auto a = __builtin_amdgcn_permlane16_swap(__float_as_uint(v), __float_as_uint(v), false, false);
  v = __uint_as_float(a[0]) + __uint_as_float(a[1]);
  auto c = __builtin_amdgcn_permlane32_swap(__float_as_uint(v), __float_as_uint(v), false, false);
  return __uint_as_float(c[0]) + __uint_as_float(c[1]);
}

#define PDV 5
constexpr int PD = PDV;
#define EL(T, n) (T).s[((n) >> 3) & 1][2 * ((n) >> 4) + (((n) >> 2) & 1)][(n) & 3]
#define ELV(T, n) (T).s[((n) >> 3) & 1][2 * ((n) >> 4) + (((n) >> 2) & 1)]
#define PWV(n) st.pw[((n) >> 3) & 1][(n) >> 4]
#define EX(v) __builtin_amdgcn_exp2f(v)
template <bool FIRST>
DI void attn_step(AttnState& st, STile& C, STile& P, lds_cptr kp, lds_cptr vp0, lds_cptr vp1, lds_cptr kn, const bool young) {
#define KRD(m, base) st.kf[m] = ldsrd128((base) + 4096 * ((m) >> 2) + 256 * ((m) & 3))
#define VRD(m) do { st.vlo[m] = ldstr((((m) & 1) ? vp1 : vp0) + 8192 * ((m) >> 3) + 256 * (((m) & 7) >> 1)); \
                    st.vhi[m] = ldstr((((m) & 1) ? vp1 : vp0) + 8192 * ((m) >> 3) + 4096 + 256 * (((m) & 7) >> 1)); } while (0)
  float ma = -3.0e38f, mb = -3.0e38f, fct0 = 1.f, fct1 = 1.f;
  bool resc = false;
  SBAR();
  static_for<32>([&](auto I) __attribute__((always_inline)) {
    constexpr int g = I.value;
    if constexpr (g == 0) { if (young) __builtin_amdgcn_s_setprio(1); }
    if constexpr (g == PRIO_TOGGLE) { __builtin_amdgcn_s_setprio(0); }
    if constexpr (g < 16) {
      constexpr int kb = g >> 2, u = g & 3;
      if constexpr (u == 0) C.s[0][kb] = MFMA16(st.kf[g], st.q[0][0], st.negm[0]); else C.s[0][kb] = MFMA16(st.kf[g], st.q[0][u], C.s[0][kb]);
      PIN(C.s[0][kb]);
    } else if constexpr (!FIRST) {
      constexpr int m = g - 16;
      st.o[0][m & 7] = MFMA16(cat(st.vlo[m], st.vhi[m]), __builtin_bit_cast(bf16x8, st.pw[0][m >> 3]), st.o[0][m & 7]);
      PIN(st.o[0][m & 7]);
    }
    SBAR();
    {
      constexpr int f = g + PD;
      if constexpr (f < 16) { KRD(f, kp); }
      else if constexpr (f < 32) { if constexpr (!FIRST) { VRD(f - 16); } }
      else { KRD(f - 32, kn); }
    }
    if constexpr ((g & 1) && g >= 5 && g <= 19) {
      constexpr int kb = (g - 5) >> 2, hf = ((g - 5) >> 1) & 1;
      ma = MX3(ma, C.s[0][kb][2 * hf], C.s[0][kb][2 * hf + 1]); PIN(ma);
    }
    SBAR();
    if constexpr (g < 16) {
      constexpr int kb = g >> 2, u = g & 3;
      if constexpr (u == 0) C.s[1][kb] = MFMA16(st.kf[g], st.q[1][0], st.negm[1]); else C.s[1][kb] = MFMA16(st.kf[g], st.q[1][u], C.s[1][kb]);
      PIN(C.s[1][kb]);
    } else if constexpr (!FIRST) {
      constexpr int m = g - 16;
      st.o[1][m & 7] = MFMA16(cat(st.vlo[m], st.vhi[m]), __builtin_bit_cast(bf16x8, st.pw[1][m >> 3]), st.o[1][m & 7]);
      PIN(st.o[1][m & 7]);
    }
    SBAR();
    if constexpr ((g & 1) == 0) {
      if constexpr (g <= 22) {
        if constexpr (!FIRST) { constexpr int n = 8 + g; PWV(n)[(n >> 1) & 3] = cvtpk(EL(P, n), EL(P, n + 1)); PIN(PWV(n)); }
      } else { constexpr int n = g - 24; PWV(n)[(n >> 1) & 3] = cvtpk(EL(C, n), EL(C, n + 1)); PIN(PWV(n)); }
    }
    if constexpr ((g & 1) && g >= 5 && g <= 19) {
      constexpr int kb = (g - 5) >> 2, hf = ((g - 5) >> 1) & 1;
      mb = MX3(mb, C.s[1][kb][2 * hf], C.s[1][kb][2 * hf + 1]); PIN(mb);
    }
    if constexpr (g == 22) {
      const bool ok = (ma <= THR) & (mb <= THR);
      if (FIRST || __builtin_expect(!__all(ok), 0)) {
#pragma unroll
        for (int qb = 0; qb < 2; ++qb) {
          float rm = qb ? mb : ma;
          rm = xmax4(rm);
          const float dl = FIRST ? rm : __builtin_fmaxf(rm, 0.f);
          st.mhat[qb] += dl;
#pragma unroll
          for (int kb = 0; kb < 4; ++kb)
#pragma unroll
            for (int r = 0; r < 4; ++r) C.s[qb][kb][r] -= dl;
#pragma unroll
          for (int r = 0; r < 4; ++r) st.negm[qb][r] = -st.mhat[qb];
          PIN(st.negm[qb]);
          if constexpr (!FIRST) { const float f_ = EX(-dl); if (qb == 0) fct0 = f_; else fct1 = f_; }
        }
        resc = true;
      }
    }
    if constexpr (g <= 21) {
      if constexpr (!FIRST) { constexpr int n = 10 + g; EL(P, n) = EX(EL(P, n)); PIN(ELV(P, n)); }
    } else { constexpr int n = g - 22; EL(C, n) = EX(EL(C, n)); PIN(ELV(C, n)); }
    SBAR();
    if constexpr (!FIRST && (g == 21 || g == 23 || g == 25 || g == 27)) {
      constexpr int qb = (g >> 1) & 1, w = g >= 24;
      st.lacc[qb] = MFMA16(st.ones, __builtin_bit_cast(bf16x8, st.pw[qb][w]), st.lacc[qb]); PIN(st.lacc[qb]);
      SBAR();
    }
  });
  if constexpr (!FIRST) {
    if (__builtin_expect(resc, 0)) {
#pragma unroll
      for (int d = 0; d < 8; ++d)
#pragma unroll
        for (int r = 0; r < 4; ++r) { st.o[0][d][r] *= fct0; st.o[1][d][r] *= fct1; }
#pragma unroll
      for (int r = 0; r < 4; ++r) { st.lacc[0][r] *= fct0; st.lacc[1][r] *= fct1; }
    }
  }
#undef KRD
#undef VRD
}

__global__ __launch_bounds__(512, 2)
void mha_k1(const char* __restrict__ x, const char* __restrict__ kc, const char* __restrict__ vc,
            const char* __restrict__ wq, const char* __restrict__ wo, char* ows, char* __restrict__ out) {
  extern __shared__ __attribute__((aligned(16))) char lds[];
  const int tid = threadIdx.x, lane = tid & 63;
  const int wid = __builtin_amdgcn_readfirstlane(tid >> 6);
  const int b = 4 * (blockIdx.x & 1) + ((blockIdx.x >> 3) & 3), h = 8 * ((blockIdx.x >> 1) & 3) + (blockIdx.x >> 5);
  const unsigned lds0 = (unsigned)(uintptr_t)lds;
  const lds_ptr L = (lds_ptr)lds;
  RSTAMP(62); STAMP(0);

#define KEEP(d) asm volatile("" :: "v"(d) : "memory")
  const __amdgpu_buffer_rsrc_t srdW = __builtin_amdgcn_make_buffer_rsrc((void*)(wq + h * 256), (short)0, DM * 8192 - h * 256, 0x00020000);
  const unsigned pl = ((lane >> 2) & 3), dbl = lane >> 4, c4 = lane & 3;
  const unsigned wv0 = pl * 8192 + dbl * 64 + c4 * 16, wv1 = wv0 + 31744;
#define DMAW2(c) DMA2(POLW, lds0 + WQO(c) + 2048 * wid, wv0, wv1, srdW, (unsigned)(((c) * 64 + 8 * wid) * 8192))
  const int ob = blockIdx.x & 7, oqt = blockIdx.x >> 6, ont = (blockIdx.x >> 3) & 7;
  const __amdgpu_buffer_rsrc_t srdWo = __builtin_amdgcn_make_buffer_rsrc((void*)(wo + ont * 128), (short)0, NH * DH * DM * 2 - ont * 128, 0x00020000);
  const unsigned ow0 = (4 * (lane >> 5) + ((lane >> 2) & 3)) * 1024 + ((lane >> 4) & 1) * 64 + (lane & 3) * 16;
  const unsigned ow1 = ow0 + 7168, ow2 = ow0 + 2 * 7168, ow3 = ow0 + 3 * 7168;
  unsigned long long* const epochp = (unsigned long long*)(ows + WS_EPOCH);
  unsigned long long* const flags = (unsigned long long*)(ows + WS_FLAGS);
  unsigned long long tag = 0;
  unsigned td = 0;
#define ROTM 0
#define ROTA 0
#define RR(i) (((i) + ROTM * ont + ROTA) & 15)
  constexpr int NA = P3NA;
  const int r32 = lane & 31, hi = lane >> 5, tq = (lane >> 2) & 3, tp = lane & 3, tblk = (lane >> 4) & 1;
  bf16x8 wf[NA][4];
#define RBP(i) ((lds_cptr)L + RB(i) + rbo)
#define WDMA(i) DMA4("", lds0 + (unsigned)(RB(i)) + 4096u * w3, ow0, ow1, ow2, ow3, srdWo, 32768u * w3 + (unsigned)(RR(i) * 256 * 1024))
#define W3() unsigned w3 = (unsigned)__builtin_amdgcn_readfirstlane(tid >> 6); asm volatile("s_nop 4" : "+s"(w3))
#define WREAD_NW(i) do { lds_cptr p_ = RBP(i); PIN(p_); _Pragma("unroll") for (int f_ = 0; f_ < 4; ++f_) \
      wf[(i) % NA][f_] = cat(ldstr(p_ + 1024 * (f_ >> 1) + 256 * (f_ & 1)), ldstr(p_ + 1024 * (f_ >> 1) + 2048 + 256 * (f_ & 1))); } while (0)
#define WRWAIT(i) asm volatile("s_waitcnt lgkmcnt(0)" : "+v"(wf[(i) % NA][0]), "+v"(wf[(i) % NA][1]), "+v"(wf[(i) % NA][2]), "+v"(wf[(i) % NA][3]) :: "memory")
#define WREAD(i) do { WREAD_NW(i); WRWAIT(i); } while (0)
#define RBO() (wid * 4096 + 512 * hi + 64 * tq + 32 * tblk + 8 * tp)
  if (wid == 7) {
    const __amdgpu_buffer_rsrc_t srdK = __builtin_amdgcn_make_buffer_rsrc((void*)(kc + (size_t)(b * NH + h) * (SK * DH * 2)), (short)0, SK * DH * 2, 0x00020000);
    const __amdgpu_buffer_rsrc_t srdV = __builtin_amdgcn_make_buffer_rsrc((void*)(vc + (size_t)(b * NH + h) * (SK * DH * 2)), (short)0, SK * DH * 2, 0x00020000);
    const unsigned wv2 = wv0 + 2 * 31744, wv3 = wv0 + 3 * 31744;
    const unsigned kvA = pl * 256 + dbl * 64 + c4 * 16, kvB = pl * 256 + dbl * 64 + ((c4 ^ 2) * 16);
#define DMAW(c) do { _Pragma("unroll") for (int i_ = 0; i_ < 4; ++i_) DMA4(POLW, lds0 + (unsigned)(WQO(c) + 4096 * i_), wv0, wv1, wv2, wv3, srdW, (unsigned)(((c) * 64 + 16 * i_) * 8192)); } while (0)
#define DMAK_(t, so) do { _Pragma("unroll") for (int i_ = 0; i_ < 4; ++i_) { WAITV(59); DMA4("nt", lds0 + (unsigned)((so) + 4096 * i_), kvA, kvA, kvB, kvB, srdK, (unsigned)((t) * 16384 + 4096 * i_)); } } while (0)
#define DMAV_(t, so) do { _Pragma("unroll") for (int i_ = 0; i_ < 4; ++i_) { WAITV(59); DMA4("nt", lds0 + (unsigned)((so) + 16384 + 4096 * i_), kvA, kvB, kvA, kvB, srdV, (unsigned)((t) * 16384 + 4096 * i_)); } } while (0)
#define DMAK(t) DMAK_(t, SLOT(t))
#define DMAV(t) DMAV_(t, SLOT((t) + 1))
    const char* tw = wo + (size_t)(blockIdx.x >> 3) * 8192 + lane * 128;
#define TW(r) do { const char* p_ = tw + (size_t)((((r) + ROTM * (lane & 7) + ROTA) & 15) * 262144); asm volatile("s_waitcnt vmcnt(62)\n\tglobal_load_dword %0, %1, off" : "+v"(td) : "v"(p_) : "memory"); } while (0)
    asm volatile("global_load_dwordx2 %0, %1, off sc1" : "=v"(tag) : "v"(epochp) : "memory");
#define RW(i) do { _Pragma("unroll") for (int w_ = 0; w_ < 8; ++w_) { WAITV(59); DMA4("", lds0 + (unsigned)(RB(i) + 4096 * w_), ow0, ow1, ow2, ow3, srdWo, (unsigned)((RR(i) * 256 + 32 * w_) * 1024)); } } while (0)
    DMAW2(0);
    BARRIER();
    DMAW(1); DMAW(2); DMAW(3);
    WAITV(48); BARRIER();
    WAITV(32); BARRIER();
    WAITV(16); BARRIER();
    DMAW(4);
    WAITV(16); BARRIER();
    DMAW(5);
    WAITV(16); BARRIER();
    DMAW(6);
    WAITV(16); BARRIER();
    DMAW(7);
    WAITV(16); BARRIER();
    DMAK(0); DMAK(1); DMAV(0);
    WAITV(48); BARRIER();
    BARRIER();
    DMAK(2);
    WAITV(32); BARRIER();
    DMAV(1); DMAK(3); DMAV(2);
    WAITV(48); BARRIER();
    DMAK(4); DMAV(3); DMAK(5); DMAV(4);
    WAITV(63); BARRIER();
    {
      int so = SLOT(1);
      for (int t = 1; t < 11; ++t) {
        DMAK_(t + 5, so); DMAV_(t + 4, so);
        so = (so == 131072) ? 0 : so + 32768;
        WAITV(63);
        BARRIER();
      }
    }
    DMAV(15); TW(0); TW(1); TW(2); TW(3);
    asm volatile("" : "+v"(tag));
    tag = (((unsigned long long)(unsigned)__builtin_amdgcn_readfirstlane((unsigned)(tag >> 32)) << 32) | (unsigned)__builtin_amdgcn_readfirstlane((unsigned)tag)) + 1;
    if (lane == 0) { *(__attribute__((address_space(3))) unsigned*)(L + PUBW) = 0u; *(__attribute__((address_space(3))) unsigned long long*)(L + PUBW + 8) = tag; }
    asm volatile("s_waitcnt lgkmcnt(0)" ::: "memory");
    WAITV(63);
    BARRIER();
    TW(5); TW(6); TW(7); TW(4);
    WAITV(40);
    RW(0);
    BARRIER();
    RW(1); TW(8); TW(10); TW(11); TW(12);
    WAITV(63);
    BARRIER();
    RW(2); TW(9); TW(13); TW(14); TW(15);
    WAITV(4);
    BARRIER();
    {
      const int rbo = RBO();
      WREAD(0); WREAD(1); WREAD(2);
    }
    BARRIER();
    RW(3); RW(5); RW(6); RW(7);
    WAITV(0);
#undef TW
#undef RW
#undef DMAW
#undef DMAK
#undef DMAV
  } else {
  const __amdgpu_buffer_rsrc_t srdX = __builtin_amdgcn_make_buffer_rsrc((void*)(x + (size_t)(b * SQ + wid * 32) * (DM * 2)), (short)0, 32 * DM * 2, 0x00020000);
  const unsigned xs = (lane >> 3) * 1024 + (((lane & 7) ^ (lane >> 4)) * 16);
  const unsigned xv0 = xs, xv1 = (xs ^ 64) + 7168, xv2 = xs + 2 * 7168, xv3 = (xs ^ 64) + 3 * 7168;
#define XDMA(c) DMA4(POLW, lds0 + (unsigned)XO(c) + 4096u * wid, xv0, xv1, xv2, xv3, srdX, (unsigned)(128 * (c)))
  const unsigned xrb = (unsigned)(wid * 4096 + r32 * 128 + ((hi ^ ((r32 >> 1) & 7)) * 16));
  bf16x8 xf[2][4];
#define XRD(c, k_) xf[(c) & 1][k_] = ldsrd128((lds_cptr)L + ((xrb ^ (unsigned)((k_) << 5)) + XO(c)))
  DMAW2(0); XDMA(0);
  BARRIER();
  XDMA(1); XDMA(2);
  const lds_cptr wrb = (lds_cptr)L + 2048 * hi + 64 * tq + 32 * (tp & 1) + 16 * tblk + 8 * (tp >> 1);
  lds_cptr wrb2 = wrb + 131072; PIN(wrb2);
#define WRB(c) ((WQO(c) < 65536 ? wrb : wrb2) + (WQO(c) & 65535))
#define WRD(c, m) do { wlo[m] = ldstr(WRB(c) + 4096 * ((m) >> 2) + 256 * ((m) & 3)); whi[m] = ldstr(WRB(c) + 4096 * ((m) >> 2) + 1024 + 256 * ((m) & 3)); } while (0)
  AttnState st;
  f32x16 acc[4];
#pragma unroll
  for (int i = 0; i < 4; ++i) acc[i] = f32x16{};
  s16x4 wlo[16], whi[16];
  WAITV(8);
  XRD(0, 0); XRD(0, 1); XRD(0, 2); XRD(0, 3);
  STAMP(1);
  BARRIER();
  STAMP(2);
#pragma unroll
  for (int m = 0; m < PD; ++m) WRD(0, m);
  u32x4 qx[4], qy[4];
#define QCV1(nb, e_) do { \
      const unsigned lo_ = cvtpk(acc[nb][2 * (e_)] * QSCALE, acc[nb][2 * (e_) + 1] * QSCALE), hi_ = cvtpk(acc[nb][8 + 2 * (e_)] * QSCALE, acc[nb][9 + 2 * (e_)] * QSCALE); \
      auto r_ = __builtin_amdgcn_permlane16_swap(lo_, hi_, false, false); qx[nb][e_] = r_[0]; qy[nb][e_] = r_[1]; PIN(qx[nb]); PIN(qy[nb]); } while (0)
  static_for<128>([&](auto I) __attribute__((always_inline)) {
    constexpr int c = I.value >> 4, mm = I.value & 15;
    constexpr int m = (c == 7) ? (4 * (mm & 3) + (mm >> 2)) : mm;
    acc[m & 3] = MFMA(cat(wlo[m], whi[m]), xf[c & 1][m >> 2], acc[m & 3]); PIN(acc[m & 3]);
    if constexpr (c == 0 && mm + PD == 16) { BARRIER(); }
    if constexpr (mm + PD < 16) { constexpr int f = mm + PD, ff = (c == 7) ? (4 * (f & 3) + (f >> 2)) : f; WRD(c, ff); }
    else if constexpr (c < 7) { constexpr int f = mm + PD - 16, ff = (c + 1 == 7) ? (4 * (f & 3) + (f >> 2)) : f; WRD(c + 1, ff); }
    if constexpr (c == 0 && mm == 2) { XDMA(3); }
    if constexpr (c >= 1 && c <= 4 && mm == 0) { XDMA(c + 3); }
    if constexpr (c < 7) {
      if constexpr (mm == 4) { WAITV(c <= 4 ? 8 : 4 * (6 - c)); }
      if constexpr (mm >= 4 && mm < 8) { XRD(c + 1, mm - 4); }
    }
    if constexpr (c == 5) { st.o[mm >> 3][mm & 7] = f32x4{}; PIN(st.o[mm >> 3][mm & 7]); }
    if constexpr (c == 7 && mm >= 5) { QCV1((mm - 5) >> 2, (mm - 5) & 3); }
    SBAR();
    if constexpr (mm == 15) { BARRIER(); STAMP(3 + c); SBAR(); }
  });
  LSTAMP(58);
#undef XDMA
#undef XRD
#undef WRD
  const int l16 = lane & 15, g4 = lane >> 4;
  const lds_cptr kb = (lds_cptr)L + 1024 * (l16 >> 2) + 64 * (l16 & 3) + 16 * (g4 ^ ((l16 >> 2) & 2));
  const lds_cptr vb0 = (lds_cptr)L + 16384 + 1024 * g4 + 64 * tq + 32 * (g4 & 1) + 8 * tp;
  const lds_cptr vb1 = (lds_cptr)L + 16384 + 1024 * g4 + 64 * tq + 32 * (1 - (g4 & 1)) + 8 * tp;
#pragma unroll
  for (int m = 0; m < PD; ++m) st.kf[m] = ldsrd128(kb + SLOT(0) + 4096 * (m >> 2) + 256 * (m & 3));
  QCV1(2, 3); QCV1(3, 0); QCV1(3, 1); QCV1(3, 2); QCV1(3, 3);
#undef QCV1
#pragma unroll
  for (int nb = 0; nb < 4; ++nb) { st.q[0][nb] = __builtin_bit_cast(bf16x8, qx[nb]); st.q[1][nb] = __builtin_bit_cast(bf16x8, qy[nb]); }
  st.negm[0] = f32x4{}; st.negm[1] = f32x4{}; PIN(st.negm[0]); PIN(st.negm[1]);
  st.mhat[0] = st.mhat[1] = 0.f; st.lacc[0] = f32x4{}; st.lacc[1] = f32x4{};
  st.ones = bf16x8{0x3F80, 0x3F80, 0x3F80, 0x3F80, 0x3F80, 0x3F80, 0x3F80, 0x3F80}; PIN(st.ones);

  STile A, B;
  attn_step<true>(st, A, B, kb + SLOT(0), vb0 + SLOT(0), vb1 + SLOT(0), kb + SLOT(1), wid >= 4);
  STAMP(11);
  BARRIER();
  STAMP(12);
  int s0 = SLOT(1);
  for (int t = 1; t < 15; t += 2) {
    const int s1 = (s0 == 131072) ? 0 : s0 + 32768, s2 = (s1 == 131072) ? 0 : s1 + 32768;
    attn_step<false>(st, B, A, kb + s0, vb0 + s0, vb1 + s0, kb + s1, wid >= 4);
    STAMP(11 + 2 * t);
    BARRIER();
    STAMP(12 + 2 * t);
    attn_step<false>(st, A, B, kb + s1, vb0 + s1, vb1 + s1, kb + s2, wid >= 4);
    STAMP(13 + 2 * t);
    BARRIER();
    STAMP(14 + 2 * t);
    s0 = s2;
  }
  attn_step<false>(st, B, A, kb + SLOT(15), vb0 + SLOT(15), vb1 + SLOT(15), kb + SLOT(15), wid >= 4);
  STAMP(41);
  BARRIER();
  STAMP(42); LSTAMP(59);
  {
    lds_cptr vd0 = vb0 + SLOT(16), vd1 = vb1 + SLOT(16); PIN(vd0); PIN(vd1);
#define VRD(m) do { st.vlo[m] = ldstr((((m) & 2) ? vd1 : vd0) + 8192 * ((m) & 1) + 256 * ((m) >> 2)); st.vhi[m] = ldstr((((m) & 2) ? vd1 : vd0) + 8192 * ((m) & 1) + 4096 + 256 * ((m) >> 2)); } while (0)
    const int rbo = RBO();
    WREAD(0); WREAD(1); WREAD(2);
    BARRIER();
#pragma unroll
    for (int m = 0; m < 8; ++m) VRD(m);
    static_for<22>([&](auto I) __attribute__((always_inline)) { constexpr int n = 10 + I.value; EL(B, n) = EX(EL(B, n)); });
    static_for<12>([&](auto I) __attribute__((always_inline)) { constexpr int n = 8 + 2 * I.value; PWV(n)[(n >> 1) & 3] = cvtpk(EL(B, n), EL(B, n + 1)); });
#pragma unroll
    for (int w = 0; w < 2; ++w) {
      st.lacc[0] = MFMA16(st.ones, __builtin_bit_cast(bf16x8, st.pw[0][w]), st.lacc[0]);
      st.lacc[1] = MFMA16(st.ones, __builtin_bit_cast(bf16x8, st.pw[1][w]), st.lacc[1]);
    }
    const float inv0 = 1.0f / st.lacc[0][0], inv1 = 1.0f / st.lacc[1][0];
    const __amdgpu_buffer_rsrc_t srdOs = __builtin_amdgcn_make_buffer_rsrc((void*)(ows + (size_t)(b * NH + h) * 57344), (short)0, 57344, 0x00020000);
    const int qr0 = 32 * wid + l16, vk0 = qr0 / 28, qr1 = qr0 + 16, vk1 = qr1 / 28;
    const unsigned osv0 = vk0 * 7168 + (g4 >> 1) * 896 + ((g4 & 1) * 28 + qr0 - 28 * vk0) * 16, osv1 = vk1 * 7168 + (g4 >> 1) * 896 + ((g4 & 1) * 28 + qr1 - 28 * vk1) * 16;
#define OSTORE(u) do { \
      u32x4 w0_ = {cvtpk(st.o[0][2 * (u)][0] * inv0, st.o[0][2 * (u)][1] * inv0), cvtpk(st.o[0][2 * (u)][2] * inv0, st.o[0][2 * (u)][3] * inv0), \
                   cvtpk(st.o[0][2 * (u) + 1][0] * inv0, st.o[0][2 * (u) + 1][1] * inv0), cvtpk(st.o[0][2 * (u) + 1][2] * inv0, st.o[0][2 * (u) + 1][3] * inv0)}; \
      __builtin_amdgcn_raw_buffer_store_b128(w0_, srdOs, osv0 + (u) * 1792, 0, 16); \
      u32x4 w1_ = {cvtpk(st.o[1][2 * (u)][0] * inv1, st.o[1][2 * (u)][1] * inv1), cvtpk(st.o[1][2 * (u)][2] * inv1, st.o[1][2 * (u)][3] * inv1), \
                   cvtpk(st.o[1][2 * (u) + 1][0] * inv1, st.o[1][2 * (u) + 1][1] * inv1), cvtpk(st.o[1][2 * (u) + 1][2] * inv1, st.o[1][2 * (u) + 1][3] * inv1)}; \
      __builtin_amdgcn_raw_buffer_store_b128(w1_, srdOs, osv1 + (u) * 1792, 0, 16); } while (0)
    SBAR();
#pragma unroll
    for (int u = 0; u < 4; ++u) {
#pragma unroll
      for (int k = 0; k < 4; ++k) {
        const int m = 4 * u + k, db = m >> 1, w = m & 1;
        st.o[0][db] = MFMA16(cat(st.vlo[m], st.vhi[m]), __builtin_bit_cast(bf16x8, st.pw[0][w]), st.o[0][db]);
        st.o[1][db] = MFMA16(cat(st.vlo[m], st.vhi[m]), __builtin_bit_cast(bf16x8, st.pw[1][w]), st.o[1][db]);
        if (u < 2) { VRD(m + 8); }
      }
      SBAR();
      if (u >= 1) { OSTORE(u - 1); }
      SBAR();
    }
    STAMP(43);
    OSTORE(3);
    STAMP(44);
#undef VRD
#undef OSTORE
    WAITV(0);
    if (lane == 0) {
      const unsigned old = __hip_atomic_fetch_add((__attribute__((address_space(3))) unsigned*)(L + PUBW), 1u, __ATOMIC_RELAXED, __HIP_MEMORY_SCOPE_WORKGROUP);
      if (old == 6) __hip_atomic_store(flags + b * NH + h, *(__attribute__((address_space(3))) unsigned long long*)(L + PUBW + 8), __ATOMIC_RELAXED, __HIP_MEMORY_SCOPE_AGENT);
    }
    asm volatile("s_waitcnt lgkmcnt(0)" ::: "memory");
    STAMP(50);
  }
  }

  {
    static_assert(NA == 8, "prefetch sequences above are written for NA = 8");
    const int rbo = RBO(); W3();
    const __amdgpu_buffer_rsrc_t srdO = __builtin_amdgcn_make_buffer_rsrc((void*)(ows + (size_t)ob * (NH * 57344)), (short)0, NH * 57344, 0x00020000);
    const unsigned ov0 = (hi * 28 + (r32 < 28 ? r32 : 27)) * 16;
    const unsigned osb = (w3 >> 2) * 57344 + (w3 & 3) * 1792 + oqt * 2 * 7168;
    u32x4 of[3][4];
    f32x16 acc[2][2];
#pragma unroll
    for (int i = 0; i < 2; ++i) { acc[i][0] = f32x16{}; acc[i][1] = f32x16{}; }
#define OLOAD(i) do { const unsigned so_ = osb + (unsigned)(RR(i) * 2 * 57344), so1_ = so_ + 7168u; \
      BLOAD("sc1", of[(i) % 3][0], ov0, srdO, so_, 0); BLOAD("sc1", of[(i) % 3][1], ov0, srdO, so1_, 0); \
      BLOAD("sc1", of[(i) % 3][2], ov0, srdO, so_, 896); BLOAD("sc1", of[(i) % 3][3], ov0, srdO, so1_, 896); } while (0)
    BARRIER();
    LSTAMP(57);
    STAMP(51);
    WDMA(4);
    WREAD(3); WDMA(8); WREAD(5); WDMA(10); WREAD(6); WDMA(11); WREAD(7); WDMA(12);
    WAITV(16); WREAD(4); WDMA(9);
    STAMP(52);
    if (w3 == 7) {
      if (blockIdx.x != 0) {
        const unsigned long long* fp = flags + ob * NH + r32;
        for (unsigned spins = 0; spins < (1u << 18); ++spins) {
          const unsigned long long v = __hip_atomic_load(fp, __ATOMIC_RELAXED, __HIP_MEMORY_SCOPE_AGENT);
          if (__all(v == tag)) break;
          __builtin_amdgcn_s_sleep(1);
        }
      } else {
        for (unsigned spins = 0; spins < (1u << 18); ++spins) {
          bool ok = true;
#pragma unroll
          for (int k = 0; k < 4; ++k) ok &= __hip_atomic_load(flags + 64 * k + lane, __ATOMIC_RELAXED, __HIP_MEMORY_SCOPE_AGENT) == tag;
          if (__all(ok)) break;
          __builtin_amdgcn_s_sleep(1);
        }
        if (lane == 0) __hip_atomic_store(epochp, tag, __ATOMIC_RELAXED, __HIP_MEMORY_SCOPE_AGENT);
      }
      KEEP(td);
    }
    STAMP(53);
    BARRIER();
    LSTAMP(60);
    STAMP(54);
    OLOAD(0); OLOAD(1);
#pragma unroll
    for (int i = 0; i < 16; ++i) {
      const int cnt = (i < 15 ? 4 : 0) + ((i - 1 >= 1 && i - 1 <= 3) ? 4 : 0);
      WAITV4(cnt, of[i % 3][0], of[i % 3][1], of[i % 3][2], of[i % 3][3]);
      if (i >= 1 && i <= 3) { WRWAIT(i - 1 + NA); WDMA(i + 12); }
      if (i + 2 < 16) { OLOAD(i + 2); }
      SBAR();
#pragma unroll
      for (int ks = 0; ks < 2; ++ks)
#pragma unroll
        for (int nb = 0; nb < 2; ++nb)
#pragma unroll
          for (int vb = 0; vb < 2; ++vb)
            acc[nb][vb] = MFMA(wf[i % NA][2 * ks + nb], __builtin_bit_cast(bf16x8, of[i % 3][2 * ks + vb]), acc[nb][vb]);
      SBAR();
      if (i + NA < 16) { WREAD_NW(i + NA); }
      SBAR();
    }
    STAMP(55);
#undef WDMA
#undef WREAD
#undef OLOAD
#undef RBP
#pragma unroll
    for (int vb = 0; vb < 2; ++vb)
#pragma unroll
      for (int nb = 0; nb < 2; ++nb)
#pragma unroll
        for (int rg = 0; rg < 4; ++rg) {
          const int q = 32 * vb + r32, slot = 8 * nb + 2 * rg + hi;
          f32x4 v = {acc[nb][vb][4 * rg], acc[nb][vb][4 * rg + 1], acc[nb][vb][4 * rg + 2], acc[nb][vb][4 * rg + 3]};
          *(__attribute__((address_space(3))) f32x4*)(L + (q >> 4) * 32768 + wid * 4096 + (q & 15) * 256 + ((slot ^ (q & 15)) * 16)) = v;
        }
    asm volatile("s_waitcnt lgkmcnt(0)" ::: "memory");
    BARRIER();
    STAMP(56); LSTAMP(61);
    const int q = tid >> 3, pc = tid & 7;
    f32x4 s0 = {0, 0, 0, 0}, s1 = {0, 0, 0, 0};
#pragma unroll
    for (int w = 0; w < 8; ++w) {
      s0 += *(const __attribute__((address_space(3))) f32x4*)(L + (q >> 4) * 32768 + w * 4096 + (q & 15) * 256 + (((2 * pc) ^ (q & 15)) * 16));
      s1 += *(const __attribute__((address_space(3))) f32x4*)(L + (q >> 4) * 32768 + w * 4096 + (q & 15) * 256 + (((2 * pc + 1) ^ (q & 15)) * 16));
    }
    u32x4 w = {cvtpk(s0[0], s0[1]), cvtpk(s0[2], s0[3]), cvtpk(s1[0], s1[1]), cvtpk(s1[2], s1[3])};
    if ((q & 31) < 28)
      *(u32x4*)(out + (size_t)(ob * SQ + 56 * oqt + 28 * (q >> 5) + (q & 31)) * (DM * 2) + ont * 128 + pc * 16) = w;
  }
  STAMP(40); RSTAMP(63);
  STFLUSH(ows + (64 << 20));
}

extern "C" void kernel_launch(void* const* d_in, const int* in_sizes, int n_in,
                              void* d_out, int out_size, void* d_ws, size_t ws_size,
                              hipStream_t stream) {
  (void)in_sizes; (void)n_in; (void)out_size; (void)ws_size;
  (void)hipFuncSetAttribute((const void*)mha_k1, hipFuncAttributeMaxDynamicSharedMemorySize, K1_LDS);
  mha_k1<<<256, 512, K1_LDS, stream>>>((const char*)d_in[0], (const char*)d_in[1], (const char*)d_in[2], (const char*)d_in[3], (const char*)d_in[4], (char*)d_ws, (char*)d_out);
}
```

```cpp
#include <hip/hip_runtime.h>
#include <hip/hip_bf16.h>
#include <stdint.h>
#include <utility>
#include <type_traits>

typedef short    bf16x8 __attribute__((ext_vector_type(8)));
typedef short    s16x4  __attribute__((ext_vector_type(4)));
typedef float    f32x16 __attribute__((ext_vector_type(16)));
typedef float    f32x4  __attribute__((ext_vector_type(4)));
typedef float    f32x2  __attribute__((ext_vector_type(2)));
typedef __bf16   bf16x2 __attribute__((ext_vector_type(2)));
typedef unsigned u32x4  __attribute__((ext_vector_type(4)));
typedef __attribute__((address_space(3))) char* lds_ptr;
typedef __attribute__((address_space(3))) const char* lds_cptr;

#define DI __device__ __forceinline__
#define SBAR() __builtin_amdgcn_sched_barrier(0)
#define PIN(x) asm volatile("" : "+v"(x))
#define MFMA(a, b, c) __builtin_amdgcn_mfma_f32_32x32x16_bf16(a, b, c, 0, 0, 0)
#define BARRIER() asm volatile("s_barrier" ::: "memory")

#define POLW ""
constexpr int PRIO_TOGGLE = 21;
constexpr int P3NA = 8;
constexpr int NB = 8, SQ = 224, DM = 512, NH = 32, SK = 1024, DH = 128;
constexpr float QSCALE = 0.088388347648318440f * 1.4426950408889634f;
constexpr float THR = 8.0f;

template <int... Is, class F> DI void static_for_(std::integer_sequence<int, Is...>, F&& f) { (f(std::integral_constant<int, Is>{}), ...); }
template <int N, class F> DI void static_for(F&& f) { static_for_(std::make_integer_sequence<int, N>{}, f); }
DI unsigned cvtpk(float lo, float hi) { f32x2 v = {lo, hi}; bf16x2 b = __builtin_convertvector(v, bf16x2); return __builtin_bit_cast(unsigned, b); }
DI bf16x8 ldsrd128(lds_cptr p) { return *(const __attribute__((address_space(3))) bf16x8*)p; }
DI s16x4 ldstr(lds_cptr p) { return __builtin_bit_cast(s16x4, __builtin_amdgcn_ds_read_tr16_b64_v4i16((__attribute__((address_space(3))) s16x4*)p)); }
DI bf16x8 cat(s16x4 a, s16x4 b) { return __builtin_shufflevector(a, b, 0, 1, 2, 3, 4, 5, 6, 7); }
#define MX3(a, b, c) __builtin_fmaxf(__builtin_fmaxf((a), (b)), (c))

#define DMA4(POL, m0v, v0, v1, v2, v3, srd, soff)                                                     \
  asm volatile("s_mov_b32 m0, %0\n\ts_nop 0\n\t"                                                      \
               "buffer_load_dwordx4 %1, %5, %6 offen " POL " lds\n\t"                                 \
               "buffer_load_dwordx4 %2, %5, %6 offen offset:1024 " POL " lds\n\t"                     \
               "buffer_load_dwordx4 %3, %5, %6 offen offset:2048 " POL " lds\n\t"                     \
               "buffer_load_dwordx4 %4, %5, %6 offen offset:3072 " POL " lds"                         \
               :: "s"(m0v), "v"(v0), "v"(v1), "v"(v2), "v"(v3), "s"(srd), "s"(soff) : "memory")
#define DMA2(POL, m0v, v0, v1, srd, soff)                                                             \
  asm volatile("s_mov_b32 m0, %0\n\ts_nop 0\n\t"                                                      \
               "buffer_load_dwordx4 %1, %3, %4 offen " POL " lds\n\t"                                 \
               "buffer_load_dwordx4 %2, %3, %4 offen offset:1024 " POL " lds"                         \
               :: "s"(m0v), "v"(v0), "v"(v1), "s"(srd), "s"(soff) : "memory")
#define BLOAD(POL, dst, voff, srd, soff, imm)                                                         \
  asm volatile("buffer_load_dwordx4 %0, %1, %2, %3 offen offset:%4 " POL                              \
               : "=v"(dst) : "v"(voff), "s"(srd), "s"(soff), "i"(imm) : "memory")
#define WAITV(n) asm volatile("s_waitcnt vmcnt(%0)" :: "i"(n) : "memory")
#define WAITV4(n, a, b, c, d) asm volatile("s_waitcnt vmcnt(%4)" : "+v"(a), "+v"(b), "+v"(c), "+v"(d) : "i"(n) : "memory")

constexpr int K1_LDS = 163840;
constexpr size_t WS_FLAGS = (size_t)32 << 20, WS_EPOCH = WS_FLAGS + 4096;
DI constexpr int SLOT(int t) { return (t % 5) * 32768; }
DI constexpr int RB(int i) { return ((i + 2) % 5) * 32768; }
constexpr int PUBW = 32768;
DI constexpr int WQO(int c) { return (c & 3) == 0 ? 0 : (c & 3) == 1 ? 16384 : (c & 3) == 2 ? 131072 : 147456; }
DI constexpr int XO(int c) { return (c % 3) == 0 ? 65536 : (c % 3) == 1 ? 98304 : 32768; }

#define MFMA16(a, b, c) __builtin_amdgcn_mfma_f32_16x16x32_bf16(a, b, c, 0, 0, 0)
struct AttnState {
  f32x4 o[2][8];
  bf16x8 q[2][4];
  f32x4 negm[2];
  float mhat[2];
  f32x4 lacc[2];
  bf16x8 ones;
  bf16x8 kf[16];
  s16x4 vlo[16], vhi[16];
  u32x4 pw[2][2];
};
struct STile { f32x4 s[2][4]; };
DI float xmax4(float v) {
  auto a = __builtin_amdgcn_permlane16_swap(__float_as_uint(v), __float_as_uint(v), false, false);
  v = __builtin_fmaxf(__uint_as_float(a[0]), __uint_as_float(a[1]));
  auto c = __builtin_amdgcn_permlane32_swap(__float_as_uint(v), __float_as_uint(v), false, false);
  return __builtin_fmaxf(__uint_as_float(c[0]), __uint_as_float(c[1]));
}

constexpr int PD = 5;
#define EL(T, n) (T).s[((n) >> 3) & 1][2 * ((n) >> 4) + (((n) >> 2) & 1)][(n) & 3]
#define ELV(T, n) (T).s[((n) >> 3) & 1][2 * ((n) >> 4) + (((n) >> 2) & 1)]
#define PWV(n) st.pw[((n) >> 3) & 1][(n) >> 4]
#define EX(v) __builtin_amdgcn_exp2f(v)
template <bool FIRST>
DI void attn_step(AttnState& st, STile& C, STile& P, lds_cptr kp, lds_cptr vp0, lds_cptr vp1, lds_cptr kn, const bool young) {
#define KRD(m, base) st.kf[m] = ldsrd128((base) + 4096 * ((m) >> 2) + 256 * ((m) & 3))
#define VRD(m) do { st.vlo[m] = ldstr((((m) & 1) ? vp1 : vp0) + 8192 * ((m) >> 3) + 256 * (((m) & 7) >> 1)); \
                    st.vhi[m] = ldstr((((m) & 1) ? vp1 : vp0) + 8192 * ((m) >> 3) + 4096 + 256 * (((m) & 7) >> 1)); } while (0)
  float ma = -3.0e38f, mb = -3.0e38f, fct0 = 1.f, fct1 = 1.f;
  bool resc = false;
  SBAR();
  static_for<32>([&](auto I) __attribute__((always_inline)) {
    constexpr int g = I.value;
    if constexpr (g == 0) { if (young) __builtin_amdgcn_s_setprio(1); }
    if constexpr (g == PRIO_TOGGLE) { __builtin_amdgcn_s_setprio(0); }
    if constexpr (g < 16) {
      constexpr int kb = g >> 2, u = g & 3;
      if constexpr (u == 0) C.s[0][kb] = MFMA16(st.kf[g], st.q[0][0], st.negm[0]); else C.s[0][kb] = MFMA16(st.kf[g], st.q[0][u], C.s[0][kb]);
      PIN(C.s[0][kb]);
    } else if constexpr (!FIRST) {
      constexpr int m = g - 16;
      st.o[0][m & 7] = MFMA16(cat(st.vlo[m], st.vhi[m]), __builtin_bit_cast(bf16x8, st.pw[0][m >> 3]), st.o[0][m & 7]);
      PIN(st.o[0][m & 7]);
    }
    SBAR();
    {
      constexpr int f = g + PD;
      if constexpr (f < 16) { KRD(f, kp); }
      else if constexpr (f < 32) { if constexpr (!FIRST) { VRD(f - 16); } }
      else { KRD(f - 32, kn); }
    }
    if constexpr ((g & 1) && g >= 5 && g <= 19) {
      constexpr int kb = (g - 5) >> 2, hf = ((g - 5) >> 1) & 1;
      ma = MX3(ma, C.s[0][kb][2 * hf], C.s[0][kb][2 * hf + 1]); PIN(ma);
    }
    SBAR();
    if constexpr (g < 16) {
      constexpr int kb = g >> 2, u = g & 3;
      if constexpr (u == 0) C.s[1][kb] = MFMA16(st.kf[g], st.q[1][0], st.negm[1]); else C.s[1][kb] = MFMA16(st.kf[g], st.q[1][u], C.s[1][kb]);
      PIN(C.s[1][kb]);
    } else if constexpr (!FIRST) {
      constexpr int m = g - 16;
      st.o[1][m & 7] = MFMA16(cat(st.vlo[m], st.vhi[m]), __builtin_bit_cast(bf16x8, st.pw[1][m >> 3]), st.o[1][m & 7]);
      PIN(st.o[1][m & 7]);
    }
    SBAR();
    if constexpr ((g & 1) == 0) {
      if constexpr (g <= 22) {
        if constexpr (!FIRST) { constexpr int n = 8 + g; PWV(n)[(n >> 1) & 3] = cvtpk(EL(P, n), EL(P, n + 1)); PIN(PWV(n)); }
      } else { constexpr int n = g - 24; PWV(n)[(n >> 1) & 3] = cvtpk(EL(C, n), EL(C, n + 1)); PIN(PWV(n)); }
    }
    if constexpr ((g & 1) && g >= 5 && g <= 19) {
      constexpr int kb = (g - 5) >> 2, hf = ((g - 5) >> 1) & 1;
      mb = MX3(mb, C.s[1][kb][2 * hf], C.s[1][kb][2 * hf + 1]); PIN(mb);
    }
    if constexpr (g == 22) {
      const bool ok = (ma <= THR) & (mb <= THR);
      if (FIRST || __builtin_expect(!__all(ok), 0)) {
#pragma unroll
        for (int qb = 0; qb < 2; ++qb) {
          float rm = qb ? mb : ma;
          rm = xmax4(rm);
          const float dl = FIRST ? rm : __builtin_fmaxf(rm, 0.f);
          st.mhat[qb] += dl;
#pragma unroll
          for (int kb = 0; kb < 4; ++kb)
#pragma unroll
            for (int r = 0; r < 4; ++r) C.s[qb][kb][r] -= dl;
#pragma unroll
          for (int r = 0; r < 4; ++r) st.negm[qb][r] = -st.mhat[qb];
          PIN(st.negm[qb]);
          if constexpr (!FIRST) { const float f_ = EX(-dl); if (qb == 0) fct0 = f_; else fct1 = f_; }
        }
        resc = true;
      }
    }
    if constexpr (g <= 21) {
      if constexpr (!FIRST) { constexpr int n = 10 + g; EL(P, n) = EX(EL(P, n)); PIN(ELV(P, n)); }
    } else { constexpr int n = g - 22; EL(C, n) = EX(EL(C, n)); PIN(ELV(C, n)); }
    SBAR();
    if constexpr (!FIRST && (g == 21 || g == 23 || g == 25 || g == 27)) {
      constexpr int qb = (g >> 1) & 1, w = g >= 24;
      st.lacc[qb] = MFMA16(st.ones, __builtin_bit_cast(bf16x8, st.pw[qb][w]), st.lacc[qb]); PIN(st.lacc[qb]);
      SBAR();
    }
  });
  if constexpr (!FIRST) {
    if (__builtin_expect(resc, 0)) {
#pragma unroll
      for (int d = 0; d < 8; ++d)
#pragma unroll
        for (int r = 0; r < 4; ++r) { st.o[0][d][r] *= fct0; st.o[1][d][r] *= fct1; }
#pragma unroll
      for (int r = 0; r < 4; ++r) { st.lacc[0][r] *= fct0; st.lacc[1][r] *= fct1; }
    }
  }
#undef KRD
#undef VRD
}

__global__ __launch_bounds__(512, 2)
void mha_k1(const char* __restrict__ x, const char* __restrict__ kc, const char* __restrict__ vc,
            const char* __restrict__ wq, const char* __restrict__ wo, char* ows, char* __restrict__ out) {
  extern __shared__ __attribute__((aligned(16))) char lds[];
  const int tid = threadIdx.x, lane = tid & 63;
  const int wid = __builtin_amdgcn_readfirstlane(tid >> 6);
  const int b = 4 * (blockIdx.x & 1) + ((blockIdx.x >> 3) & 3), h = 8 * ((blockIdx.x >> 1) & 3) + (blockIdx.x >> 5);
  const unsigned lds0 = (unsigned)(uintptr_t)lds;
  const lds_ptr L = (lds_ptr)lds;

#define KEEP(d) asm volatile("" :: "v"(d) : "memory")
  const __amdgpu_buffer_rsrc_t srdW = __builtin_amdgcn_make_buffer_rsrc((void*)(wq + h * 256), (short)0, DM * 8192 - h * 256, 0x00020000);
  const unsigned pl = ((lane >> 2) & 3), dbl = lane >> 4, c4 = lane & 3;
  const unsigned wv0 = pl * 8192 + dbl * 64 + c4 * 16, wv1 = wv0 + 31744;
#define DMAW2(c) DMA2(POLW, lds0 + WQO(c) + 2048 * wid, wv0, wv1, srdW, (unsigned)(((c) * 64 + 8 * wid) * 8192))
  const int ob = blockIdx.x & 7, oqt = blockIdx.x >> 6, ont = (blockIdx.x >> 3) & 7;
  const __amdgpu_buffer_rsrc_t srdWo = __builtin_amdgcn_make_buffer_rsrc((void*)(wo + ont * 128), (short)0, NH * DH * DM * 2 - ont * 128, 0x00020000);
  const unsigned ow0 = (4 * (lane >> 5) + ((lane >> 2) & 3)) * 1024 + ((lane >> 4) & 1) * 64 + (lane & 3) * 16;
  const unsigned ow1 = ow0 + 7168, ow2 = ow0 + 2 * 7168, ow3 = ow0 + 3 * 7168;
  unsigned long long* const epochp = (unsigned long long*)(ows + WS_EPOCH);
  unsigned long long* const flags = (unsigned long long*)(ows + WS_FLAGS);
  unsigned long long tag = 0;
  unsigned td = 0;
#define RR(i) (i)
  constexpr int NA = P3NA;
  const int r32 = lane & 31, hi = lane >> 5, tq = (lane >> 2) & 3, tp = lane & 3, tblk = (lane >> 4) & 1;
  bf16x8 wf[NA][4];
#define RBP(i) ((lds_cptr)L + RB(i) + rbo)
#define WDMA(i) DMA4("", lds0 + (unsigned)(RB(i)) + 4096u * w3, ow0, ow1, ow2, ow3, srdWo, 32768u * w3 + (unsigned)(RR(i) * 256 * 1024))
#define W3() unsigned w3 = (unsigned)__builtin_amdgcn_readfirstlane(tid >> 6); asm volatile("s_nop 4" : "+s"(w3))
#define WREAD_NW(i) do { lds_cptr p_ = RBP(i); PIN(p_); _Pragma("unroll") for (int f_ = 0; f_ < 4; ++f_) \
      wf[(i) % NA][f_] = cat(ldstr(p_ + 1024 * (f_ >> 1) + 256 * (f_ & 1)), ldstr(p_ + 1024 * (f_ >> 1) + 2048 + 256 * (f_ & 1))); } while (0)
#define WRWAIT(i) asm volatile("s_waitcnt lgkmcnt(0)" : "+v"(wf[(i) % NA][0]), "+v"(wf[(i) % NA][1]), "+v"(wf[(i) % NA][2]), "+v"(wf[(i) % NA][3]) :: "memory")
#define WREAD(i) do { WREAD_NW(i); WRWAIT(i); } while (0)
#define RBO() (wid * 4096 + 512 * hi + 64 * tq + 32 * tblk + 8 * tp)
  if (wid == 7) {
    const __amdgpu_buffer_rsrc_t srdK = __builtin_amdgcn_make_buffer_rsrc((void*)(kc + (size_t)(b * NH + h) * (SK * DH * 2)), (short)0, SK * DH * 2, 0x00020000);
    const __amdgpu_buffer_rsrc_t srdV = __builtin_amdgcn_make_buffer_rsrc((void*)(vc + (size_t)(b * NH + h) * (SK * DH * 2)), (short)0, SK * DH * 2, 0x00020000);
    const unsigned wv2 = wv0 + 2 * 31744, wv3 = wv0 + 3 * 31744;
    const unsigned kvA = pl * 256 + dbl * 64 + c4 * 16, kvB = pl * 256 + dbl * 64 + ((c4 ^ 2) * 16);
#define DMAW(c) do { _Pragma("unroll") for (int i_ = 0; i_ < 4; ++i_) DMA4(POLW, lds0 + (unsigned)(WQO(c) + 4096 * i_), wv0, wv1, wv2, wv3, srdW, (unsigned)(((c) * 64 + 16 * i_) * 8192)); } while (0)
#define DMAK_(t, so) do { _Pragma("unroll") for (int i_ = 0; i_ < 4; ++i_) { WAITV(59); DMA4("nt", lds0 + (unsigned)((so) + 4096 * i_), kvA, kvA, kvB, kvB, srdK, (unsigned)((t) * 16384 + 4096 * i_)); } } while (0)
#define DMAV_(t, so) do { _Pragma("unroll") for (int i_ = 0; i_ < 4; ++i_) { WAITV(59); DMA4("nt", lds0 + (unsigned)((so) + 16384 + 4096 * i_), kvA, kvB, kvA, kvB, srdV, (unsigned)((t) * 16384 + 4096 * i_)); } } while (0)
#define DMAK(t) DMAK_(t, SLOT(t))
#define DMAV(t) DMAV_(t, SLOT((t) + 1))
    const char* tw = wo + (size_t)(blockIdx.x >> 3) * 8192 + lane * 128;
#define TW(r) do { const char* p_ = tw + (size_t)(r) * 262144; asm volatile("s_waitcnt vmcnt(62)\n\tglobal_load_dword %0, %1, off" : "+v"(td) : "v"(p_) : "memory"); } while (0)
#define RW(i) do { _Pragma("unroll") for (int w_ = 0; w_ < 8; ++w_) { WAITV(59); DMA4("", lds0 + (unsigned)(RB(i) + 4096 * w_), ow0, ow1, ow2, ow3, srdWo, (unsigned)((RR(i) * 256 + 32 * w_) * 1024)); } } while (0)
    DMAW2(0);
    BARRIER();
    DMAW(1);
    WAITV(16); BARRIER();
    DMAW(2);
    WAITV(16); BARRIER();
    DMAW(3);
    WAITV(16); BARRIER();
    DMAW(4);
    WAITV(16); BARRIER();
    DMAW(5);
    WAITV(16); BARRIER();
    DMAW(6);
    WAITV(16); BARRIER();
    DMAW(7);
    WAITV(16); BARRIER();
    DMAK(0); DMAK(1); DMAV(0);
    WAITV(48); BARRIER();
    BARRIER();
    DMAK(2);
    WAITV(32); BARRIER();
    DMAV(1); DMAK(3); DMAV(2);
    WAITV(48); BARRIER();
    DMAK(4); DMAV(3); DMAK(5); DMAV(4);
    WAITV(63); BARRIER();
    asm volatile("s_waitcnt vmcnt(62)\n\tglobal_load_dwordx2 %0, %1, off sc1" : "=v"(tag) : "v"(epochp) : "memory");
    {
      int so = SLOT(1);
      for (int t = 1; t < 11; ++t) {
        DMAK_(t + 5, so); DMAV_(t + 4, so);
        so = (so == 131072) ? 0 : so + 32768;
        WAITV(63);
        BARRIER();
      }
    }
    DMAV(15); TW(0); TW(1); TW(2); TW(3);
    asm volatile("" : "+v"(tag));
    tag = (((unsigned long long)(unsigned)__builtin_amdgcn_readfirstlane((unsigned)(tag >> 32)) << 32) | (unsigned)__builtin_amdgcn_readfirstlane((unsigned)tag)) + 1;
    if (lane == 0) { *(__attribute__((address_space(3))) unsigned*)(L + PUBW) = 0u; *(__attribute__((address_space(3))) unsigned long long*)(L + PUBW + 8) = tag; }
    asm volatile("s_waitcnt lgkmcnt(0)" ::: "memory");
    WAITV(63);
    BARRIER();
    TW(5); TW(6); TW(7); TW(4);
    WAITV(40);
    RW(0);
    BARRIER();
    RW(1); TW(8); TW(10); TW(11); TW(12);
    WAITV(63);
    BARRIER();
    RW(2); TW(9); TW(13); TW(14); TW(15);
    WAITV(4);
    BARRIER();
    {
      const int rbo = RBO();
      WREAD(0); WREAD(1); WREAD(2);
    }
    BARRIER();
    RW(3); RW(5); RW(6); RW(7);
    WAITV(0);
#undef TW
#undef RW
#undef DMAW
#undef DMAK
#undef DMAV
  } else {
  const __amdgpu_buffer_rsrc_t srdX = __builtin_amdgcn_make_buffer_rsrc((void*)(x + (size_t)(b * SQ + wid * 32) * (DM * 2)), (short)0, 32 * DM * 2, 0x00020000);
  const unsigned xs = (lane >> 3) * 1024 + (((lane & 7) ^ (lane >> 4)) * 16);
  const unsigned xv0 = xs, xv1 = (xs ^ 64) + 7168, xv2 = xs + 2 * 7168, xv3 = (xs ^ 64) + 3 * 7168;
#define XDMA(c) DMA4(POLW, lds0 + (unsigned)XO(c) + 4096u * wid, xv0, xv1, xv2, xv3, srdX, (unsigned)(128 * (c)))
  const unsigned xrb = (unsigned)(wid * 4096 + r32 * 128 + ((hi ^ ((r32 >> 1) & 7)) * 16));
  bf16x8 xf[2][4];
#define XRD(c, k_) xf[(c) & 1][k_] = ldsrd128((lds_cptr)L + ((xrb ^ (unsigned)((k_) << 5)) + XO(c)))
  DMAW2(0); XDMA(0);
  BARRIER();
  XDMA(1);
  const lds_cptr wrb = (lds_cptr)L + 2048 * hi + 64 * tq + 32 * (tp & 1) + 16 * tblk + 8 * (tp >> 1);
  lds_cptr wrb2 = wrb + 131072; PIN(wrb2);
#define WRB(c) ((WQO(c) < 65536 ? wrb : wrb2) + (WQO(c) & 65535))
#define WRD(c, m) do { wlo[m] = ldstr(WRB(c) + 4096 * ((m) >> 2) + 256 * ((m) & 3)); whi[m] = ldstr(WRB(c) + 4096 * ((m) >> 2) + 1024 + 256 * ((m) & 3)); } while (0)
  AttnState st;
  f32x16 acc[4];
#pragma unroll
  for (int i = 0; i < 4; ++i) acc[i] = f32x16{};
  s16x4 wlo[16], whi[16];
  WAITV(4);
  XRD(0, 0); XRD(0, 1); XRD(0, 2); XRD(0, 3);
  BARRIER();
#pragma unroll
  for (int m = 0; m < PD; ++m) WRD(0, m);
  u32x4 qx[4], qy[4];
#define QCV1(nb, e_) do { \
      const unsigned lo_ = cvtpk(acc[nb][2 * (e_)] * QSCALE, acc[nb][2 * (e_) + 1] * QSCALE), hi_ = cvtpk(acc[nb][8 + 2 * (e_)] * QSCALE, acc[nb][9 + 2 * (e_)] * QSCALE); \
      auto r_ = __builtin_amdgcn_permlane16_swap(lo_, hi_, false, false); qx[nb][e_] = r_[0]; qy[nb][e_] = r_[1]; PIN(qx[nb]); PIN(qy[nb]); } while (0)
  static_for<128>([&](auto I) __attribute__((always_inline)) {
    constexpr int c = I.value >> 4, mm = I.value & 15;
    constexpr int m = (c == 7) ? (4 * (mm & 3) + (mm >> 2)) : mm;
    acc[m & 3] = MFMA(cat(wlo[m], whi[m]), xf[c & 1][m >> 2], acc[m & 3]); PIN(acc[m & 3]);
    if constexpr (c == 0 && mm + PD == 16) { BARRIER(); }
    if constexpr (mm + PD < 16) { constexpr int f = mm + PD, ff = (c == 7) ? (4 * (f & 3) + (f >> 2)) : f; WRD(c, ff); }
    else if constexpr (c < 7) { constexpr int f = mm + PD - 16, ff = (c + 1 == 7) ? (4 * (f & 3) + (f >> 2)) : f; WRD(c + 1, ff); }
    if constexpr (c == 0 && mm == 0) { XDMA(2); }
    if constexpr (c == 0 && mm == 2) { XDMA(3); }
    if constexpr (c >= 1 && c <= 4 && mm == 0) { XDMA(c + 3); }
    if constexpr (c < 7) {
      if constexpr (mm == 4) { WAITV(c <= 4 ? 8 : 4 * (6 - c)); }
      if constexpr (mm >= 4 && mm < 8) { XRD(c + 1, mm - 4); }
    }
    if constexpr (c == 5) { st.o[mm >> 3][mm & 7] = f32x4{}; PIN(st.o[mm >> 3][mm & 7]); }
    if constexpr (c == 7 && mm >= 5) { QCV1((mm - 5) >> 2, (mm - 5) & 3); }
    SBAR();
    if constexpr (mm == 15) { BARRIER(); SBAR(); }
  });
#undef XDMA
#undef XRD
#undef WRD
  const int l16 = lane & 15, g4 = lane >> 4;
  const lds_cptr kb = (lds_cptr)L + 1024 * (l16 >> 2) + 64 * (l16 & 3) + 16 * (g4 ^ ((l16 >> 2) & 2));
  const lds_cptr vb0 = (lds_cptr)L + 16384 + 1024 * g4 + 64 * tq + 32 * (g4 & 1) + 8 * tp;
  const lds_cptr vb1 = (lds_cptr)L + 16384 + 1024 * g4 + 64 * tq + 32 * (1 - (g4 & 1)) + 8 * tp;
#pragma unroll
  for (int m = 0; m < PD; ++m) st.kf[m] = ldsrd128(kb + SLOT(0) + 4096 * (m >> 2) + 256 * (m & 3));
  QCV1(2, 3); QCV1(3, 0); QCV1(3, 1); QCV1(3, 2); QCV1(3, 3);
#undef QCV1
#pragma unroll
  for (int nb = 0; nb < 4; ++nb) { st.q[0][nb] = __builtin_bit_cast(bf16x8, qx[nb]); st.q[1][nb] = __builtin_bit_cast(bf16x8, qy[nb]); }
  st.negm[0] = f32x4{}; st.negm[1] = f32x4{}; PIN(st.negm[0]); PIN(st.negm[1]);
  st.mhat[0] = st.mhat[1] = 0.f; st.lacc[0] = f32x4{}; st.lacc[1] = f32x4{};
  st.ones = bf16x8{0x3F80, 0x3F80, 0x3F80, 0x3F80, 0x3F80, 0x3F80, 0x3F80, 0x3F80}; PIN(st.ones);

  STile A, B;
  attn_step<true>(st, A, B, kb + SLOT(0), vb0 + SLOT(0), vb1 + SLOT(0), kb + SLOT(1), wid >= 4);
  BARRIER();
  int s0 = SLOT(1);
  for (int t = 1; t < 15; t += 2) {
    const int s1 = (s0 == 131072) ? 0 : s0 + 32768, s2 = (s1 == 131072) ? 0 : s1 + 32768;
    attn_step<false>(st, B, A, kb + s0, vb0 + s0, vb1 + s0, kb + s1, wid >= 4);
    BARRIER();
    attn_step<false>(st, A, B, kb + s1, vb0 + s1, vb1 + s1, kb + s2, wid >= 4);
    BARRIER();
    s0 = s2;
  }
  attn_step<false>(st, B, A, kb + SLOT(15), vb0 + SLOT(15), vb1 + SLOT(15), kb + SLOT(15), wid >= 4);
  BARRIER();
  {
    lds_cptr vd0 = vb0 + SLOT(16), vd1 = vb1 + SLOT(16); PIN(vd0); PIN(vd1);
#define VRD(m) do { st.vlo[m] = ldstr((((m) & 2) ? vd1 : vd0) + 8192 * ((m) & 1) + 256 * ((m) >> 2)); st.vhi[m] = ldstr((((m) & 2) ? vd1 : vd0) + 8192 * ((m) & 1) + 4096 + 256 * ((m) >> 2)); } while (0)
    const int rbo = RBO();
    WREAD(0); WREAD(1); WREAD(2);
    BARRIER();
#pragma unroll
    for (int m = 0; m < 8; ++m) VRD(m);
    static_for<22>([&](auto I) __attribute__((always_inline)) { constexpr int n = 10 + I.value; EL(B, n) = EX(EL(B, n)); });
    static_for<12>([&](auto I) __attribute__((always_inline)) { constexpr int n = 8 + 2 * I.value; PWV(n)[(n >> 1) & 3] = cvtpk(EL(B, n), EL(B, n + 1)); });
#pragma unroll
    for (int w = 0; w < 2; ++w) {
      st.lacc[0] = MFMA16(st.ones, __builtin_bit_cast(bf16x8, st.pw[0][w]), st.lacc[0]);
      st.lacc[1] = MFMA16(st.ones, __builtin_bit_cast(bf16x8, st.pw[1][w]), st.lacc[1]);
    }
    const float inv0 = 1.0f / st.lacc[0][0], inv1 = 1.0f / st.lacc[1][0];
    const __amdgpu_buffer_rsrc_t srdOs = __builtin_amdgcn_make_buffer_rsrc((void*)(ows + (size_t)(b * NH + h) * 57344), (short)0, 57344, 0x00020000);
    const int qr0 = 32 * wid + l16, vk0 = qr0 / 28, qr1 = qr0 + 16, vk1 = qr1 / 28;
    const unsigned osv0 = vk0 * 7168 + (g4 >> 1) * 896 + ((g4 & 1) * 28 + qr0 - 28 * vk0) * 16, osv1 = vk1 * 7168 + (g4 >> 1) * 896 + ((g4 & 1) * 28 + qr1 - 28 * vk1) * 16;
#define OSTORE(u) do { \
      u32x4 w0_ = {cvtpk(st.o[0][2 * (u)][0] * inv0, st.o[0][2 * (u)][1] * inv0), cvtpk(st.o[0][2 * (u)][2] * inv0, st.o[0][2 * (u)][3] * inv0), \
                   cvtpk(st.o[0][2 * (u) + 1][0] * inv0, st.o[0][2 * (u) + 1][1] * inv0), cvtpk(st.o[0][2 * (u) + 1][2] * inv0, st.o[0][2 * (u) + 1][3] * inv0)}; \
      __builtin_amdgcn_raw_buffer_store_b128(w0_, srdOs, osv0 + (u) * 1792, 0, 16); \
      u32x4 w1_ = {cvtpk(st.o[1][2 * (u)][0] * inv1, st.o[1][2 * (u)][1] * inv1), cvtpk(st.o[1][2 * (u)][2] * inv1, st.o[1][2 * (u)][3] * inv1), \
                   cvtpk(st.o[1][2 * (u) + 1][0] * inv1, st.o[1][2 * (u) + 1][1] * inv1), cvtpk(st.o[1][2 * (u) + 1][2] * inv1, st.o[1][2 * (u) + 1][3] * inv1)}; \
      __builtin_amdgcn_raw_buffer_store_b128(w1_, srdOs, osv1 + (u) * 1792, 0, 16); } while (0)
    SBAR();
#pragma unroll
    for (int u = 0; u < 4; ++u) {
#pragma unroll
      for (int k = 0; k < 4; ++k) {
        const int m = 4 * u + k, db = m >> 1, w = m & 1;
        st.o[0][db] = MFMA16(cat(st.vlo[m], st.vhi[m]), __builtin_bit_cast(bf16x8, st.pw[0][w]), st.o[0][db]);
        st.o[1][db] = MFMA16(cat(st.vlo[m], st.vhi[m]), __builtin_bit_cast(bf16x8, st.pw[1][w]), st.o[1][db]);
        if (u < 2) { VRD(m + 8); }
      }
      SBAR();
      if (u >= 1) { OSTORE(u - 1); }
      SBAR();
    }
    OSTORE(3);
#undef VRD
#undef OSTORE
    WAITV(0);
    if (lane == 0) {
      const unsigned old = __hip_atomic_fetch_add((__attribute__((address_space(3))) unsigned*)(L + PUBW), 1u, __ATOMIC_RELAXED, __HIP_MEMORY_SCOPE_WORKGROUP);
      if (old == 6) __hip_atomic_store(flags + b * NH + h, *(__attribute__((address_space(3))) unsigned long long*)(L + PUBW + 8), __ATOMIC_RELAXED, __HIP_MEMORY_SCOPE_AGENT);
    }
    asm volatile("s_waitcnt lgkmcnt(0)" ::: "memory");
  }
  }

  {
    static_assert(NA == 8, "prefetch sequences above are written for NA = 8");
    const int rbo = RBO(); W3();
    const __amdgpu_buffer_rsrc_t srdO = __builtin_amdgcn_make_buffer_rsrc((void*)(ows + (size_t)ob * (NH * 57344)), (short)0, NH * 57344, 0x00020000);
    const unsigned ov0 = (hi * 28 + (r32 < 28 ? r32 : 27)) * 16;
    const unsigned osb = (w3 >> 2) * 57344 + (w3 & 3) * 1792 + oqt * 2 * 7168;
    u32x4 of[3][4];
    f32x16 acc[2][2];
#pragma unroll
    for (int i = 0; i < 2; ++i) { acc[i][0] = f32x16{}; acc[i][1] = f32x16{}; }
#define OLOAD(i) do { const unsigned so_ = osb + (unsigned)(RR(i) * 2 * 57344), so1_ = so_ + 7168u; \
      BLOAD("sc1", of[(i) % 3][0], ov0, srdO, so_, 0); BLOAD("sc1", of[(i) % 3][1], ov0, srdO, so1_, 0); \
      BLOAD("sc1", of[(i) % 3][2], ov0, srdO, so_, 896); BLOAD("sc1", of[(i) % 3][3], ov0, srdO, so1_, 896); } while (0)
    BARRIER();
    WDMA(4);
    WREAD(3); WDMA(8); WREAD(5); WDMA(10); WREAD(6); WDMA(11); WREAD(7); WDMA(12);
    WAITV(16); WREAD(4); WDMA(9);
    if (w3 == 7) {
      if (blockIdx.x != 0) {
        const unsigned long long* fp = flags + ob * NH + r32;
        for (unsigned spins = 0; spins < (1u << 18); ++spins) {
          const unsigned long long v = __hip_atomic_load(fp, __ATOMIC_RELAXED, __HIP_MEMORY_SCOPE_AGENT);
          if (__all(v == tag)) break;
          __builtin_amdgcn_s_sleep(1);
        }
      } else {
        for (unsigned spins = 0; spins < (1u << 18); ++spins) {
          bool ok = true;
#pragma unroll
          for (int k = 0; k < 4; ++k) ok &= __hip_atomic_load(flags + 64 * k + lane, __ATOMIC_RELAXED, __HIP_MEMORY_SCOPE_AGENT) == tag;
          if (__all(ok)) break;
          __builtin_amdgcn_s_sleep(1);
        }
        if (lane == 0) __hip_atomic_store(epochp, tag, __ATOMIC_RELAXED, __HIP_MEMORY_SCOPE_AGENT);
      }
      KEEP(td);
    }
    BARRIER();
    OLOAD(0); OLOAD(1);
#pragma unroll
    for (int i = 0; i < 16; ++i) {
      const int cnt = (i < 15 ? 4 : 0) + ((i - 1 >= 1 && i - 1 <= 3) ? 4 : 0);
      WAITV4(cnt, of[i % 3][0], of[i % 3][1], of[i % 3][2], of[i % 3][3]);
      if (i >= 1 && i <= 3) { WRWAIT(i - 1 + NA); WDMA(i + 12); }
      if (i + 2 < 16) { OLOAD(i + 2); }
      SBAR();
#pragma unroll
      for (int ks = 0; ks < 2; ++ks)
#pragma unroll
        for (int nb = 0; nb < 2; ++nb)
#pragma unroll
          for (int vb = 0; vb < 2; ++vb)
            acc[nb][vb] = MFMA(wf[i % NA][2 * ks + nb], __builtin_bit_cast(bf16x8, of[i % 3][2 * ks + vb]), acc[nb][vb]);
      SBAR();
      if (i + NA < 16) { WREAD_NW(i + NA); }
      SBAR();
    }
#undef WDMA
#undef WREAD
#undef OLOAD
#undef RBP
#pragma unroll
    for (int vb = 0; vb < 2; ++vb)
#pragma unroll
      for (int nb = 0; nb < 2; ++nb)
#pragma unroll
        for (int rg = 0; rg < 4; ++rg) {
          const int q = 32 * vb + r32, slot = 8 * nb + 2 * rg + hi;
          f32x4 v = {acc[nb][vb][4 * rg], acc[nb][vb][4 * rg + 1], acc[nb][vb][4 * rg + 2], acc[nb][vb][4 * rg + 3]};
          *(__attribute__((address_space(3))) f32x4*)(L + (q >> 4) * 32768 + wid * 4096 + (q & 15) * 256 + ((slot ^ (q & 15)) * 16)) = v;
        }
    asm volatile("s_waitcnt lgkmcnt(0)" ::: "memory");
    BARRIER();
    const int q = tid >> 3, pc = tid & 7;
    f32x4 s0 = {0, 0, 0, 0}, s1 = {0, 0, 0, 0};
#pragma unroll
    for (int w = 0; w < 8; ++w) {
      s0 += *(const __attribute__((address_space(3))) f32x4*)(L + (q >> 4) * 32768 + w * 4096 + (q & 15) * 256 + (((2 * pc) ^ (q & 15)) * 16));
      s1 += *(const __attribute__((address_space(3))) f32x4*)(L + (q >> 4) * 32768 + w * 4096 + (q & 15) * 256 + (((2 * pc + 1) ^ (q & 15)) * 16));
    }
    u32x4 w = {cvtpk(s0[0], s0[1]), cvtpk(s0[2], s0[3]), cvtpk(s1[0], s1[1]), cvtpk(s1[2], s1[3])};
    if ((q & 31) < 28)
      __builtin_amdgcn_raw_buffer_store_b128(w, __builtin_amdgcn_make_buffer_rsrc((void*)(out + (size_t)(ob * SQ + 56 * oqt) * (DM * 2)), (short)0, 56 * DM * 2, 0x00020000),
                                             (28 * (q >> 5) + (q & 31)) * (DM * 2) + ont * 128 + pc * 16, 0, 16);
  }
}

extern "C" void kernel_launch(void* const* d_in, const int* in_sizes, int n_in,
                              void* d_out, int out_size, void* d_ws, size_t ws_size,
                              hipStream_t stream) {
  (void)in_sizes; (void)n_in; (void)out_size; (void)ws_size;
  (void)hipFuncSetAttribute((const void*)mha_k1, hipFuncAttributeMaxDynamicSharedMemorySize, K1_LDS);
  mha_k1<<<256, 512, K1_LDS, stream>>>((const char*)d_in[0], (const char*)d_in[1], (const char*)d_in[2], (const char*)d_in[3], (const char*)d_in[4], (char*)d_ws + ((ws_size / 2) & ~(size_t)0xFFFFF), (char*)d_out);
}
```

```cpp
#include <hip/hip_runtime.h>
#include <hip/hip_bf16.h>
#include <stdint.h>
#include <utility>
#include <type_traits>

typedef short    bf16x8 __attribute__((ext_vector_type(8)));
typedef short    s16x4  __attribute__((ext_vector_type(4)));
typedef float    f32x16 __attribute__((ext_vector_type(16)));
typedef float    f32x4  __attribute__((ext_vector_type(4)));
typedef float    f32x2  __attribute__((ext_vector_type(2)));
typedef __bf16   bf16x2 __attribute__((ext_vector_type(2)));
typedef unsigned u32x4  __attribute__((ext_vector_type(4)));
typedef __attribute__((address_space(3))) char* lds_ptr;
typedef __attribute__((address_space(3))) const char* lds_cptr;

#define DI __device__ __forceinline__
#define SBAR() __builtin_amdgcn_sched_barrier(0)
#define PIN(x) asm volatile("" : "+v"(x))
#define MFMA(a, b, c) __builtin_amdgcn_mfma_f32_32x32x16_bf16(a, b, c, 0, 0, 0)
#define BARRIER() asm volatile("s_barrier" ::: "memory")

#define POLW ""
constexpr int PRIO_TOGGLE = 21;
constexpr int P3NA = 8;
constexpr int NB = 8, SQ = 224, DM = 512, NH = 32, SK = 1024, DH = 128;
constexpr float QSCALE = 0.088388347648318440f * 1.4426950408889634f;
constexpr float THR = 8.0f;

template <int... Is, class F> DI void static_for_(std::integer_sequence<int, Is...>, F&& f) { (f(std::integral_constant<int, Is>{}), ...); }
template <int N, class F> DI void static_for(F&& f) { static_for_(std::make_integer_sequence<int, N>{}, f); }
DI unsigned cvtpk(float lo, float hi) { f32x2 v = {lo, hi}; bf16x2 b = __builtin_convertvector(v, bf16x2); return __builtin_bit_cast(unsigned, b); }
DI bf16x8 ldsrd128(lds_cptr p) { return *(const __attribute__((address_space(3))) bf16x8*)p; }
DI s16x4 ldstr(lds_cptr p) { return __builtin_bit_cast(s16x4, __builtin_amdgcn_ds_read_tr16_b64_v4i16((__attribute__((address_space(3))) s16x4*)p)); }
DI bf16x8 cat(s16x4 a, s16x4 b) { return __builtin_shufflevector(a, b, 0, 1, 2, 3, 4, 5, 6, 7); }
#define MX3(a, b, c) __builtin_fmaxf(__builtin_fmaxf((a), (b)), (c))

#define DMA4(POL, m0v, v0, v1, v2, v3, srd, soff)                                                     \
  asm volatile("s_mov_b32 m0, %0\n\ts_nop 0\n\t"                                                      \
               "buffer_load_dwordx4 %1, %5, %6 offen " POL " lds\n\t"                                 \
               "buffer_load_dwordx4 %2, %5, %6 offen offset:1024 " POL " lds\n\t"                     \
               "buffer_load_dwordx4 %3, %5, %6 offen offset:2048 " POL " lds\n\t"                     \
               "buffer_load_dwordx4 %4, %5, %6 offen offset:3072 " POL " lds"                         \
               :: "s"(m0v), "v"(v0), "v"(v1), "v"(v2), "v"(v3), "s"(srd), "s"(soff) : "memory")
#define DMA2(POL, m0v, v0, v1, srd, soff)                                                             \
  asm volatile("s_mov_b32 m0, %0\n\ts_nop 0\n\t"                                                      \
               "buffer_load_dwordx4 %1, %3, %4 offen " POL " lds\n\t"                                 \
               "buffer_load_dwordx4 %2, %3, %4 offen offset:1024 " POL " lds"                         \
               :: "s"(m0v), "v"(v0), "v"(v1), "s"(srd), "s"(soff) : "memory")
#define BLOAD(POL, dst, voff, srd, soff, imm)                                                         \
  asm volatile("buffer_load_dwordx4 %0, %1, %2, %3 offen offset:%4 " POL                              \
               : "=v"(dst) : "v"(voff), "s"(srd), "s"(soff), "i"(imm) : "memory")
#define WAITV(n) asm volatile("s_waitcnt vmcnt(%0)" :: "i"(n) : "memory")
#define WAITV4(n, a, b, c, d) asm volatile("s_waitcnt vmcnt(%4)" : "+v"(a), "+v"(b), "+v"(c), "+v"(d) : "i"(n) : "memory")

constexpr int K1_LDS = 163840;
constexpr size_t WS_FLAGS = (size_t)32 << 20, WS_EPOCH = WS_FLAGS + 4096;
DI constexpr int SLOT(int t) { return (t % 5) * 32768; }
DI constexpr int RB(int i) { return ((i + 2) % 5) * 32768; }
constexpr int PUBW = 32768;
DI constexpr int WQO(int c) { return (c & 3) == 0 ? 0 : (c & 3) == 1 ? 16384 : (c & 3) == 2 ? 131072 : 147456; }
DI constexpr int XO(int c) { return (c % 3) == 0 ? 65536 : (c % 3) == 1 ? 98304 : 32768; }

#define MFMA16(a, b, c) __builtin_amdgcn_mfma_f32_16x16x32_bf16(a, b, c, 0, 0, 0)
struct AttnState {
  f32x4 o[2][8];
  bf16x8 q[2][4];
  f32x4 negm[2];
  float mhat[2];
  f32x4 lacc[2];
  bf16x8 ones;
  bf16x8 kf[16];
  s16x4 vlo[16], vhi[16];
  u32x4 pw[2][2];
};
struct STile { f32x4 s[2][4]; };
DI float xmax4(float v) {
  auto a = __builtin_amdgcn_permlane16_swap(__float_as_uint(v), __float_as_uint(v), false, false);
  v = __builtin_fmaxf(__uint_as_float(a[0]), __uint_as_float(a[1]));
  auto c = __builtin_amdgcn_permlane32_swap(__float_as_uint(v), __float_as_uint(v), false, false);
  return __builtin_fmaxf(__uint_as_float(c[0]), __uint_as_float(c[1]));
}

constexpr int PD = 5;
#define EL(T, n) (T).s[((n) >> 3) & 1][2 * ((n) >> 4) + (((n) >> 2) & 1)][(n) & 3]
#define ELV(T, n) (T).s[((n) >> 3) & 1][2 * ((n) >> 4) + (((n) >> 2) & 1)]
#define PWV(n) st.pw[((n) >> 3) & 1][(n) >> 4]
#define EX(v) __builtin_amdgcn_exp2f(v)
template <bool FIRST>
DI void attn_step(AttnState& st, STile& C, STile& P, lds_cptr kp, lds_cptr vp0, lds_cptr vp1, lds_cptr kn, const bool young) {
#define KRD(m, base) st.kf[m] = ldsrd128((base) + 4096 * ((m) >> 2) + 256 * ((m) & 3))
#define VRD(m) do { st.vlo[m] = ldstr((((m) & 1) ? vp1 : vp0) + 8192 * ((m) >> 3) + 256 * (((m) & 7) >> 1)); \
                    st.vhi[m] = ldstr((((m) & 1) ? vp1 : vp0) + 8192 * ((m) >> 3) + 4096 + 256 * (((m) & 7) >> 1)); } while (0)
  float ma = -3.0e38f, mb = -3.0e38f, fct0 = 1.f, fct1 = 1.f;
  bool resc = false;
  SBAR();
  static_for<32>([&](auto I) __attribute__((always_inline)) {
    constexpr int g = I.value;
    if constexpr (g == 0) { if (young) __builtin_amdgcn_s_setprio(1); }
    if constexpr (g == PRIO_TOGGLE) { __builtin_amdgcn_s_setprio(0); }
    if constexpr (g < 16) {
      constexpr int kb = g >> 2, u = g & 3;
      if constexpr (u == 0) C.s[0][kb] = MFMA16(st.kf[g], st.q[0][0], st.negm[0]); else C.s[0][kb] = MFMA16(st.kf[g], st.q[0][u], C.s[0][kb]);
      PIN(C.s[0][kb]);
    } else if constexpr (!FIRST) {
      constexpr int m = g - 16;
      st.o[0][m & 7] = MFMA16(cat(st.vlo[m], st.vhi[m]), __builtin_bit_cast(bf16x8, st.pw[0][m >> 3]), st.o[0][m & 7]);
      PIN(st.o[0][m & 7]);
    }
    SBAR();
    {
      constexpr int f = g + PD;
      if constexpr (f < 16) { KRD(f, kp); }
      else if constexpr (f < 32) { if constexpr (!FIRST) { VRD(f - 16); } }
      else { KRD(f - 32, kn); }
    }
    if constexpr ((g & 1) && g >= 5 && g <= 19) {
      constexpr int kb = (g - 5) >> 2, hf = ((g - 5) >> 1) & 1;
      ma = MX3(ma, C.s[0][kb][2 * hf], C.s[0][kb][2 * hf + 1]); PIN(ma);
    }
    SBAR();
    if constexpr (g < 16) {
      constexpr int kb = g >> 2, u = g & 3;
      if constexpr (u == 0) C.s[1][kb] = MFMA16(st.kf[g], st.q[1][0], st.negm[1]); else C.s[1][kb] = MFMA16(st.kf[g], st.q[1][u], C.s[1][kb]);
      PIN(C.s[1][kb]);
    } else if constexpr (!FIRST) {
      constexpr int m = g - 16;
      st.o[1][m & 7] = MFMA16(cat(st.vlo[m], st.vhi[m]), __builtin_bit_cast(bf16x8, st.pw[1][m >> 3]), st.o[1][m & 7]);
      PIN(st.o[1][m & 7]);
    }
    SBAR();
    if constexpr ((g & 1) == 0) {
      if constexpr (g <= 22) {
        if constexpr (!FIRST) { constexpr int n = 8 + g; PWV(n)[(n >> 1) & 3] = cvtpk(EL(P, n), EL(P, n + 1)); PIN(PWV(n)); }
      } else { constexpr int n = g - 24; PWV(n)[(n >> 1) & 3] = cvtpk(EL(C, n), EL(C, n + 1)); PIN(PWV(n)); }
    }
    if constexpr ((g & 1) && g >= 5 && g <= 19) {
      constexpr int kb = (g - 5) >> 2, hf = ((g - 5) >> 1) & 1;
      mb = MX3(mb, C.s[1][kb][2 * hf], C.s[1][kb][2 * hf + 1]); PIN(mb);
    }
    if constexpr (g == 22) {
      const bool ok = (ma <= THR) & (mb <= THR);
      if (FIRST || __builtin_expect(!__all(ok), 0)) {
#pragma unroll
        for (int qb = 0; qb < 2; ++qb) {
          float rm = qb ? mb : ma;
          rm = xmax4(rm);
          const float dl = FIRST ? rm : __builtin_fmaxf(rm, 0.f);
          st.mhat[qb] += dl;
#pragma unroll
          for (int kb = 0; kb < 4; ++kb)
#pragma unroll
            for (int r = 0; r < 4; ++r) C.s[qb][kb][r] -= dl;
#pragma unroll
          for (int r = 0; r < 4; ++r) st.negm[qb][r] = -st.mhat[qb];
          PIN(st.negm[qb]);
          if constexpr (!FIRST) { const float f_ = EX(-dl); if (qb == 0) fct0 = f_; else fct1 = f_; }
        }
        resc = true;
      }
    }
    if constexpr (g <= 21) {
      if constexpr (!FIRST) { constexpr int n = 10 + g; EL(P, n) = EX(EL(P, n)); PIN(ELV(P, n)); }
    } else { constexpr int n = g - 22; EL(C, n) = EX(EL(C, n)); PIN(ELV(C, n)); }
    SBAR();
    if constexpr (!FIRST && (g == 21 || g == 23 || g == 25 || g == 27)) {
      constexpr int qb = (g >> 1) & 1, w = g >= 24;
      st.lacc[qb] = MFMA16(st.ones, __builtin_bit_cast(bf16x8, st.pw[qb][w]), st.lacc[qb]); PIN(st.lacc[qb]);
      SBAR();
    }
  });
  if constexpr (!FIRST) {
    if (__builtin_expect(resc, 0)) {
#pragma unroll
      for (int d = 0; d < 8; ++d)
#pragma unroll
        for (int r = 0; r < 4; ++r) { st.o[0][d][r] *= fct0; st.o[1][d][r] *= fct1; }
#pragma unroll
      for (int r = 0; r < 4; ++r) { st.lacc[0][r] *= fct0; st.lacc[1][r] *= fct1; }
    }
  }
#undef KRD
#undef VRD
}

__global__ __launch_bounds__(512, 2)
void mha_k1(const char* __restrict__ x, const char* __restrict__ kc, const char* __restrict__ vc,
            const char* __restrict__ wq, const char* __restrict__ wo, char* ows, char* __restrict__ out) {
  extern __shared__ __attribute__((aligned(16))) char lds[];
  const int tid = threadIdx.x, lane = tid & 63;
  const int wid = __builtin_amdgcn_readfirstlane(tid >> 6);
  const int b = 4 * (blockIdx.x & 1) + ((blockIdx.x >> 3) & 3), h = 8 * ((blockIdx.x >> 1) & 3) + (blockIdx.x >> 5);
  const unsigned lds0 = (unsigned)(uintptr_t)lds;
  const lds_ptr L = (lds_ptr)lds;

#define KEEP(d) asm volatile("" :: "v"(d) : "memory")
  const __amdgpu_buffer_rsrc_t srdW = __builtin_amdgcn_make_buffer_rsrc((void*)(wq + h * 256), (short)0, DM * 8192 - h * 256, 0x00020000);
  const unsigned pl = ((lane >> 2) & 3), dbl = lane >> 4, c4 = lane & 3;
  const unsigned wv0 = pl * 8192 + dbl * 64 + c4 * 16, wv1 = wv0 + 31744;
#define DMAW2(c) DMA2(POLW, lds0 + WQO(c) + 2048 * wid, wv0, wv1, srdW, (unsigned)(((c) * 64 + 8 * wid) * 8192))
  const int ob = (blockIdx.x & 6) + ((blockIdx.x >> 5) & 1), oqt = blockIdx.x >> 6, ont = 4 * (blockIdx.x & 1) + ((blockIdx.x >> 3) & 3);
  const __amdgpu_buffer_rsrc_t srdWo = __builtin_amdgcn_make_buffer_rsrc((void*)(wo + ont * 128), (short)0, NH * DH * DM * 2 - ont * 128, 0x00020000);
  const unsigned ow0 = (4 * (lane >> 5) + ((lane >> 2) & 3)) * 1024 + ((lane >> 4) & 1) * 64 + (lane & 3) * 16;
  const unsigned ow1 = ow0 + 7168, ow2 = ow0 + 2 * 7168, ow3 = ow0 + 3 * 7168;
  unsigned long long* const epochp = (unsigned long long*)(ows + WS_EPOCH);
  unsigned long long* const flags = (unsigned long long*)(ows + WS_FLAGS);
  unsigned long long tag = 0;
  unsigned td = 0;
#define RR(i) (i)
  constexpr int NA = P3NA;
  const int r32 = lane & 31, hi = lane >> 5, tq = (lane >> 2) & 3, tp = lane & 3, tblk = (lane >> 4) & 1;
  bf16x8 wf[NA][4];
#define RBP(i) ((lds_cptr)L + RB(i) + rbo)
#define WDMA(i) DMA4("", lds0 + (unsigned)(RB(i)) + 4096u * w3, ow0, ow1, ow2, ow3, srdWo, 32768u * w3 + (unsigned)(RR(i) * 256 * 1024))
#define W3() unsigned w3 = (unsigned)__builtin_amdgcn_readfirstlane(tid >> 6); asm volatile("s_nop 4" : "+s"(w3))
#define WREAD_NW(i) do { lds_cptr p_ = RBP(i); PIN(p_); _Pragma("unroll") for (int f_ = 0; f_ < 4; ++f_) \
      wf[(i) % NA][f_] = cat(ldstr(p_ + 1024 * (f_ >> 1) + 256 * (f_ & 1)), ldstr(p_ + 1024 * (f_ >> 1) + 2048 + 256 * (f_ & 1))); } while (0)
#define WRWAIT(i) asm volatile("s_waitcnt lgkmcnt(0)" : "+v"(wf[(i) % NA][0]), "+v"(wf[(i) % NA][1]), "+v"(wf[(i) % NA][2]), "+v"(wf[(i) % NA][3]) :: "memory")
#define WREAD(i) do { WREAD_NW(i); WRWAIT(i); } while (0)
#define RBO() (wid * 4096 + 512 * hi + 64 * tq + 32 * tblk + 8 * tp)
  if (wid == 7) {
    const __amdgpu_buffer_rsrc_t srdK = __builtin_amdgcn_make_buffer_rsrc((void*)(kc + (size_t)(b * NH + h) * (SK * DH * 2)), (short)0, SK * DH * 2, 0x00020000);
    const __amdgpu_buffer_rsrc_t srdV = __builtin_amdgcn_make_buffer_rsrc((void*)(vc + (size_t)(b * NH + h) * (SK * DH * 2)), (short)0, SK * DH * 2, 0x00020000);
    const unsigned wv2 = wv0 + 2 * 31744, wv3 = wv0 + 3 * 31744;
    const unsigned kvA = pl * 256 + dbl * 64 + c4 * 16, kvB = pl * 256 + dbl * 64 + ((c4 ^ 2) * 16);
#define DMAW(c) do { _Pragma("unroll") for (int i_ = 0; i_ < 4; ++i_) DMA4(POLW, lds0 + (unsigned)(WQO(c) + 4096 * i_), wv0, wv1, wv2, wv3, srdW, (unsigned)(((c) * 64 + 16 * i_) * 8192)); } while (0)
#define DMAK_(t, so) do { _Pragma("unroll") for (int i_ = 0; i_ < 4; ++i_) { WAITV(59); DMA4("nt", lds0 + (unsigned)((so) + 4096 * i_), kvA, kvA, kvB, kvB, srdK, (unsigned)((t) * 16384 + 4096 * i_)); } } while (0)
#define DMAV_(t, so) do { _Pragma("unroll") for (int i_ = 0; i_ < 4; ++i_) { WAITV(59); DMA4("nt", lds0 + (unsigned)((so) + 16384 + 4096 * i_), kvA, kvB, kvA, kvB, srdV, (unsigned)((t) * 16384 + 4096 * i_)); } } while (0)
#define DMAK(t) DMAK_(t, SLOT(t))
#define DMAV(t) DMAV_(t, SLOT((t) + 1))
    const char* tw = wo + (size_t)(blockIdx.x >> 3) * 8192 + (lane >> 5) * 262144 + ((lane >> 2) & 7) * 1024 + (blockIdx.x & 1) * 512 + (lane & 3) * 128;
#define TW(r) do { const char* p_ = tw + (size_t)(r) * 524288; asm volatile("s_waitcnt vmcnt(62)\n\tglobal_load_dword %0, %1, off" : "+v"(td) : "v"(p_) : "memory"); } while (0)
#define RW(i) do { _Pragma("unroll") for (int w_ = 0; w_ < 8; ++w_) { WAITV(59); DMA4("", lds0 + (unsigned)(RB(i) + 4096 * w_), ow0, ow1, ow2, ow3, srdWo, (unsigned)((RR(i) * 256 + 32 * w_) * 1024)); } } while (0)
    DMAW2(0);
    BARRIER();
    DMAW(1);
    WAITV(16); BARRIER();
    DMAW(2);
    WAITV(16); BARRIER();
    DMAW(3);
    WAITV(16); BARRIER();
    DMAW(4);
    WAITV(16); BARRIER();
    DMAW(5);
    WAITV(16); BARRIER();
    DMAW(6);
    WAITV(16); BARRIER();
    DMAW(7);
    WAITV(16); BARRIER();
    DMAK(0); DMAK(1); DMAV(0);
    WAITV(48); BARRIER();
    BARRIER();
    DMAK(2);
    WAITV(32); BARRIER();
    DMAV(1); DMAK(3); DMAV(2);
    WAITV(48); BARRIER();
    DMAK(4); DMAV(3); DMAK(5); DMAV(4);
    WAITV(63); BARRIER();
    asm volatile("s_waitcnt vmcnt(62)\n\tglobal_load_dwordx2 %0, %1, off sc1" : "=v"(tag) : "v"(epochp) : "memory");
    {
      int so = SLOT(1);
      for (int t = 1; t < 11; ++t) {
        DMAK_(t + 5, so); DMAV_(t + 4, so);
        so = (so == 131072) ? 0 : so + 32768;
        WAITV(63);
        BARRIER();
      }
    }
    DMAV(15); TW(0);
    asm volatile("" : "+v"(tag));
    tag = (((unsigned long long)(unsigned)__builtin_amdgcn_readfirstlane((unsigned)(tag >> 32)) << 32) | (unsigned)__builtin_amdgcn_readfirstlane((unsigned)tag)) + 1;
    if (lane == 0) { *(__attribute__((address_space(3))) unsigned*)(L + PUBW) = 0u; *(__attribute__((address_space(3))) unsigned long long*)(L + PUBW + 8) = tag; }
    asm volatile("s_waitcnt lgkmcnt(0)" ::: "memory");
    WAITV(63);
    BARRIER();
    TW(1); TW(2);
    WAITV(35);
    RW(0);
    BARRIER();
    RW(1); TW(3); TW(4); TW(5);
    WAITV(63);
    BARRIER();
    RW(2); TW(6); TW(7);
    WAITV(2);
    BARRIER();
    {
      const int rbo = RBO();
      WREAD(0); WREAD(1); WREAD(2);
    }
    BARRIER();
    RW(3); RW(5); RW(6); RW(7);
    WAITV(0);
#undef TW
#undef RW
#undef DMAW
#undef DMAK
#undef DMAV
  } else {
  const __amdgpu_buffer_rsrc_t srdX = __builtin_amdgcn_make_buffer_rsrc((void*)(x + (size_t)(b * SQ + wid * 32) * (DM * 2)), (short)0, 32 * DM * 2, 0x00020000);
  const unsigned xs = (lane >> 3) * 1024 + (((lane & 7) ^ (lane >> 4)) * 16);
  const unsigned xv0 = xs, xv1 = (xs ^ 64) + 7168, xv2 = xs + 2 * 7168, xv3 = (xs ^ 64) + 3 * 7168;
#define XDMA(c) DMA4(POLW, lds0 + (unsigned)XO(c) + 4096u * wid, xv0, xv1, xv2, xv3, srdX, (unsigned)(128 * (c)))
  const unsigned xrb = (unsigned)(wid * 4096 + r32 * 128 + ((hi ^ ((r32 >> 1) & 7)) * 16));
  bf16x8 xf[2][4];
#define XRD(c, k_) xf[(c) & 1][k_] = ldsrd128((lds_cptr)L + ((xrb ^ (unsigned)((k_) << 5)) + XO(c)))
  DMAW2(0); XDMA(0);
  BARRIER();
  XDMA(1);
  const lds_cptr wrb = (lds_cptr)L + 2048 * hi + 64 * tq + 32 * (tp & 1) + 16 * tblk + 8 * (tp >> 1);
  lds_cptr wrb2 = wrb + 131072; PIN(wrb2);
#define WRB(c) ((WQO(c) < 65536 ? wrb : wrb2) + (WQO(c) & 65535))
#define WRD(c, m) do { wlo[m] = ldstr(WRB(c) + 4096 * ((m) >> 2) + 256 * ((m) & 3)); whi[m] = ldstr(WRB(c) + 4096 * ((m) >> 2) + 1024 + 256 * ((m) & 3)); } while (0)
  AttnState st;
  f32x16 acc[4];
#pragma unroll
  for (int i = 0; i < 4; ++i) acc[i] = f32x16{};
  s16x4 wlo[16], whi[16];
  WAITV(4);
  XRD(0, 0); XRD(0, 1); XRD(0, 2); XRD(0, 3);
  BARRIER();
#pragma unroll
  for (int m = 0; m < PD; ++m) WRD(0, m);
  u32x4 qx[4], qy[4];
#define QCV1(nb, e_) do { \
      const unsigned lo_ = cvtpk(acc[nb][2 * (e_)] * QSCALE, acc[nb][2 * (e_) + 1] * QSCALE), hi_ = cvtpk(acc[nb][8 + 2 * (e_)] * QSCALE, acc[nb][9 + 2 * (e_)] * QSCALE); \
      auto r_ = __builtin_amdgcn_permlane16_swap(lo_, hi_, false, false); qx[nb][e_] = r_[0]; qy[nb][e_] = r_[1]; PIN(qx[nb]); PIN(qy[nb]); } while (0)
  static_for<128>([&](auto I) __attribute__((always_inline)) {
    constexpr int c = I.value >> 4, mm = I.value & 15;
    constexpr int m = (c == 7) ? (4 * (mm & 3) + (mm >> 2)) : mm;
    acc[m & 3] = MFMA(cat(wlo[m], whi[m]), xf[c & 1][m >> 2], acc[m & 3]); PIN(acc[m & 3]);
    if constexpr (c == 0 && mm + PD == 16) { BARRIER(); }
    if constexpr (mm + PD < 16) { constexpr int f = mm + PD, ff = (c == 7) ? (4 * (f & 3) + (f >> 2)) : f; WRD(c, ff); }
    else if constexpr (c < 7) { constexpr int f = mm + PD - 16, ff = (c + 1 == 7) ? (4 * (f & 3) + (f >> 2)) : f; WRD(c + 1, ff); }
    if constexpr (c == 0 && mm == 0) { XDMA(2); }
    if constexpr (c == 0 && mm == 2) { XDMA(3); }
    if constexpr (c >= 1 && c <= 4 && mm == 0) { XDMA(c + 3); }
    if constexpr (c < 7) {
      if constexpr (mm == 4) { WAITV(c <= 4 ? 8 : 4 * (6 - c)); }
      if constexpr (mm >= 4 && mm < 8) { XRD(c + 1, mm - 4); }
    }
    if constexpr (c == 5) { st.o[mm >> 3][mm & 7] = f32x4{}; PIN(st.o[mm >> 3][mm & 7]); }
    if constexpr (c == 7 && mm >= 5) { QCV1((mm - 5) >> 2, (mm - 5) & 3); }
    SBAR();
    if constexpr (mm == 15) { BARRIER(); SBAR(); }
  });
#undef XDMA
#undef XRD
#undef WRD
  const int l16 = lane & 15, g4 = lane >> 4;
  const lds_cptr kb = (lds_cptr)L + 1024 * (l16 >> 2) + 64 * (l16 & 3) + 16 * (g4 ^ ((l16 >> 2) & 2));
  const lds_cptr vb0 = (lds_cptr)L + 16384 + 1024 * g4 + 64 * tq + 32 * (g4 & 1) + 8 * tp;
  const lds_cptr vb1 = (lds_cptr)L + 16384 + 1024 * g4 + 64 * tq + 32 * (1 - (g4 & 1)) + 8 * tp;
#pragma unroll
  for (int m = 0; m < PD; ++m) st.kf[m] = ldsrd128(kb + SLOT(0) + 4096 * (m >> 2) + 256 * (m & 3));
  QCV1(2, 3); QCV1(3, 0); QCV1(3, 1); QCV1(3, 2); QCV1(3, 3);
#undef QCV1
#pragma unroll
  for (int nb = 0; nb < 4; ++nb) { st.q[0][nb] = __builtin_bit_cast(bf16x8, qx[nb]); st.q[1][nb] = __builtin_bit_cast(bf16x8, qy[nb]); }
  st.negm[0] = f32x4{}; st.negm[1] = f32x4{}; PIN(st.negm[0]); PIN(st.negm[1]);
  st.mhat[0] = st.mhat[1] = 0.f; st.lacc[0] = f32x4{}; st.lacc[1] = f32x4{};
  st.ones = bf16x8{0x3F80, 0x3F80, 0x3F80, 0x3F80, 0x3F80, 0x3F80, 0x3F80, 0x3F80}; PIN(st.ones);

  STile A, B;
  attn_step<true>(st, A, B, kb + SLOT(0), vb0 + SLOT(0), vb1 + SLOT(0), kb + SLOT(1), wid >= 4);
  BARRIER();
  int s0 = SLOT(1);
  for (int t = 1; t < 15; t += 2) {
    const int s1 = (s0 == 131072) ? 0 : s0 + 32768, s2 = (s1 == 131072) ? 0 : s1 + 32768;
    attn_step<false>(st, B, A, kb + s0, vb0 + s0, vb1 + s0, kb + s1, wid >= 4);
    BARRIER();
    attn_step<false>(st, A, B, kb + s1, vb0 + s1, vb1 + s1, kb + s2, wid >= 4);
    BARRIER();
    s0 = s2;
  }
  attn_step<false>(st, B, A, kb + SLOT(15), vb0 + SLOT(15), vb1 + SLOT(15), kb + SLOT(15), wid >= 4);
  BARRIER();
  {
    lds_cptr vd0 = vb0 + SLOT(16), vd1 = vb1 + SLOT(16); PIN(vd0); PIN(vd1);
#define VRD(m) do { st.vlo[m] = ldstr((((m) & 2) ? vd1 : vd0) + 8192 * ((m) & 1) + 256 * ((m) >> 2)); st.vhi[m] = ldstr((((m) & 2) ? vd1 : vd0) + 8192 * ((m) & 1) + 4096 + 256 * ((m) >> 2)); } while (0)
    const int rbo = RBO();
    WREAD(0); WREAD(1); WREAD(2);
    BARRIER();
#pragma unroll
    for (int m = 0; m < 8; ++m) VRD(m);
    static_for<22>([&](auto I) __attribute__((always_inline)) { constexpr int n = 10 + I.value; EL(B, n) = EX(EL(B, n)); });
    static_for<12>([&](auto I) __attribute__((always_inline)) { constexpr int n = 8 + 2 * I.value; PWV(n)[(n >> 1) & 3] = cvtpk(EL(B, n), EL(B, n + 1)); });
#pragma unroll
    for (int w = 0; w < 2; ++w) {
      st.lacc[0] = MFMA16(st.ones, __builtin_bit_cast(bf16x8, st.pw[0][w]), st.lacc[0]);
      st.lacc[1] = MFMA16(st.ones, __builtin_bit_cast(bf16x8, st.pw[1][w]), st.lacc[1]);
    }
    const float inv0 = 1.0f / st.lacc[0][0], inv1 = 1.0f / st.lacc[1][0];
    const __amdgpu_buffer_rsrc_t srdOs = __builtin_amdgcn_make_buffer_rsrc((void*)(ows + (size_t)(b * NH + h) * 57344), (short)0, 57344, 0x00020000);
    const int qr0 = 32 * wid + l16, vk0 = qr0 / 28, qr1 = qr0 + 16, vk1 = qr1 / 28;
    const unsigned osv0 = vk0 * 7168 + (g4 >> 1) * 896 + ((g4 & 1) * 28 + qr0 - 28 * vk0) * 16, osv1 = vk1 * 7168 + (g4 >> 1) * 896 + ((g4 & 1) * 28 + qr1 - 28 * vk1) * 16;
#define OSTORE(u) do { \
      u32x4 w0_ = {cvtpk(st.o[0][2 * (u)][0] * inv0, st.o[0][2 * (u)][1] * inv0), cvtpk(st.o[0][2 * (u)][2] * inv0, st.o[0][2 * (u)][3] * inv0), \
                   cvtpk(st.o[0][2 * (u) + 1][0] * inv0, st.o[0][2 * (u) + 1][1] * inv0), cvtpk(st.o[0][2 * (u) + 1][2] * inv0, st.o[0][2 * (u) + 1][3] * inv0)}; \
      __builtin_amdgcn_raw_buffer_store_b128(w0_, srdOs, osv0 + (u) * 1792, 0, 16); \
      u32x4 w1_ = {cvtpk(st.o[1][2 * (u)][0] * inv1, st.o[1][2 * (u)][1] * inv1), cvtpk(st.o[1][2 * (u)][2] * inv1, st.o[1][2 * (u)][3] * inv1), \
                   cvtpk(st.o[1][2 * (u) + 1][0] * inv1, st.o[1][2 * (u) + 1][1] * inv1), cvtpk(st.o[1][2 * (u) + 1][2] * inv1, st.o[1][2 * (u) + 1][3] * inv1)}; \
      __builtin_amdgcn_raw_buffer_store_b128(w1_, srdOs, osv1 + (u) * 1792, 0, 16); } while (0)
    SBAR();
#pragma unroll
    for (int u = 0; u < 4; ++u) {
#pragma unroll
      for (int k = 0; k < 4; ++k) {
        const int m = 4 * u + k, db = m >> 1, w = m & 1;
        st.o[0][db] = MFMA16(cat(st.vlo[m], st.vhi[m]), __builtin_bit_cast(bf16x8, st.pw[0][w]), st.o[0][db]);
        st.o[1][db] = MFMA16(cat(st.vlo[m], st.vhi[m]), __builtin_bit_cast(bf16x8, st.pw[1][w]), st.o[1][db]);
        if (u < 2) { VRD(m + 8); }
      }
      SBAR();
      if (u >= 1) { OSTORE(u - 1); }
      SBAR();
    }
    OSTORE(3);
#undef VRD
#undef OSTORE
    WAITV(0);
    if (lane == 0) {
      const unsigned old = __hip_atomic_fetch_add((__attribute__((address_space(3))) unsigned*)(L + PUBW), 1u, __ATOMIC_RELAXED, __HIP_MEMORY_SCOPE_WORKGROUP);
      if (old == 6) __hip_atomic_store(flags + b * NH + h, *(__attribute__((address_space(3))) unsigned long long*)(L + PUBW + 8), __ATOMIC_RELAXED, __HIP_MEMORY_SCOPE_AGENT);
    }
    asm volatile("s_waitcnt lgkmcnt(0)" ::: "memory");
  }
  }

  {
    static_assert(NA == 8, "prefetch sequences above are written for NA = 8");
    const int rbo = RBO(); W3();
    const __amdgpu_buffer_rsrc_t srdO = __builtin_amdgcn_make_buffer_rsrc((void*)(ows + (size_t)ob * (NH * 57344)), (short)0, NH * 57344, 0x00020000);
    const unsigned ov0 = (hi * 28 + (r32 < 28 ? r32 : 27)) * 16;
    const unsigned osb = (w3 >> 2) * 57344 + (w3 & 3) * 1792 + oqt * 2 * 7168;
    u32x4 of[3][4];
    f32x16 acc[2][2];
#pragma unroll
    for (int i = 0; i < 2; ++i) { acc[i][0] = f32x16{}; acc[i][1] = f32x16{}; }
#define OLOAD(i) do { const unsigned so_ = osb + (unsigned)(RR(i) * 2 * 57344), so1_ = so_ + 7168u; \
      BLOAD("sc1", of[(i) % 3][0], ov0, srdO, so_, 0); BLOAD("sc1", of[(i) % 3][1], ov0, srdO, so1_, 0); \
      BLOAD("sc1", of[(i) % 3][2], ov0, srdO, so_, 896); BLOAD("sc1", of[(i) % 3][3], ov0, srdO, so1_, 896); } while (0)
    BARRIER();
    WDMA(4);
    WREAD(3); WDMA(8); WREAD(5); WDMA(10); WREAD(6); WDMA(11); WREAD(7); WDMA(12);
    WAITV(16); WREAD(4); WDMA(9);
    if (w3 == 7) {
      if (blockIdx.x != 0) {
        const unsigned long long* fp = flags + ob * NH + r32;
        for (unsigned spins = 0; spins < (1u << 18); ++spins) {
          const unsigned long long v = __hip_atomic_load(fp, __ATOMIC_RELAXED, __HIP_MEMORY_SCOPE_AGENT);
          if (__all(v == tag)) break;
          __builtin_amdgcn_s_sleep(1);
        }
      } else {
        for (unsigned spins = 0; spins < (1u << 18); ++spins) {
          bool ok = true;
#pragma unroll
          for (int k = 0; k < 4; ++k) ok &= __hip_atomic_load(flags + 64 * k + lane, __ATOMIC_RELAXED, __HIP_MEMORY_SCOPE_AGENT) == tag;
          if (__all(ok)) break;
          __builtin_amdgcn_s_sleep(1);
        }
        if (lane == 0) __hip_atomic_store(epochp, tag, __ATOMIC_RELAXED, __HIP_MEMORY_SCOPE_AGENT);
      }
      KEEP(td);
    }
    BARRIER();
    OLOAD(0); OLOAD(1);
#pragma unroll
    for (int i = 0; i < 16; ++i) {
      const int cnt = (i < 15 ? 4 : 0) + ((i - 1 >= 1 && i - 1 <= 3) ? 4 : 0);
      WAITV4(cnt, of[i % 3][0], of[i % 3][1], of[i % 3][2], of[i % 3][3]);
      if (i >= 1 && i <= 3) { WRWAIT(i - 1 + NA); WDMA(i + 12); }
      if (i + 2 < 16) { OLOAD(i + 2); }
      SBAR();
#pragma unroll
      for (int ks = 0; ks < 2; ++ks)
#pragma unroll
        for (int nb = 0; nb < 2; ++nb)
#pragma unroll
          for (int vb = 0; vb < 2; ++vb)
            acc[nb][vb] = MFMA(wf[i % NA][2 * ks + nb], __builtin_bit_cast(bf16x8, of[i % 3][2 * ks + vb]), acc[nb][vb]);
      SBAR();
      if (i + NA < 16) { WREAD_NW(i + NA); }
      SBAR();
    }
#undef WDMA
#undef WREAD
#undef OLOAD
#undef RBP
#pragma unroll
    for (int vb = 0; vb < 2; ++vb)
#pragma unroll
      for (int nb = 0; nb < 2; ++nb)
#pragma unroll
        for (int rg = 0; rg < 4; ++rg) {
          const int q = 32 * vb + r32, slot = 8 * nb + 2 * rg + hi;
          f32x4 v = {acc[nb][vb][4 * rg], acc[nb][vb][4 * rg + 1], acc[nb][vb][4 * rg + 2], acc[nb][vb][4 * rg + 3]};
          *(__attribute__((address_space(3))) f32x4*)(L + (q >> 4) * 32768 + wid * 4096 + (q & 15) * 256 + ((slot ^ (q & 15)) * 16)) = v;
        }
    asm volatile("s_waitcnt lgkmcnt(0)" ::: "memory");
    BARRIER();
    const int q = tid >> 3, pc = tid & 7;
    f32x4 s0 = {0, 0, 0, 0}, s1 = {0, 0, 0, 0};
#pragma unroll
    for (int w = 0; w < 8; ++w) {
      s0 += *(const __attribute__((address_space(3))) f32x4*)(L + (q >> 4) * 32768 + w * 4096 + (q & 15) * 256 + (((2 * pc) ^ (q & 15)) * 16));
      s1 += *(const __attribute__((address_space(3))) f32x4*)(L + (q >> 4) * 32768 + w * 4096 + (q & 15) * 256 + (((2 * pc + 1) ^ (q & 15)) * 16));
    }
    u32x4 w = {cvtpk(s0[0], s0[1]), cvtpk(s0[2], s0[3]), cvtpk(s1[0], s1[1]), cvtpk(s1[2], s1[3])};
    if ((q & 31) < 28)
      __builtin_amdgcn_raw_buffer_store_b128(w, __builtin_amdgcn_make_buffer_rsrc((void*)(out + (size_t)(ob * SQ + 56 * oqt) * (DM * 2)), (short)0, 56 * DM * 2, 0x00020000),
                                             (28 * (q >> 5) + (q & 31)) * (DM * 2) + ont * 128 + pc * 16, 0, 16);
  }
}

extern "C" void kernel_launch(void* const* d_in, const int* in_sizes, int n_in,
                              void* d_out, int out_size, void* d_ws, size_t ws_size,
                              hipStream_t stream) {
  (void)in_sizes; (void)n_in; (void)out_size; (void)ws_size;
  (void)hipFuncSetAttribute((const void*)mha_k1, hipFuncAttributeMaxDynamicSharedMemorySize, K1_LDS);
  mha_k1<<<256, 512, K1_LDS, stream>>>((const char*)d_in[0], (const char*)d_in[1], (const char*)d_in[2], (const char*)d_in[3], (const char*)d_in[4], (char*)d_ws, (char*)d_out);
}
```

```cpp
#include <hip/hip_runtime.h>
#include <hip/hip_bf16.h>
#include <stdint.h>
#include <utility>
#include <type_traits>

typedef short    bf16x8 __attribute__((ext_vector_type(8)));
typedef short    s16x4  __attribute__((ext_vector_type(4)));
typedef float    f32x16 __attribute__((ext_vector_type(16)));
typedef float    f32x4  __attribute__((ext_vector_type(4)));
typedef float    f32x2  __attribute__((ext_vector_type(2)));
typedef __bf16   bf16x2 __attribute__((ext_vector_type(2)));
typedef unsigned u32x4  __attribute__((ext_vector_type(4)));
typedef __attribute__((address_space(3))) char* lds_ptr;
typedef __attribute__((address_space(3))) const char* lds_cptr;

#define DI __device__ __forceinline__
#define SBAR() __builtin_amdgcn_sched_barrier(0)
#define PIN(x) asm volatile("" : "+v"(x))
#define MFMA(a, b, c) __builtin_amdgcn_mfma_f32_32x32x16_bf16(a, b, c, 0, 0, 0)
#define BARRIER() asm volatile("s_barrier" ::: "memory")

#define POLW ""
constexpr int PRIO_TOGGLE = 21;
constexpr int P3NA = 8;
constexpr int NB = 8, SQ = 224, DM = 512, NH = 32, SK = 1024, DH = 128;
constexpr float QSCALE = 0.088388347648318440f * 1.4426950408889634f;
constexpr float THR = 8.0f;

template <int... Is, class F> DI void static_for_(std::integer_sequence<int, Is...>, F&& f) { (f(std::integral_constant<int, Is>{}), ...); }
template <int N, class F> DI void static_for(F&& f) { static_for_(std::make_integer_sequence<int, N>{}, f); }
DI unsigned cvtpk(float lo, float hi) { f32x2 v = {lo, hi}; bf16x2 b = __builtin_convertvector(v, bf16x2); return __builtin_bit_cast(unsigned, b); }
DI bf16x8 ldsrd128(lds_cptr p) { return *(const __attribute__((address_space(3))) bf16x8*)p; }
DI s16x4 ldstr(lds_cptr p) { return __builtin_bit_cast(s16x4, __builtin_amdgcn_ds_read_tr16_b64_v4i16((__attribute__((address_space(3))) s16x4*)p)); }
DI bf16x8 cat(s16x4 a, s16x4 b) { return __builtin_shufflevector(a, b, 0, 1, 2, 3, 4, 5, 6, 7); }
#define MX3(a, b, c) __builtin_fmaxf(__builtin_fmaxf((a), (b)), (c))

#define DMA4(POL, m0v, v0, v1, v2, v3, srd, soff)                                                     \
  asm volatile("s_mov_b32 m0, %0\n\ts_nop 0\n\t"                                                      \
               "buffer_load_dwordx4 %1, %5, %6 offen " POL " lds\n\t"                                 \
               "buffer_load_dwordx4 %2, %5, %6 offen offset:1024 " POL " lds\n\t"                     \
               "buffer_load_dwordx4 %3, %5, %6 offen offset:2048 " POL " lds\n\t"                     \
               "buffer_load_dwordx4 %4, %5, %6 offen offset:3072 " POL " lds"                         \
               :: "s"(m0v), "v"(v0), "v"(v1), "v"(v2), "v"(v3), "s"(srd), "s"(soff) : "memory")
#define DMA2(POL, m0v, v0, v1, srd, soff)                                                             \
  asm volatile("s_mov_b32 m0, %0\n\ts_nop 0\n\t"                                                      \
               "buffer_load_dwordx4 %1, %3, %4 offen " POL " lds\n\t"                                 \
               "buffer_load_dwordx4 %2, %3, %4 offen offset:1024 " POL " lds"                         \
               :: "s"(m0v), "v"(v0), "v"(v1), "s"(srd), "s"(soff) : "memory")
#define BLOAD(POL, dst, voff, srd, soff, imm)                                                         \
  asm volatile("buffer_load_dwordx4 %0, %1, %2, %3 offen offset:%4 " POL                              \
               : "=v"(dst) : "v"(voff), "s"(srd), "s"(soff), "i"(imm) : "memory")
#define WAITV(n) asm volatile("s_waitcnt vmcnt(%0)" :: "i"(n) : "memory")
#define WAITV4(n, a, b, c, d) asm volatile("s_waitcnt vmcnt(%4)" : "+v"(a), "+v"(b), "+v"(c), "+v"(d) : "i"(n) : "memory")

constexpr int K1_LDS = 163840;
constexpr size_t WS_FLAGS = (size_t)32 << 20, WS_EPOCH = WS_FLAGS + 4096;
DI constexpr int SLOT(int t) { return (t % 5) * 32768; }
DI constexpr int RB(int i) { return ((i + 2) % 5) * 32768; }
constexpr int PUBW = 32768;
DI constexpr int WQO(int c) { return (c & 3) == 0 ? 0 : (c & 3) == 1 ? 16384 : (c & 3) == 2 ? 131072 : 147456; }
DI constexpr int XO(int c) { return (c % 3) == 0 ? 65536 : (c % 3) == 1 ? 98304 : 32768; }

#define MFMA16(a, b, c) __builtin_amdgcn_mfma_f32_16x16x32_bf16(a, b, c, 0, 0, 0)
struct AttnState {
  f32x4 o[2][8];
  bf16x8 q[2][4];
  f32x4 negm[2];
  float mhat[2];
  f32x4 lacc[2];
  bf16x8 ones;
  bf16x8 kf[16];
  s16x4 vlo[16], vhi[16];
  u32x4 pw[2][2];
};
struct STile { f32x4 s[2][4]; };
DI float xmax4(float v) {
  auto a = __builtin_amdgcn_permlane16_swap(__float_as_uint(v), __float_as_uint(v), false, false);
  v = __builtin_fmaxf(__uint_as_float(a[0]), __uint_as_float(a[1]));
  auto c = __builtin_amdgcn_permlane32_swap(__float_as_uint(v), __float_as_uint(v), false, false);
  return __builtin_fmaxf(__uint_as_float(c[0]), __uint_as_float(c[1]));
}

constexpr int PD = 5;
#define EL(T, n) (T).s[((n) >> 3) & 1][2 * ((n) >> 4) + (((n) >> 2) & 1)][(n) & 3]
#define ELV(T, n) (T).s[((n) >> 3) & 1][2 * ((n) >> 4) + (((n) >> 2) & 1)]
#define PWV(n) st.pw[((n) >> 3) & 1][(n) >> 4]
#define EX(v) __builtin_amdgcn_exp2f(v)
template <bool FIRST>
DI void attn_step(AttnState& st, STile& C, STile& P, lds_cptr kp, lds_cptr vp0, lds_cptr vp1, lds_cptr kn, const bool young) {
#define KRD(m, base) st.kf[m] = ldsrd128((base) + 4096 * ((m) >> 2) + 256 * ((m) & 3))
#define VRD(m) do { st.vlo[m] = ldstr((((m) & 1) ? vp1 : vp0) + 8192 * ((m) >> 3) + 256 * (((m) & 7) >> 1)); \
                    st.vhi[m] = ldstr((((m) & 1) ? vp1 : vp0) + 8192 * ((m) >> 3) + 4096 + 256 * (((m) & 7) >> 1)); } while (0)
  float ma = -3.0e38f, mb = -3.0e38f, fct0 = 1.f, fct1 = 1.f;
  bool resc = false;
  SBAR();
  static_for<32>([&](auto I) __attribute__((always_inline)) {
    constexpr int g = I.value;
    if constexpr (g == 0) { if (young) __builtin_amdgcn_s_setprio(1); }
    if constexpr (g == PRIO_TOGGLE) { __builtin_amdgcn_s_setprio(0); }
    if constexpr (g < 16) {
      constexpr int kb = g >> 2, u = g & 3;
      if constexpr (u == 0) C.s[0][kb] = MFMA16(st.kf[g], st.q[0][0], st.negm[0]); else C.s[0][kb] = MFMA16(st.kf[g], st.q[0][u], C.s[0][kb]);
      PIN(C.s[0][kb]);
    } else if constexpr (!FIRST) {
      constexpr int m = g - 16;
      st.o[0][m & 7] = MFMA16(cat(st.vlo[m], st.vhi[m]), __builtin_bit_cast(bf16x8, st.pw[0][m >> 3]), st.o[0][m & 7]);
      PIN(st.o[0][m & 7]);
    }
    SBAR();
    {
      constexpr int f = g + PD;
      if constexpr (f < 16) { KRD(f, kp); }
      else if constexpr (f < 32) { if constexpr (!FIRST) { VRD(f - 16); } }
      else { KRD(f - 32, kn); }
    }
    if constexpr ((g & 1) && g >= 5 && g <= 19) {
      constexpr int kb = (g - 5) >> 2, hf = ((g - 5) >> 1) & 1;
      ma = MX3(ma, C.s[0][kb][2 * hf], C.s[0][kb][2 * hf + 1]); PIN(ma);
    }
    SBAR();
    if constexpr (g < 16) {
      constexpr int kb = g >> 2, u = g & 3;
      if constexpr (u == 0) C.s[1][kb] = MFMA16(st.kf[g], st.q[1][0], st.negm[1]); else C.s[1][kb] = MFMA16(st.kf[g], st.q[1][u], C.s[1][kb]);
      PIN(C.s[1][kb]);
    } else if constexpr (!FIRST) {
      constexpr int m = g - 16;
      st.o[1][m & 7] = MFMA16(cat(st.vlo[m], st.vhi[m]), __builtin_bit_cast(bf16x8, st.pw[1][m >> 3]), st.o[1][m & 7]);
      PIN(st.o[1][m & 7]);
    }
    SBAR();
    if constexpr ((g & 1) == 0) {
      if constexpr (g <= 22) {
        if constexpr (!FIRST) { constexpr int n = 8 + g; PWV(n)[(n >> 1) & 3] = cvtpk(EL(P, n), EL(P, n + 1)); PIN(PWV(n)); }
      } else { constexpr int n = g - 24; PWV(n)[(n >> 1) & 3] = cvtpk(EL(C, n), EL(C, n + 1)); PIN(PWV(n)); }
    }
    if constexpr ((g & 1) && g >= 5 && g <= 19) {
      constexpr int kb = (g - 5) >> 2, hf = ((g - 5) >> 1) & 1;
      mb = MX3(mb, C.s[1][kb][2 * hf], C.s[1][kb][2 * hf + 1]); PIN(mb);
    }
    if constexpr (g == 22) {
      const bool ok = (ma <= THR) & (mb <= THR);
      if (FIRST || __builtin_expect(!__all(ok), 0)) {
#pragma unroll
        for (int qb = 0; qb < 2; ++qb) {
          float rm = qb ? mb : ma;
          rm = xmax4(rm);
          const float dl = FIRST ? rm : __builtin_fmaxf(rm, 0.f);
          st.mhat[qb] += dl;
#pragma unroll
          for (int kb = 0; kb < 4; ++kb)
#pragma unroll
            for (int r = 0; r < 4; ++r) C.s[qb][kb][r] -= dl;
#pragma unroll
          for (int r = 0; r < 4; ++r) st.negm[qb][r] = -st.mhat[qb];
          PIN(st.negm[qb]);
          if constexpr (!FIRST) { const float f_ = EX(-dl); if (qb == 0) fct0 = f_; else fct1 = f_; }
        }
        resc = true;
      }
    }
    if constexpr (g <= 21) {
      if constexpr (!FIRST) { constexpr int n = 10 + g; EL(P, n) = EX(EL(P, n)); PIN(ELV(P, n)); }
    } else { constexpr int n = g - 22; EL(C, n) = EX(EL(C, n)); PIN(ELV(C, n)); }
    SBAR();
    if constexpr (!FIRST && (g == 21 || g == 23 || g == 25 || g == 27)) {
      constexpr int qb = (g >> 1) & 1, w = g >= 24;
      st.lacc[qb] = MFMA16(st.ones, __builtin_bit_cast(bf16x8, st.pw[qb][w]), st.lacc[qb]); PIN(st.lacc[qb]);
      SBAR();
    }
  });
  if constexpr (!FIRST) {
    if (__builtin_expect(resc, 0)) {
#pragma unroll
      for (int d = 0; d < 8; ++d)
#pragma unroll
        for (int r = 0; r < 4; ++r) { st.o[0][d][r] *= fct0; st.o[1][d][r] *= fct1; }
#pragma unroll
      for (int r = 0; r < 4; ++r) { st.lacc[0][r] *= fct0; st.lacc[1][r] *= fct1; }
    }
  }
#undef KRD
#undef VRD
}

__global__ __launch_bounds__(512, 2)
void mha_k1(const char* __restrict__ x, const char* __restrict__ kc, const char* __restrict__ vc,
            const char* __restrict__ wq, const char* __restrict__ wo, char* ows, char* __restrict__ out) {
  extern __shared__ __attribute__((aligned(16))) char lds[];
  const int tid = threadIdx.x, lane = tid & 63;
  const int wid = __builtin_amdgcn_readfirstlane(tid >> 6);
  const int b = 4 * (blockIdx.x & 1) + ((blockIdx.x >> 3) & 3), h = 8 * ((blockIdx.x >> 1) & 3) + (blockIdx.x >> 5);
  const unsigned lds0 = (unsigned)(uintptr_t)lds;
  const lds_ptr L = (lds_ptr)lds;

#define KEEP(d) asm volatile("" :: "v"(d) : "memory")
  const __amdgpu_buffer_rsrc_t srdW = __builtin_amdgcn_make_buffer_rsrc((void*)(wq + h * 256), (short)0, DM * 8192 - h * 256, 0x00020000);
  const unsigned pl = ((lane >> 2) & 3), dbl = lane >> 4, c4 = lane & 3;
  const unsigned wv0 = pl * 8192 + dbl * 64 + c4 * 16, wv1 = wv0 + 31744;
#define DMAW2(c) DMA2(POLW, lds0 + WQO(c) + 2048 * wid, wv0, wv1, srdW, (unsigned)(((c) * 64 + 8 * wid) * 8192))
  const int ob = (blockIdx.x & 6) + ((blockIdx.x >> 5) & 1), oqt = blockIdx.x >> 6, ont = 4 * (blockIdx.x & 1) + ((blockIdx.x >> 3) & 3);
  const __amdgpu_buffer_rsrc_t srdWo = __builtin_amdgcn_make_buffer_rsrc((void*)(wo + ont * 128), (short)0, NH * DH * DM * 2 - ont * 128, 0x00020000);
  const unsigned ow0 = (4 * (lane >> 5) + ((lane >> 2) & 3)) * 1024 + ((lane >> 4) & 1) * 64 + (lane & 3) * 16;
  const unsigned ow1 = ow0 + 7168, ow2 = ow0 + 2 * 7168, ow3 = ow0 + 3 * 7168;
  unsigned long long* const epochp = (unsigned long long*)(ows + WS_EPOCH);
  unsigned long long* const flags = (unsigned long long*)(ows + WS_FLAGS);
  unsigned long long tag = 0;
  unsigned td = 0;
#define RR(i) (i)
  constexpr int NA = P3NA;
  const int r32 = lane & 31, hi = lane >> 5, tq = (lane >> 2) & 3, tp = lane & 3, tblk = (lane >> 4) & 1;
  bf16x8 wf[NA][4];
#define RBP(i) ((lds_cptr)L + RB(i) + rbo)
#define WDMA(i) DMA4("", lds0 + (unsigned)(RB(i)) + 4096u * w3, ow0, ow1, ow2, ow3, srdWo, 32768u * w3 + (unsigned)(RR(i) * 256 * 1024))
#define W3() unsigned w3 = (unsigned)__builtin_amdgcn_readfirstlane(tid >> 6); asm volatile("s_nop 4" : "+s"(w3))
#define WREAD_NW(i) do { lds_cptr p_ = RBP(i); PIN(p_); _Pragma("unroll") for (int f_ = 0; f_ < 4; ++f_) \
      wf[(i) % NA][f_] = cat(ldstr(p_ + 1024 * (f_ >> 1) + 256 * (f_ & 1)), ldstr(p_ + 1024 * (f_ >> 1) + 2048 + 256 * (f_ & 1))); } while (0)
#define WRWAIT(i) asm volatile("s_waitcnt lgkmcnt(0)" : "+v"(wf[(i) % NA][0]), "+v"(wf[(i) % NA][1]), "+v"(wf[(i) % NA][2]), "+v"(wf[(i) % NA][3]) :: "memory")
#define WREAD(i) do { WREAD_NW(i); WRWAIT(i); } while (0)
#define RBO() (wid * 4096 + 512 * hi + 64 * tq + 32 * tblk + 8 * tp)
  if (wid == 7) {
    const __amdgpu_buffer_rsrc_t srdK = __builtin_amdgcn_make_buffer_rsrc((void*)(kc + (size_t)(b * NH + h) * (SK * DH * 2)), (short)0, SK * DH * 2, 0x00020000);
    const __amdgpu_buffer_rsrc_t srdV = __builtin_amdgcn_make_buffer_rsrc((void*)(vc + (size_t)(b * NH + h) * (SK * DH * 2)), (short)0, SK * DH * 2, 0x00020000);
    const unsigned wv2 = wv0 + 2 * 31744, wv3 = wv0 + 3 * 31744;
    const unsigned kvA = pl * 256 + dbl * 64 + c4 * 16, kvB = pl * 256 + dbl * 64 + ((c4 ^ 2) * 16);
#define DMAW(c) do { _Pragma("unroll") for (int i_ = 0; i_ < 4; ++i_) DMA4(POLW, lds0 + (unsigned)(WQO(c) + 4096 * i_), wv0, wv1, wv2, wv3, srdW, (unsigned)(((c) * 64 + 16 * i_) * 8192)); } while (0)
#define DMAK_(t, so) do { _Pragma("unroll") for (int i_ = 0; i_ < 4; ++i_) { WAITV(59); DMA4("nt", lds0 + (unsigned)((so) + 4096 * i_), kvA, kvA, kvB, kvB, srdK, (unsigned)((t) * 16384 + 4096 * i_)); } } while (0)
#define DMAV_(t, so) do { _Pragma("unroll") for (int i_ = 0; i_ < 4; ++i_) { WAITV(59); DMA4("nt", lds0 + (unsigned)((so) + 16384 + 4096 * i_), kvA, kvB, kvA, kvB, srdV, (unsigned)((t) * 16384 + 4096 * i_)); } } while (0)
#define DMAK(t) DMAK_(t, SLOT(t))
#define DMAV(t) DMAV_(t, SLOT((t) + 1))
    const char* tw = wo + (size_t)(blockIdx.x >> 3) * 8192 + (lane >> 5) * 262144 + ((lane >> 2) & 7) * 1024 + (blockIdx.x & 1) * 512 + (lane & 3) * 128;
#define TW(r) do { const char* p_ = tw + (size_t)(r) * 524288; asm volatile("s_waitcnt vmcnt(62)\n\tglobal_load_dword %0, %1, off" : "+v"(td) : "v"(p_) : "memory"); } while (0)
#define RW(i) do { _Pragma("unroll") for (int w_ = 0; w_ < 8; ++w_) { WAITV(59); DMA4("", lds0 + (unsigned)(RB(i) + 4096 * w_), ow0, ow1, ow2, ow3, srdWo, (unsigned)((RR(i) * 256 + 32 * w_) * 1024)); } } while (0)
    DMAW2(0);
    BARRIER();
    DMAW(1);
    WAITV(16); BARRIER();
    DMAW(2);
    WAITV(16); BARRIER();
    DMAW(3);
    WAITV(16); BARRIER();
    DMAW(4);
    WAITV(16); BARRIER();
    DMAW(5);
    WAITV(16); BARRIER();
    DMAW(6);
    WAITV(16); BARRIER();
    DMAW(7);
    WAITV(16); BARRIER();
    DMAK(0); DMAK(1); DMAV(0);
    WAITV(48); BARRIER();
    BARRIER();
    DMAK(2);
    WAITV(32); BARRIER();
    DMAV(1); DMAK(3); DMAV(2);
    WAITV(48); BARRIER();
    DMAK(4); DMAV(3); DMAK(5); DMAV(4);
    WAITV(63); BARRIER();
    asm volatile("s_waitcnt vmcnt(62)\n\tglobal_load_dwordx2 %0, %1, off sc1" : "=v"(tag) : "v"(epochp) : "memory");
    {
      int so = SLOT(1);
      for (int t = 1; t < 11; ++t) {
        DMAK_(t + 5, so); DMAV_(t + 4, so);
        so = (so == 131072) ? 0 : so + 32768;
        WAITV(63);
        BARRIER();
      }
    }
    DMAV(15); TW(0); TW(1);
    asm volatile("" : "+v"(tag));
    tag = (((unsigned long long)(unsigned)__builtin_amdgcn_readfirstlane((unsigned)(tag >> 32)) << 32) | (unsigned)__builtin_amdgcn_readfirstlane((unsigned)tag)) + 1;
    if (lane == 0) { *(__attribute__((address_space(3))) unsigned*)(L + PUBW) = 0u; *(__attribute__((address_space(3))) unsigned long long*)(L + PUBW + 8) = tag; }
    asm volatile("s_waitcnt lgkmcnt(0)" ::: "memory");
    WAITV(63);
    BARRIER();
    TW(2); TW(3);
    WAITV(36);
    RW(0);
    BARRIER();
    RW(1); TW(4); TW(5);
    WAITV(63);
    BARRIER();
    RW(2); TW(6); TW(7);
    WAITV(2);
    BARRIER();
    {
      const int rbo = RBO();
      WREAD(0); WREAD(1); WREAD(2);
    }
    BARRIER();
    RW(3); RW(5); RW(6); RW(7);
    WAITV(0);
#undef TW
#undef RW
#undef DMAW
#undef DMAK
#undef DMAV
  } else {
  const __amdgpu_buffer_rsrc_t srdX = __builtin_amdgcn_make_buffer_rsrc((void*)(x + (size_t)(b * SQ + wid * 32) * (DM * 2)), (short)0, 32 * DM * 2, 0x00020000);
  const unsigned xs = (lane >> 3) * 1024 + (((lane & 7) ^ (lane >> 4)) * 16);
  const unsigned xv0 = xs, xv1 = (xs ^ 64) + 7168, xv2 = xs + 2 * 7168, xv3 = (xs ^ 64) + 3 * 7168;
#define XDMA(c) DMA4(POLW, lds0 + (unsigned)XO(c) + 4096u * wid, xv0, xv1, xv2, xv3, srdX, (unsigned)(128 * (c)))
  const unsigned xrb = (unsigned)(wid * 4096 + r32 * 128 + ((hi ^ ((r32 >> 1) & 7)) * 16));
  bf16x8 xf[2][4];
#define XRD(c, k_) xf[(c) & 1][k_] = ldsrd128((lds_cptr)L + ((xrb ^ (unsigned)((k_) << 5)) + XO(c)))
  DMAW2(0); XDMA(0);
  BARRIER();
  XDMA(1);
  const lds_cptr wrb = (lds_cptr)L + 2048 * hi + 64 * tq + 32 * (tp & 1) + 16 * tblk + 8 * (tp >> 1);
  lds_cptr wrb2 = wrb + 131072; PIN(wrb2);
#define WRB(c) ((WQO(c) < 65536 ? wrb : wrb2) + (WQO(c) & 65535))
#define WRD(c, m) do { wlo[m] = ldstr(WRB(c) + 4096 * ((m) >> 2) + 256 * ((m) & 3)); whi[m] = ldstr(WRB(c) + 4096 * ((m) >> 2) + 1024 + 256 * ((m) & 3)); } while (0)
  AttnState st;
  f32x16 acc[4];
#pragma unroll
  for (int i = 0; i < 4; ++i) acc[i] = f32x16{};
  s16x4 wlo[16], whi[16];
  WAITV(4);
  XRD(0, 0); XRD(0, 1); XRD(0, 2); XRD(0, 3);
  BARRIER();
#pragma unroll
  for (int m = 0; m < PD; ++m) WRD(0, m);
  u32x4 qx[4], qy[4];
#define QCV1(nb, e_) do { \
      const unsigned lo_ = cvtpk(acc[nb][2 * (e_)] * QSCALE, acc[nb][2 * (e_) + 1] * QSCALE), hi_ = cvtpk(acc[nb][8 + 2 * (e_)] * QSCALE, acc[nb][9 + 2 * (e_)] * QSCALE); \
      auto r_ = __builtin_amdgcn_permlane16_swap(lo_, hi_, false, false); qx[nb][e_] = r_[0]; qy[nb][e_] = r_[1]; PIN(qx[nb]); PIN(qy[nb]); } while (0)
  static_for<128>([&](auto I) __attribute__((always_inline)) {
    constexpr int c = I.value >> 4, mm = I.value & 15;
    constexpr int m = (c == 7) ? (4 * (mm & 3) + (mm >> 2)) : mm;
    acc[m & 3] = MFMA(cat(wlo[m], whi[m]), xf[c & 1][m >> 2], acc[m & 3]); PIN(acc[m & 3]);
    if constexpr (c == 0 && mm + PD == 16) { BARRIER(); }
    if constexpr (mm + PD < 16) { constexpr int f = mm + PD, ff = (c == 7) ? (4 * (f & 3) + (f >> 2)) : f; WRD(c, ff); }
    else if constexpr (c < 7) { constexpr int f = mm + PD - 16, ff = (c + 1 == 7) ? (4 * (f & 3) + (f >> 2)) : f; WRD(c + 1, ff); }
    if constexpr (c == 0 && mm == 0) { XDMA(2); }
    if constexpr (c == 0 && mm == 2) { XDMA(3); }
    if constexpr (c >= 1 && c <= 4 && mm == 0) { XDMA(c + 3); }
    if constexpr (c < 7) {
      if constexpr (mm == 4) { WAITV(c <= 4 ? 8 : 4 * (6 - c)); }
      if constexpr (mm >= 4 && mm < 8) { XRD(c + 1, mm - 4); }
    }
    if constexpr (c == 5) { st.o[mm >> 3][mm & 7] = f32x4{}; PIN(st.o[mm >> 3][mm & 7]); }
    if constexpr (c == 7 && mm >= 5) { QCV1((mm - 5) >> 2, (mm - 5) & 3); }
    SBAR();
    if constexpr (mm == 15) { BARRIER(); SBAR(); }
  });
#undef XDMA
#undef XRD
#undef WRD
  const int l16 = lane & 15, g4 = lane >> 4;
  const lds_cptr kb = (lds_cptr)L + 1024 * (l16 >> 2) + 64 * (l16 & 3) + 16 * (g4 ^ ((l16 >> 2) & 2));
  const lds_cptr vb0 = (lds_cptr)L + 16384 + 1024 * g4 + 64 * tq + 32 * (g4 & 1) + 8 * tp;
  const lds_cptr vb1 = (lds_cptr)L + 16384 + 1024 * g4 + 64 * tq + 32 * (1 - (g4 & 1)) + 8 * tp;
#pragma unroll
  for (int m = 0; m < PD; ++m) st.kf[m] = ldsrd128(kb + SLOT(0) + 4096 * (m >> 2) + 256 * (m & 3));
  QCV1(2, 3); QCV1(3, 0); QCV1(3, 1); QCV1(3, 2); QCV1(3, 3);
#undef QCV1
#pragma unroll
  for (int nb = 0; nb < 4; ++nb) { st.q[0][nb] = __builtin_bit_cast(bf16x8, qx[nb]); st.q[1][nb] = __builtin_bit_cast(bf16x8, qy[nb]); }
  st.negm[0] = f32x4{}; st.negm[1] = f32x4{}; PIN(st.negm[0]); PIN(st.negm[1]);
  st.mhat[0] = st.mhat[1] = 0.f; st.lacc[0] = f32x4{}; st.lacc[1] = f32x4{};
  st.ones = bf16x8{0x3F80, 0x3F80, 0x3F80, 0x3F80, 0x3F80, 0x3F80, 0x3F80, 0x3F80}; PIN(st.ones);

  STile A, B;
  attn_step<true>(st, A, B, kb + SLOT(0), vb0 + SLOT(0), vb1 + SLOT(0), kb + SLOT(1), wid >= 4);
  BARRIER();
  int s0 = SLOT(1);
  for (int t = 1; t < 15; t += 2) {
    const int s1 = (s0 == 131072) ? 0 : s0 + 32768, s2 = (s1 == 131072) ? 0 : s1 + 32768;
    attn_step<false>(st, B, A, kb + s0, vb0 + s0, vb1 + s0, kb + s1, wid >= 4);
    BARRIER();
    attn_step<false>(st, A, B, kb + s1, vb0 + s1, vb1 + s1, kb + s2, wid >= 4);
    BARRIER();
    s0 = s2;
  }
  attn_step<false>(st, B, A, kb + SLOT(15), vb0 + SLOT(15), vb1 + SLOT(15), kb + SLOT(15), wid >= 4);
  BARRIER();
  {
    lds_cptr vd0 = vb0 + SLOT(16), vd1 = vb1 + SLOT(16); PIN(vd0); PIN(vd1);
#define VRD(m) do { st.vlo[m] = ldstr((((m) & 2) ? vd1 : vd0) + 8192 * ((m) & 1) + 256 * ((m) >> 2)); st.vhi[m] = ldstr((((m) & 2) ? vd1 : vd0) + 8192 * ((m) & 1) + 4096 + 256 * ((m) >> 2)); } while (0)
    const int rbo = RBO();
    WREAD(0); WREAD(1); WREAD(2);
    BARRIER();
#pragma unroll
    for (int m = 0; m < 8; ++m) VRD(m);
    static_for<22>([&](auto I) __attribute__((always_inline)) { constexpr int n = 10 + I.value; EL(B, n) = EX(EL(B, n)); });
    static_for<12>([&](auto I) __attribute__((always_inline)) { constexpr int n = 8 + 2 * I.value; PWV(n)[(n >> 1) & 3] = cvtpk(EL(B, n), EL(B, n + 1)); });
#pragma unroll
    for (int w = 0; w < 2; ++w) {
      st.lacc[0] = MFMA16(st.ones, __builtin_bit_cast(bf16x8, st.pw[0][w]), st.lacc[0]);
      st.lacc[1] = MFMA16(st.ones, __builtin_bit_cast(bf16x8, st.pw[1][w]), st.lacc[1]);
    }
    const float inv0 = 1.0f / st.lacc[0][0], inv1 = 1.0f / st.lacc[1][0];
    const __amdgpu_buffer_rsrc_t srdOs = __builtin_amdgcn_make_buffer_rsrc((void*)(ows + (size_t)(b * NH + h) * 57344), (short)0, 57344, 0x00020000);
    const int qr0 = 32 * wid + l16, vk0 = qr0 / 28, qr1 = qr0 + 16, vk1 = qr1 / 28;
    const unsigned osv0 = vk0 * 7168 + (g4 >> 1) * 896 + ((g4 & 1) * 28 + qr0 - 28 * vk0) * 16, osv1 = vk1 * 7168 + (g4 >> 1) * 896 + ((g4 & 1) * 28 + qr1 - 28 * vk1) * 16;
#define OSTORE(u) do { \
      u32x4 w0_ = {cvtpk(st.o[0][2 * (u)][0] * inv0, st.o[0][2 * (u)][1] * inv0), cvtpk(st.o[0][2 * (u)][2] * inv0, st.o[0][2 * (u)][3] * inv0), \
                   cvtpk(st.o[0][2 * (u) + 1][0] * inv0, st.o[0][2 * (u) + 1][1] * inv0), cvtpk(st.o[0][2 * (u) + 1][2] * inv0, st.o[0][2 * (u) + 1][3] * inv0)}; \
      __builtin_amdgcn_raw_buffer_store_b128(w0_, srdOs, osv0 + (u) * 1792, 0, 16); \
      u32x4 w1_ = {cvtpk(st.o[1][2 * (u)][0] * inv1, st.o[1][2 * (u)][1] * inv1), cvtpk(st.o[1][2 * (u)][2] * inv1, st.o[1][2 * (u)][3] * inv1), \
                   cvtpk(st.o[1][2 * (u) + 1][0] * inv1, st.o[1][2 * (u) + 1][1] * inv1), cvtpk(st.o[1][2 * (u) + 1][2] * inv1, st.o[1][2 * (u) + 1][3] * inv1)}; \
      __builtin_amdgcn_raw_buffer_store_b128(w1_, srdOs, osv1 + (u) * 1792, 0, 16); } while (0)
    SBAR();
#pragma unroll
    for (int u = 0; u < 4; ++u) {
#pragma unroll
      for (int k = 0; k < 4; ++k) {
        const int m = 4 * u + k, db = m >> 1, w = m & 1;
        st.o[0][db] = MFMA16(cat(st.vlo[m], st.vhi[m]), __builtin_bit_cast(bf16x8, st.pw[0][w]), st.o[0][db]);
        st.o[1][db] = MFMA16(cat(st.vlo[m], st.vhi[m]), __builtin_bit_cast(bf16x8, st.pw[1][w]), st.o[1][db]);
        if (u < 2) { VRD(m + 8); }
      }
      SBAR();
      if (u >= 1) { OSTORE(u - 1); }
      SBAR();
    }
    OSTORE(3);
#undef VRD
#undef OSTORE
    WAITV(0);
    if (lane == 0) {
      const unsigned old = __hip_atomic_fetch_add((__attribute__((address_space(3))) unsigned*)(L + PUBW), 1u, __ATOMIC_RELAXED, __HIP_MEMORY_SCOPE_WORKGROUP);
      if (old == 6) __hip_atomic_store(flags + b * NH + h, *(__attribute__((address_space(3))) unsigned long long*)(L + PUBW + 8), __ATOMIC_RELAXED, __HIP_MEMORY_SCOPE_AGENT);
    }
    asm volatile("s_waitcnt lgkmcnt(0)" ::: "memory");
  }
  }

  {
    static_assert(NA == 8, "prefetch sequences above are written for NA = 8");
    const int rbo = RBO(); W3();
    const __amdgpu_buffer_rsrc_t srdO = __builtin_amdgcn_make_buffer_rsrc((void*)(ows + (size_t)ob * (NH * 57344)), (short)0, NH * 57344, 0x00020000);
    const unsigned ov0 = (hi * 28 + (r32 < 28 ? r32 : r32 - 4)) * 16;
    const unsigned osb = (w3 >> 2) * 57344 + (w3 & 3) * 1792 + oqt * 2 * 7168;
    u32x4 of[3][4];
    f32x16 acc[2][2];
#pragma unroll
    for (int i = 0; i < 2; ++i) { acc[i][0] = f32x16{}; acc[i][1] = f32x16{}; }
#define OLOAD(i) do { const unsigned so_ = osb + (unsigned)(RR(i) * 2 * 57344), so1_ = so_ + 7168u; \
      BLOAD("sc1", of[(i) % 3][0], ov0, srdO, so_, 0); BLOAD("sc1", of[(i) % 3][1], ov0, srdO, so1_, 0); \
      BLOAD("sc1", of[(i) % 3][2], ov0, srdO, so_, 896); BLOAD("sc1", of[(i) % 3][3], ov0, srdO, so1_, 896); } while (0)
    BARRIER();
    WDMA(4);
    WREAD(3); WDMA(8); WREAD(5); WDMA(10); WREAD(6); WDMA(11); WREAD(7); WDMA(12);
    WAITV(16); WREAD(4); WDMA(9);
    if (w3 == 7) {
      if (blockIdx.x != 0) {
        const unsigned long long* fp = flags + ob * NH + r32;
        for (unsigned spins = 0; spins < (1u << 18); ++spins) {
          const unsigned long long v = __hip_atomic_load(fp, __ATOMIC_RELAXED, __HIP_MEMORY_SCOPE_AGENT);
          if (__all(v == tag)) break;
          __builtin_amdgcn_s_sleep(1);
        }
      } else {
        for (unsigned spins = 0; spins < (1u << 18); ++spins) {
          bool ok = true;
#pragma unroll
          for (int k = 0; k < 4; ++k) ok &= __hip_atomic_load(flags + 64 * k + lane, __ATOMIC_RELAXED, __HIP_MEMORY_SCOPE_AGENT) == tag;
          if (__all(ok)) break;
          __builtin_amdgcn_s_sleep(1);
        }
        if (lane == 0) __hip_atomic_store(epochp, tag, __ATOMIC_RELAXED, __HIP_MEMORY_SCOPE_AGENT);
      }
      KEEP(td);
    }
    BARRIER();
    OLOAD(0); OLOAD(1);
#pragma unroll
    for (int i = 0; i < 16; ++i) {
      const int cnt = (i < 15 ? 4 : 0) + ((i - 1 >= 1 && i - 1 <= 3) ? 4 : 0);
      WAITV4(cnt, of[i % 3][0], of[i % 3][1], of[i % 3][2], of[i % 3][3]);
      if (i >= 1 && i <= 3) { WRWAIT(i - 1 + NA); WDMA(i + 12); }
      if (i + 2 < 16) { OLOAD(i + 2); }
      SBAR();
#pragma unroll
      for (int ks = 0; ks < 2; ++ks)
#pragma unroll
        for (int nb = 0; nb < 2; ++nb)
#pragma unroll
          for (int vb = 0; vb < 2; ++vb)
            acc[nb][vb] = MFMA(wf[i % NA][2 * ks + nb], __builtin_bit_cast(bf16x8, of[i % 3][2 * ks + vb]), acc[nb][vb]);
      SBAR();
      if (i + NA < 16) { WREAD_NW(i + NA); }
      SBAR();
    }
#undef WDMA
#undef WREAD
#undef OLOAD
#undef RBP
#pragma unroll
    for (int vb = 0; vb < 2; ++vb)
#pragma unroll
      for (int nb = 0; nb < 2; ++nb)
#pragma unroll
        for (int rg = 0; rg < 4; ++rg) {
          const int q = 32 * vb + r32, slot = 8 * nb + 2 * rg + hi;
          f32x4 v = {acc[nb][vb][4 * rg], acc[nb][vb][4 * rg + 1], acc[nb][vb][4 * rg + 2], acc[nb][vb][4 * rg + 3]};
          *(__attribute__((address_space(3))) f32x4*)(L + (q >> 4) * 32768 + wid * 4096 + (q & 15) * 256 + ((slot ^ (q & 15)) * 16)) = v;
        }
    asm volatile("s_waitcnt lgkmcnt(0)" ::: "memory");
    BARRIER();
    const int q = tid >> 3, pc = tid & 7;
    f32x4 s0 = {0, 0, 0, 0}, s1 = {0, 0, 0, 0};
#pragma unroll
    for (int w = 0; w < 8; ++w) {
      s0 += *(const __attribute__((address_space(3))) f32x4*)(L + (q >> 4) * 32768 + w * 4096 + (q & 15) * 256 + (((2 * pc) ^ (q & 15)) * 16));
      s1 += *(const __attribute__((address_space(3))) f32x4*)(L + (q >> 4) * 32768 + w * 4096 + (q & 15) * 256 + (((2 * pc + 1) ^ (q & 15)) * 16));
    }
    u32x4 w = {cvtpk(s0[0], s0[1]), cvtpk(s0[2], s0[3]), cvtpk(s1[0], s1[1]), cvtpk(s1[2], s1[3])};
    if ((q & 31) < 28)
      __builtin_amdgcn_raw_buffer_store_b128(w, __builtin_amdgcn_make_buffer_rsrc((void*)(out + (size_t)(ob * SQ + 56 * oqt) * (DM * 2)), (short)0, 56 * DM * 2, 0x00020000),
                                             (28 * (q >> 5) + (q & 31)) * (DM * 2) + ont * 128 + pc * 16, 0, 16);
  }
}

extern "C" void kernel_launch(void* const* d_in, const int* in_sizes, int n_in,
                              void* d_out, int out_size, void* d_ws, size_t ws_size,
                              hipStream_t stream) {
  (void)in_sizes; (void)n_in; (void)out_size; (void)ws_size;
  (void)hipFuncSetAttribute((const void*)mha_k1, hipFuncAttributeMaxDynamicSharedMemorySize, K1_LDS);
  mha_k1<<<256, 512, K1_LDS, stream>>>((const char*)d_in[0], (const char*)d_in[1], (const char*)d_in[2], (const char*)d_in[3], (const char*)d_in[4], (char*)d_ws, (char*)d_out);
}
```
